```python
import torch

def _softmax(x, axis=-1):
    return torch.softmax(x, dim=axis)

def _gelu(x):
    return torch.nn.functional.gelu(x, approximate='tanh')

def _silu(x):
    return torch.nn.functional.silu(x)

def _sigmoid(x):
    return torch.sigmoid(x)

def _layernorm(x, axis=-1, eps=1e-05):
    m = x.mean(dim=axis, keepdim=True)
    v = x.var(dim=axis, keepdim=True, unbiased=False)
    return (x - m) / torch.sqrt(v + eps)

def _scatter_add(tgt, idx, src, axis=0):
    return tgt.clone().index_add_(axis, idx, src)

def _segment_sum(src, seg, n):
    out = torch.zeros((n,) + src.shape[1:], dtype=src.dtype)
    return out.index_add_(0, seg, src)

def _pad(x, pw):
    flat = tuple((v for pair in reversed(pw) for v in pair))
    return torch.nn.functional.pad(x, flat)
kernel_name = 'adaLN_cond__ssm_conv_h3__glu_gated_reglu__prompt'

def setup_inputs():

    _w = lambda a: (lambda t: t.to(torch.bfloat16) if t.is_floating_point() else t)(torch.as_tensor(a))

    def _A_setup():
        B, S, D = (1, 2048, 640)
        ks = 4
        return {'q': _w((lambda *s: torch.randn(*s, dtype=torch.bfloat16) * 0.05)(B, S, D)), 'k': _w((lambda *s: torch.randn(*s, dtype=torch.bfloat16) * 1.6)(B, S, D)), 'v': _w((lambda *s: torch.randn(*s, dtype=torch.bfloat16) * 1.6)(B, S, D)), 'shift_k': _w((lambda *s: torch.randn(*s, dtype=torch.bfloat16) * 1.6)(D, ks) * 0.5), 'ssm_k': _w((lambda *s: torch.randn(*s, dtype=torch.bfloat16) * 1.6)(D, S) * (1.0 / S ** 0.5)), 'ssm_D': _w((lambda *s: torch.randn(*s, dtype=torch.bfloat16) * 1.6)(D))}
    _A_in = _A_setup()

    def _B_setup():
        B, S, D, F = (1, 2048, 640, 1728)
        return {'x': _w((lambda *s: torch.randn(*s, dtype=torch.bfloat16) * 0.05)(B, S, D)), 'W_gate': _w((lambda *s: torch.randn(*s, dtype=torch.bfloat16) * 3.2)(D, F) * (2.0 / D) ** 0.5), 'W_up': _w((lambda *s: torch.randn(*s, dtype=torch.bfloat16) * 3.2)(D, F) * (2.0 / D) ** 0.5), 'W_down': _w((lambda *s: torch.randn(*s, dtype=torch.bfloat16) * 3.2)(F, D) * (1.0 / F) ** 0.5)}
    _B_in = _B_setup()
    return {'A_q': _A_in['q'], 'A_k': _A_in['k'], 'A_v': _A_in['v'], 'A_shift_k': _A_in['shift_k'], 'A_ssm_k': _A_in['ssm_k'], 'A_ssm_D': _A_in['ssm_D'], 'B_x': _B_in['x'], 'B_W_gate': _B_in['W_gate'], 'B_W_up': _B_in['W_up'], 'B_W_down': _B_in['W_down'], 'c': _w((lambda *s: torch.randn(*s, dtype=torch.bfloat16) * 0.1)(1, 640) * 8.0), 'w_mod': _w((lambda *s: torch.randn(*s, dtype=torch.bfloat16) * 0.1)(640, 6 * 640) * 640 ** (-0.5) * 8.0)}

def reference(A_q, A_k, A_v, A_shift_k, A_ssm_k, A_ssm_D, B_x, B_W_gate, B_W_up, B_W_down, c, w_mod):

    def _A_ref(q, k, v, shift_k, ssm_k, ssm_D):
        B, S, D = q.shape
        ks = shift_k.shape[-1]
        k_pad = _pad(k, ((0, 0), (ks - 1, 0), (0, 0)))
        k_shift = torch.zeros_like(k)
        for t in range(ks):
            k_shift = k_shift + k_pad[:, t:t + S, :] * shift_k[:, ks - 1 - t][None, None, :]
        kv = k_shift * v
        L = 2 * S
        kv_pad = _pad(kv, ((0, 0), (0, S), (0, 0)))
        kf_pad = _pad(ssm_k, ((0, 0), (0, S)))
        kv_f = (lambda x, axes=(-2, -1): torch.fft.rfft2(x.float(), dim=axes))(kv_pad[:, None, :, :], axes=(1, 2))
        kf_f = (lambda x, axes=(-2, -1): torch.fft.rfft2(x.float(), dim=axes))(kf_pad[:, None, :], axes=(1, 2))
        kf_f = (lambda x, *d: x.permute(*d))(kf_f, 1, 2, 0)
        y_f = kv_f * kf_f[None, :, :, :]
        y = (lambda x, s, axes=(-2, -1): torch.fft.irfft2(x, s=s, dim=axes).to(torch.bfloat16))(y_f, (1, L), axes=(1, 2))[:, 0, :S, :]
        y = y + kv * ssm_D[None, None, :]
        return q * y

    def _B_ref(x, W_gate, W_up, W_down):
        gate = torch.einsum('bsd,df->bsf', x, W_gate)
        up = torch.einsum('bsd,df->bsf', x, W_up)
        gate = gate * (lambda x, dt: x.to(dt))(gate > 0, gate.dtype)
        h = gate * up
        return torch.einsum('bsf,fd->bsd', h, W_down)
    x = A_q
    mod = (_silu(c) @ w_mod).reshape(c.shape[0], 6, 1, -1)
    sa, ca, ga, sb, cb, gb = (mod[:, 0].clone(), mod[:, 1].clone(), mod[:, 2].clone(), mod[:, 3].clone(), mod[:, 4].clone(), mod[:, 5].clone())
    h = _layernorm(x, axis=-1) * (1 + ca) + sa
    x = x + ga * _A_ref(h, A_k, A_v, A_shift_k, A_ssm_k, A_ssm_D)
    h = _layernorm(x, axis=-1) * (1 + cb) + sb
    x = x + gb * _B_ref(h, B_W_gate, B_W_up, B_W_down)
    return x
```

```cpp
#include <hip/hip_runtime.h>
#include <hip/hip_bf16.h>
#include <stdint.h>

typedef unsigned short u16;
constexpr int S = 2048, D = 640, F = 1728;

__device__ __forceinline__ float bf2f(u16 x) { return __uint_as_float(((unsigned)x) << 16); }
__device__ __forceinline__ u16 f2bf(float f) { __hip_bfloat16 b = __float2bfloat16(f); return *(u16*)&b; }

__global__ void k_mod(const u16* c, const u16* w, float* mod) {
    int j = blockIdx.x * blockDim.x + threadIdx.x;
    if (j >= 6 * D) return;
    float acc = 0.f;
    for (int i = 0; i < D; ++i) {
        float cv = bf2f(c[i]);
        float s = cv / (1.f + __expf(-cv));
        s = bf2f(f2bf(s));
        acc += s * bf2f(w[i * 6 * D + j]);
    }
    mod[j] = bf2f(f2bf(acc));
}

__global__ void k_kv(const u16* k, const u16* v, const u16* sh, u16* kv) {
    int idx = blockIdx.x * blockDim.x + threadIdx.x;
    if (idx >= S * D) return;
    int t = idx / D, d = idx % D;
    float acc = 0.f;
    for (int i = 0; i < 4; ++i) if (t - i >= 0) acc += bf2f(k[(t - i) * D + d]) * bf2f(sh[d * 4 + i]);
    kv[idx] = f2bf(acc * bf2f(v[idx]));
}

__global__ void k_conv(const u16* kv, const u16* sk, const u16* sD, u16* y) {
    int idx = blockIdx.x * blockDim.x + threadIdx.x;
    if (idx >= S * D) return;
    int t = idx / D, d = idx % D;
    float acc = 0.f;
    for (int s = 0; s <= t; ++s) acc += bf2f(kv[s * D + d]) * bf2f(sk[d * S + t - s]);
    acc += bf2f(kv[idx]) * bf2f(sD[d]);
    y[idx] = f2bf(acc);
}

__global__ void k_ln(const u16* q, const u16* y, const float* mod, u16* x1, u16* h2) {
    __shared__ float xs[D];
    __shared__ float red[4];
    int t = blockIdx.x;
    int tid = threadIdx.x;
    float s = 0.f;
    for (int d = tid; d < D; d += 64) { xs[d] = bf2f(q[t * D + d]); s += xs[d]; }
    for (int o = 32; o > 0; o >>= 1) s += __shfl_xor(s, o);
    float m = s / D;
    float vs = 0.f;
    for (int d = tid; d < D; d += 64) { float z = xs[d] - m; vs += z * z; }
    for (int o = 32; o > 0; o >>= 1) vs += __shfl_xor(vs, o);
    float rs = rsqrtf(vs / D + 1e-5f);
    s = 0.f;
    for (int d = tid; d < D; d += 64) {
        float h = (xs[d] - m) * rs * (1.f + mod[D + d]) + mod[d];
        float x = xs[d] + mod[2 * D + d] * (h * bf2f(y[t * D + d]));
        u16 xb = f2bf(x);
        x1[t * D + d] = xb;
        xs[d] = bf2f(xb);
        s += xs[d];
    }
    for (int o = 32; o > 0; o >>= 1) s += __shfl_xor(s, o);
    m = s / D;
    vs = 0.f;
    for (int d = tid; d < D; d += 64) { float z = xs[d] - m; vs += z * z; }
    for (int o = 32; o > 0; o >>= 1) vs += __shfl_xor(vs, o);
    rs = rsqrtf(vs / D + 1e-5f);
    for (int d = tid; d < D; d += 64) {
        float h = (xs[d] - m) * rs * (1.f + mod[4 * D + d]) + mod[3 * D + d];
        h2[t * D + d] = f2bf(h);
    }
}

__global__ void k_g1(const u16* h2, const u16* wg, const u16* wu, u16* H) {
    int idx = blockIdx.x * blockDim.x + threadIdx.x;
    if (idx >= S * F) return;
    int t = idx / F, f = idx % F;
    float g = 0.f, u = 0.f;
    for (int d = 0; d < D; ++d) { float a = bf2f(h2[t * D + d]); g += a * bf2f(wg[d * F + f]); u += a * bf2f(wu[d * F + f]); }
    g = g > 0.f ? g : 0.f;
    H[idx] = f2bf(g * u);
}

__global__ void k_g2(const u16* H, const u16* wd, const u16* x1, const float* mod, u16* out) {
    int idx = blockIdx.x * blockDim.x + threadIdx.x;
    if (idx >= S * D) return;
    int t = idx / D, d = idx % D;
    float a = 0.f;
    for (int f = 0; f < F; ++f) a += bf2f(H[t * F + f]) * bf2f(wd[f * D + d]);
    out[idx] = f2bf(bf2f(x1[idx]) + mod[5 * D + d] * a);
}

extern "C" void kernel_launch(void* const* d_in, const int* in_sizes, int n_in,
                              void* d_out, int out_size, void* d_ws, size_t ws_size,
                              hipStream_t stream) {
    const u16* q = (const u16*)d_in[0];
    const u16* k = (const u16*)d_in[1];
    const u16* v = (const u16*)d_in[2];
    const u16* sh = (const u16*)d_in[3];
    const u16* sk = (const u16*)d_in[4];
    const u16* sD = (const u16*)d_in[5];
    const u16* wg = (const u16*)d_in[7];
    const u16* wu = (const u16*)d_in[8];
    const u16* wd = (const u16*)d_in[9];
    const u16* c = (const u16*)d_in[10];
    const u16* wm = (const u16*)d_in[11];
    char* ws = (char*)d_ws;
    float* mod = (float*)ws;
    u16* kv = (u16*)(ws + (1 << 20));
    u16* y = (u16*)(ws + (4 << 20));
    u16* x1 = (u16*)(ws + (8 << 20));
    u16* h2 = (u16*)(ws + (12 << 20));
    u16* H = (u16*)(ws + (16 << 20));
    k_mod<<<(6 * D + 63) / 64, 64, 0, stream>>>(c, wm, mod);
    k_kv<<<(S * D + 255) / 256, 256, 0, stream>>>(k, v, sh, kv);
    k_conv<<<(S * D + 255) / 256, 256, 0, stream>>>(kv, sk, sD, y);
    k_ln<<<S, 64, 0, stream>>>(q, y, mod, x1, h2);
    k_g1<<<(S * F + 255) / 256, 256, 0, stream>>>(h2, wg, wu, H);
    k_g2<<<(S * D + 255) / 256, 256, 0, stream>>>(H, wd, x1, mod, (u16*)d_out);
}
```

```cpp
#include <hip/hip_runtime.h>
#include <hip/hip_bf16.h>
#include <stdint.h>

typedef unsigned short u16;
constexpr int S = 2048, D = 640, F = 1728;

__device__ __forceinline__ float bf2f(u16 x) { return __uint_as_float(((unsigned)x) << 16); }
__device__ __forceinline__ u16 f2bf(float f) { __hip_bfloat16 b = __float2bfloat16(f); return *(u16*)&b; }

__global__ void k_mod(const u16* c, const u16* w, float* mod) {
    int j = blockIdx.x * blockDim.x + threadIdx.x;
    if (j >= 6 * D) return;
    float acc = 0.f;
    for (int i = 0; i < D; ++i) {
        float cv = bf2f(c[i]);
        float s = cv / (1.f + __expf(-cv));
        s = bf2f(f2bf(s));
        acc += s * bf2f(w[i * 6 * D + j]);
    }
    mod[j] = bf2f(f2bf(acc));
}

__global__ void k_kv(const u16* k, const u16* v, const u16* sh, u16* kv) {
    int idx = blockIdx.x * blockDim.x + threadIdx.x;
    if (idx >= S * D) return;
    int t = idx / D, d = idx % D;
    float acc = 0.f;
    for (int i = 0; i < 4; ++i) if (t - i >= 0) acc += bf2f(k[(t - i) * D + d]) * bf2f(sh[d * 4 + i]);
    kv[idx] = f2bf(acc * bf2f(v[idx]));
}

__global__ void k_conv(const u16* kv, const u16* sk, const u16* sD, u16* y) {
    int idx = blockIdx.x * blockDim.x + threadIdx.x;
    if (idx >= S * D) return;
    int t = idx / D, d = idx % D;
    float acc = 0.f;
    for (int s = 0; s <= t; ++s) acc += bf2f(kv[s * D + d]) * bf2f(sk[d * S + t - s]);
    acc += bf2f(kv[idx]) * bf2f(sD[d]);
    y[idx] = f2bf(acc);
}

__global__ void k_ln(const u16* q, const u16* y, const float* mod, u16* x1, u16* h2) {
    __shared__ float xs[D];
    __shared__ float red[4];
    int t = blockIdx.x;
    int tid = threadIdx.x;
    float s = 0.f;
    for (int d = tid; d < D; d += 64) { xs[d] = bf2f(q[t * D + d]); s += xs[d]; }
    for (int o = 32; o > 0; o >>= 1) s += __shfl_xor(s, o);
    float m = s / D;
    float vs = 0.f;
    for (int d = tid; d < D; d += 64) { float z = xs[d] - m; vs += z * z; }
    for (int o = 32; o > 0; o >>= 1) vs += __shfl_xor(vs, o);
    float rs = rsqrtf(vs / D + 1e-5f);
    s = 0.f;
    for (int d = tid; d < D; d += 64) {
        float h = (xs[d] - m) * rs * (1.f + mod[D + d]) + mod[d];
        float x = xs[d] + mod[2 * D + d] * (h * bf2f(y[t * D + d]));
        u16 xb = f2bf(x);
        x1[t * D + d] = xb;
        xs[d] = bf2f(xb);
        s += xs[d];
    }
    for (int o = 32; o > 0; o >>= 1) s += __shfl_xor(s, o);
    m = s / D;
    vs = 0.f;
    for (int d = tid; d < D; d += 64) { float z = xs[d] - m; vs += z * z; }
    for (int o = 32; o > 0; o >>= 1) vs += __shfl_xor(vs, o);
    rs = rsqrtf(vs / D + 1e-5f);
    for (int d = tid; d < D; d += 64) {
        float h = (xs[d] - m) * rs * (1.f + mod[4 * D + d]) + mod[3 * D + d];
        h2[t * D + d] = f2bf(h);
    }
}

__global__ void k_g1(const u16* h2, const u16* wg, const u16* wu, u16* H) {
    int idx = blockIdx.x * blockDim.x + threadIdx.x;
    if (idx >= S * F) return;
    int t = idx / F, f = idx % F;
    float g = 0.f, u = 0.f;
    for (int d = 0; d < D; ++d) { float a = bf2f(h2[t * D + d]); g += a * bf2f(wg[d * F + f]); u += a * bf2f(wu[d * F + f]); }
    g = g > 0.f ? g : 0.f;
    H[idx] = f2bf(g * u);
}

__global__ void k_g2(const u16* H, const u16* wd, const u16* x1, const float* mod, u16* out) {
    int idx = blockIdx.x * blockDim.x + threadIdx.x;
    if (idx >= S * D) return;
    int t = idx / D, d = idx % D;
    float a = 0.f;
    for (int f = 0; f < F; ++f) a += bf2f(H[t * F + f]) * bf2f(wd[f * D + d]);
    out[idx] = f2bf(bf2f(x1[idx]) + mod[5 * D + d] * a);
}


typedef unsigned long long u64;
typedef unsigned v4u __attribute__((ext_vector_type(4)));
__device__ __forceinline__ u64 rt() { return __builtin_amdgcn_s_memrealtime(); }
struct Bufs { const v4u* p[8]; int n1k[8]; };
__global__ __launch_bounds__(512) void k_probe(Bufs bs, u64* ts, unsigned* sink, v4u* wr, int wr1k) {
    u64 t0 = rt();
    int b = blockIdx.x; int w = threadIdx.x >> 6, l = threadIdx.x & 63;
    unsigned s = 0;
    v4u a[24];
    int cnt = 0;
#pragma unroll
    for (int i = 0; i < 8; ++i) {
#pragma unroll
        for (int j = 0; j < 3; ++j) {
            int per = (bs.n1k[i] + 255) / 256;
            int k = w + 8 * j;
            int idx = b * per + k;
            bool ok = k < per && idx < bs.n1k[i];
            a[i * 3 + j] = ok ? bs.p[i][(size_t)idx * 64 + l] : (v4u){0, 0, 0, 0};
        }
    }
#pragma unroll
    for (int i = 0; i < 24; ++i) s += a[i].x ^ a[i].y ^ a[i].z ^ a[i].w;
    if (wr1k) { int per = wr1k / 256; for (int k = w; k < per; k += 8) wr[(size_t)(b * per + k) * 64 + l] = (v4u){s, 1, 2, 3}; }
    sink[b * 512 + threadIdx.x] = s;
    asm volatile("s_waitcnt vmcnt(0)" ::: "memory");
    __syncthreads();
    u64 t1 = rt();
    if (threadIdx.x == 0) { ts[2 * b] = t0; ts[2 * b + 1] = t1; }
}
__global__ void k_spin(const u64* ts) {
    u64 mn = ~0ull, mx = 0;
    for (int b = 0; b < 256; ++b) { mn = ts[2 * b] < mn ? ts[2 * b] : mn; mx = ts[2 * b + 1] > mx ? ts[2 * b + 1] : mx; }
    u64 span = mx - mn; if (span > 3000) span = 3000;
    u64 t0 = rt();
    while (rt() - t0 < span * 1000) __builtin_amdgcn_s_sleep(8);
}
extern "C" void kernel_launch(void* const* d_in, const int* in_sizes, int n_in,
                              void* d_out, int out_size, void* d_ws, size_t ws_size,
                              hipStream_t stream) {
    const u16* q = (const u16*)d_in[0];
    const u16* k = (const u16*)d_in[1];
    const u16* v = (const u16*)d_in[2];
    const u16* sh = (const u16*)d_in[3];
    const u16* sk = (const u16*)d_in[4];
    const u16* sD = (const u16*)d_in[5];
    const u16* wg = (const u16*)d_in[7];
    const u16* wu = (const u16*)d_in[8];
    const u16* wd = (const u16*)d_in[9];
    const u16* c = (const u16*)d_in[10];
    const u16* wm = (const u16*)d_in[11];
    char* ws = (char*)d_ws;
    float* mod = (float*)ws;
    u16* kv = (u16*)(ws + (1 << 20));
    u16* y = (u16*)(ws + (4 << 20));
    u16* x1 = (u16*)(ws + (8 << 20));
    u16* h2 = (u16*)(ws + (12 << 20));
    u16* H = (u16*)(ws + (16 << 20));
    Bufs bs;
    for (int i = 0; i < 8; ++i) { bs.p[i] = (const v4u*)q; bs.n1k[i] = 0; }
    k_probe<<<256, 512, 0, stream>>>(bs, (u64*)(ws + (40 << 20)), (unsigned*)(ws + (41 << 20)), (v4u*)(ws + (48 << 20)), 0);
    k_spin<<<1, 1, 0, stream>>>((const u64*)(ws + (40 << 20)));
    k_mod<<<(6 * D + 63) / 64, 64, 0, stream>>>(c, wm, mod);
    k_kv<<<(S * D + 255) / 256, 256, 0, stream>>>(k, v, sh, kv);
    k_conv<<<(S * D + 255) / 256, 256, 0, stream>>>(kv, sk, sD, y);
    k_ln<<<S, 64, 0, stream>>>(q, y, mod, x1, h2);
    k_g1<<<(S * F + 255) / 256, 256, 0, stream>>>(h2, wg, wu, H);
    k_g2<<<(S * D + 255) / 256, 256, 0, stream>>>(H, wd, x1, mod, (u16*)d_out);
}
```

```cpp
#include <hip/hip_runtime.h>
#include <hip/hip_bf16.h>
#include <stdint.h>

typedef unsigned short u16;
typedef unsigned u32;
typedef unsigned long long u64;
typedef short bf16x8 __attribute__((ext_vector_type(8)));
typedef short s16x4 __attribute__((ext_vector_type(4)));
typedef float f32x2 __attribute__((ext_vector_type(2)));
typedef float f32x4 __attribute__((ext_vector_type(4)));
typedef float f32x16 __attribute__((ext_vector_type(16)));
typedef u32 u32x2 __attribute__((ext_vector_type(2)));
typedef u32 u32x4 __attribute__((ext_vector_type(4)));
typedef u32 u32x8 __attribute__((ext_vector_type(8)));
typedef __bf16 bf16x2_t __attribute__((ext_vector_type(2)));
#define LAS __attribute__((address_space(3)))
#define GAS __attribute__((address_space(1)))
typedef LAS char* lptr;

constexpr int S = 2048, D = 640, F = 1728;
constexpr int KVS = 2112;
constexpr size_t WS_MOD = 0, WS_ST1 = 16 << 10, WS_C1 = 32 << 10, WS_C2 = 48 << 10;
constexpr size_t WS_KVR = 1 << 20, WS_QT = 4 << 20, WS_X1T = 8 << 20, WS_WP = 12 << 20, WS_WDP = 20 << 20, WS_HP = 24 << 20;

#define STAMP_AT(ws, kid, slot) do {} while (0)

__device__ __forceinline__ u32 cvtpk(float lo, float hi) { f32x2 v = {lo, hi}; return __builtin_bit_cast(u32, __builtin_convertvector(v, bf16x2_t)); }
__device__ __forceinline__ float lo16(u32 x) { return __uint_as_float(x << 16); }
__device__ __forceinline__ float hi16(u32 x) { return __uint_as_float(x & 0xffff0000u); }
__device__ __forceinline__ float rbf(float x) { return lo16(cvtpk(x, x)); }
__device__ __forceinline__ float bf2f(u16 x) { return __uint_as_float(((u32)x) << 16); }
__device__ __forceinline__ void glds16(const void* g, u32 lds_base) {
    asm volatile("s_mov_b32 m0, %1\n\ts_nop 0\n\tglobal_load_lds_dwordx4 %0, off" :: "v"(g), "s"(lds_base) : "memory");
}
__device__ __forceinline__ void glds16s(const void* sbase, u32 voff, u32 lds_base) {
    asm volatile("s_mov_b32 m0, %2\n\ts_nop 0\n\tglobal_load_lds_dwordx4 %0, %1" :: "v"(voff), "s"(sbase), "s"(lds_base) : "memory");
}
#define WAITV(n) asm volatile("s_waitcnt vmcnt(" #n ")" ::: "memory")
#define WAITV_BAR(n) asm volatile("s_waitcnt vmcnt(" #n ") lgkmcnt(0)\n\ts_barrier" ::: "memory")
#define SBAR() __builtin_amdgcn_sched_barrier(0)
__device__ __forceinline__ s16x4 ldtr(lptr p) { return __builtin_bit_cast(s16x4, __builtin_amdgcn_ds_read_tr16_b64_v4i16((LAS s16x4*)p)); }
__device__ __forceinline__ bf16x8 cat8(s16x4 a, s16x4 b) { return __builtin_shufflevector(a, b, 0, 1, 2, 3, 4, 5, 6, 7); }
template <int CTRL> __device__ __forceinline__ float dpp_f(float x) { return __int_as_float(__builtin_amdgcn_update_dpp(0, __float_as_int(x), CTRL, 0xf, 0xf, true)); }
__device__ __forceinline__ float row16_sum(float x) { x += dpp_f<0x128>(x); x += dpp_f<0x124>(x); x += dpp_f<0x122>(x); x += dpp_f<0x121>(x); return x; }
__device__ __forceinline__ int rfl(int x) { return __builtin_amdgcn_readfirstlane(x); }

constexpr int K0_NM = 120, K0_NS = 128, K0_NT = 320, K0_LDS = 25 * 1040;
__global__ __launch_bounds__(256) void k0_prep(const u16* __restrict__ q, const u16* __restrict__ k, const u16* __restrict__ v, const u16* __restrict__ sh,
                                               const u16* __restrict__ c, const u16* __restrict__ wm, char* __restrict__ ws) {
    extern __shared__ __attribute__((aligned(16))) char smem[];
    const int b = blockIdx.x, tid = threadIdx.x, l = tid & 63, w = rfl(tid >> 6);
    STAMP_AT(ws, 0, 0);
    if (b < K0_NT) {
        const int tt = b / 10, cg = b - tt * 10, t0 = tt * 64;
        const u32 lds0 = (u32)(uintptr_t)smem;
        const u32 voff = (u32)((l >> 3) * 1280 + (l & 7) * 16);
        const char* kb = (const char*)k + (size_t)(t0 - 8) * 1280 + cg * 128;
        const char* vb = (const char*)v + (size_t)t0 * 1280 + cg * 128;
        const char* qb = (const char*)q + (size_t)t0 * 1280 + cg * 128;
        const int o = l & 7, cp = (l >> 3) + 8 * w;
        const u32x4 shw = *(const u32x4*)(sh + (cg * 64 + 2 * cp) * 4);
#pragma unroll
        for (int i = 0; i < 2; ++i) {
            const int g = w + 4 * i;
            glds16s(kb + (size_t)(g + 1) * 10240, voff, lds0 + (g + 1) * 1040);
            glds16s(vb + (size_t)g * 10240, voff, lds0 + (9 + g) * 1040);
        }
        if (w == 0) {
            if (tt > 0) { if (l >= 40) glds16s(kb, voff, lds0); }
            else if (l >= 40) *(LAS u32x4*)((lptr)smem + l * 16) = (u32x4){0, 0, 0, 0};
        }
#pragma unroll
        for (int i = 0; i < 2; ++i) { const int g = w + 4 * i; glds16s(qb + (size_t)g * 10240, voff, lds0 + (17 + g) * 1040); }
        WAITV_BAR(0);
        const lptr base = (lptr)smem + o * 1040 + cp * 4;
        u32 kd[11], vd[8], qd[8];
#pragma unroll
        for (int i = 0; i < 3; ++i) kd[i] = *(LAS u32*)(base + 640 + 128 * i);
#pragma unroll
        for (int i = 0; i < 8; ++i) { kd[3 + i] = *(LAS u32*)(base + 1040 + 128 * i); vd[i] = *(LAS u32*)(base + 9 * 1040 + 128 * i); qd[i] = *(LAS u32*)(base + 17 * 1040 + 128 * i); }
        const f32x2 w0 = {lo16(shw.x), lo16(shw.z)}, w1 = {hi16(shw.x), hi16(shw.z)}, w2 = {lo16(shw.y), lo16(shw.w)}, w3 = {hi16(shw.y), hi16(shw.w)};
        f32x2 kf[11];
#pragma unroll
        for (int i = 0; i < 11; ++i) kf[i] = (f32x2){lo16(kd[i]), hi16(kd[i])};
        f32x2 kv[8];
#define R2(x) ({ const u32 u_ = cvtpk((x)[0], (x)[1]); (f32x2){lo16(u_), hi16(u_)}; })
#pragma unroll
        for (int i = 0; i < 8; ++i) {
            f32x2 a = kf[i] * w3; a = R2(a);
            f32x2 p = kf[i + 1] * w2; p = R2(p); a = a + p; a = R2(a);
            p = kf[i + 2] * w1; p = R2(p); a = a + p; a = R2(a);
            p = kf[i + 3] * w0; p = R2(p); a = a + p; a = R2(a);
            kv[i] = a * (f32x2){lo16(vd[i]), hi16(vd[i])};
        }
        u16* kvr = (u16*)(ws + WS_KVR); u16* qT = (u16*)(ws + WS_QT);
        const int ch = cg * 64 + 2 * cp, J = 255 - (tt * 8 + o);
#pragma unroll
        for (int e = 0; e < 2; ++e) {
            u32x4 r = {cvtpk(kv[7][e], kv[6][e]), cvtpk(kv[5][e], kv[4][e]), cvtpk(kv[3][e], kv[2][e]), cvtpk(kv[1][e], kv[0][e])};
            *(u32x4*)(kvr + (size_t)(ch + e) * KVS + J * 8) = r;
            const u32 sel = e ? 0x07060302u : 0x05040100u;
            u32x4 t = {__builtin_amdgcn_perm(qd[1], qd[0], sel), __builtin_amdgcn_perm(qd[3], qd[2], sel), __builtin_amdgcn_perm(qd[5], qd[4], sel), __builtin_amdgcn_perm(qd[7], qd[6], sel)};
            *(u32x4*)(qT + (size_t)(ch + e) * S + t0 + o * 8) = t;
        }
        if (tt == 0 && o < 2) *(u32x4*)(kvr + (size_t)(ch + o) * KVS + 2048) = (u32x4){0, 0, 0, 0};
    } else if (b < K0_NT + K0_NM) {
        const int mbi = b - K0_NT, mb = (mbi & 7) * 15 + (mbi >> 3);
        const int rl = l >> 2, cq = l & 3;
        const u16* wp = wm + (size_t)(w * 160 + rl) * (6 * D) + mb * 32 + cq * 8;
        u32x4 a[10]; float s[10];
#pragma unroll
        for (int i = 0; i < 10; ++i) a[i] = *(const u32x4*)(wp + (size_t)i * 16 * 6 * D);
#pragma unroll
        for (int i = 0; i < 10; ++i) { const float cv = bf2f(c[w * 160 + rl + 16 * i]); s[i] = rbf(cv / (1.f + __expf(-cv))); }
        float acc[8];
#pragma unroll
        for (int j = 0; j < 8; ++j) acc[j] = 0.f;
#pragma unroll
        for (int i = 0; i < 10; ++i)
#pragma unroll
            for (int j = 0; j < 4; ++j) { acc[2 * j] += s[i] * lo16(a[i][j]); acc[2 * j + 1] += s[i] * hi16(a[i][j]); }
        float* red = (float*)smem;
#pragma unroll
        for (int j = 0; j < 8; ++j) { float x = acc[j]; x += dpp_f<0x128>(x); x += dpp_f<0x124>(x); acc[j] = x; }
        if ((l & 12) == 0) {
#pragma unroll
            for (int j = 0; j < 8; ++j) red[(w * 4 + (l >> 4)) * 32 + cq * 8 + j] = acc[j];
        }
        __syncthreads();
        if (tid < 32) {
            float x = 0.f;
#pragma unroll
            for (int i = 0; i < 16; ++i) x += red[i * 32 + tid];
            ((float*)(ws + WS_MOD))[mb * 32 + tid] = rbf(x);
        }
    } else {
        const int t = (b - K0_NT - K0_NM) * 16 + w * 4 + (l >> 4);
        const u32x4* p = (const u32x4*)(q + (size_t)t * D) + (l & 15);
        u32x4 a[5];
#pragma unroll
        for (int i = 0; i < 5; ++i) a[i] = p[16 * i];
        float x[40], s = 0.f;
#pragma unroll
        for (int i = 0; i < 5; ++i)
#pragma unroll
            for (int j = 0; j < 4; ++j) { x[i * 8 + 2 * j] = lo16(a[i][j]); x[i * 8 + 2 * j + 1] = hi16(a[i][j]); s += x[i * 8 + 2 * j] + x[i * 8 + 2 * j + 1]; }
        const float m = row16_sum(s) * (1.f / D);
        float vs = 0.f;
#pragma unroll
        for (int i = 0; i < 40; ++i) { const float z = x[i] - m; vs += z * z; }
        const float var = row16_sum(vs) * (1.f / D);
        const float sd = rbf(sqrtf(rbf(rbf(var) + 1e-5f)));
        const float ri = 1.f / sd;
        if ((l & 15) == 0) ((f32x2*)(ws + WS_ST1))[t] = (f32x2){ri, -rbf(m) * ri};
    }
    STAMP_AT(ws, 0, 1);
}

constexpr int K1_NC = 160, K1_NW1 = 54, K1_NWD = 27;
constexpr int CV_A = 0, CV_B = 4352, CV_BC = 6656, CV_W = CV_B + 2 * CV_BC, CV_ST = 4 * CV_W, K1_LDS = CV_ST + 16 * 1040;
#define MFMA16(a, b, c) __builtin_amdgcn_mfma_f32_16x16x32_bf16(a, b, c, 0, 0, 0)
#define MFMA32(a, b, c) __builtin_amdgcn_mfma_f32_32x32x16_bf16(a, b, c, 0, 0, 0)

struct AWin { bf16x8 v3, v4, v7, v8; };
__device__ __forceinline__ AWin conv_win(lptr p) {
    const u32x4 x = *(LAS u32x4*)p, y = *(LAS u32x4*)(p + 16);
    const u32x8 d = __builtin_shufflevector(x, y, 0, 1, 2, 3, 4, 5, 6, 7);
    u32x8 f;
    f[0] = __builtin_amdgcn_alignbit(d[2], d[1], 16); f[1] = __builtin_amdgcn_alignbit(d[3], d[2], 16); f[2] = __builtin_amdgcn_alignbit(d[4], d[3], 16);
    f[3] = __builtin_amdgcn_alignbit(d[5], d[4], 16); f[4] = __builtin_amdgcn_alignbit(d[6], d[5], 16); f[5] = __builtin_amdgcn_alignbit(d[7], d[6], 16);
    f[6] = 0; f[7] = 0;
    AWin r;
    r.v3 = __builtin_bit_cast(bf16x8, __builtin_shufflevector(f, f, 0, 1, 2, 3));
    r.v7 = __builtin_bit_cast(bf16x8, __builtin_shufflevector(f, f, 2, 3, 4, 5));
    r.v4 = __builtin_bit_cast(bf16x8, __builtin_shufflevector(d, d, 2, 3, 4, 5));
    r.v8 = __builtin_bit_cast(bf16x8, y);
    return r;
}

__global__ __launch_bounds__(256) void k1_conv(const u16* __restrict__ sk, const u16* __restrict__ sD, const u16* __restrict__ wg, const u16* __restrict__ wu,
                                               const u16* __restrict__ wd, char* __restrict__ ws) {
    extern __shared__ __attribute__((aligned(16))) char smem[];
    const int b = blockIdx.x, tid = threadIdx.x, l = tid & 63, w = rfl(tid >> 6);
    const u32 lds0 = (u32)(uintptr_t)smem;
    const float* mod = (const float*)(ws + WS_MOD);
    STAMP_AT(ws, 1, 0);
    if (b < K1_NC) {
        const int d = b * 4 + w;
        const u32 wb = lds0 + w * CV_W;
        const char* kvrow = ws + WS_KVR + (size_t)d * KVS * 2;
#pragma unroll
        for (int i = 0; i < 5; ++i) {
            const int p = 64 * i + l, blk = (p * 3641) >> 16, in = min(p - 18 * blk, 15);
            const u32 off = (u32)(16 * blk + in) * 16;
            if (i < 4 || l < 32) { glds16s(kvrow, off, wb + CV_B + i * 1024); glds16s(kvrow + 4, off, wb + CV_B + CV_BC + i * 1024); }
        }
        const char* krow = (const char*)(sk + (size_t)d * S);
#pragma unroll
        for (int i = 0; i < 4; ++i) glds16s(krow + i * 1024, (u32)l * 16, wb + CV_A + 256 + i * 1024);
#pragma unroll
        for (int i = 0; i < 4; ++i) glds16s(ws + WS_ST1 + (w * 4 + i) * 1024, (u32)l * 16, lds0 + CV_ST + (w * 4 + i) * 1040);
        const lptr wl = (lptr)smem + w * CV_W;
        const u32x4 z4 = {0, 0, 0, 0};
        if (l < 16) *(LAS u32x4*)(wl + CV_A + l * 16) = z4;
#pragma unroll
        for (int i = 0; i < 2; ++i) { *(LAS u32x4*)(wl + CV_B + 288 * 16 + i * 1024 + l * 16) = z4; *(LAS u32x4*)(wl + CV_B + CV_BC + 288 * 16 + i * 1024 + l * 16) = z4; }
        const int col = l & 15, qq = l >> 4;
        const lptr pa = wl + CV_A + 16 * ((col >> 2) + 4 * (col & 3) + qq);
        const lptr pb1 = wl + CV_B + 16 * (18 * (15 - col) + qq);
        const lptr pb2 = wl + CV_B + (col >> 3) * CV_BC + 16 * (18 * (7 - (col & 7)) + qq);
        f32x4 acc[4][2], acc2[4];
#pragma unroll
        for (int i = 0; i < 4; ++i) { acc[i][0] = (f32x4){0, 0, 0, 0}; acc[i][1] = (f32x4){0, 0, 0, 0}; acc2[i] = (f32x4){0, 0, 0, 0}; }
        asm volatile("s_waitcnt vmcnt(4) lgkmcnt(0)" ::: "memory");
        STAMP_AT(ws, 1, 2);
        const u16* qrow = (const u16*)(ws + WS_QT) + (size_t)d * S + 128 * col + 8 * qq;
        u32x4 qv[4];
#pragma unroll
        for (int j = 0; j < 4; ++j) qv[j] = *(const u32x4*)(qrow + 32 * j);
#pragma unroll 2
        for (int nb = 0; nb < 8; ++nb) {
#pragma unroll
            for (int i = 0; i < 4; ++i) {
                const AWin a = conv_win(pa + 256 * nb + 64 * i);
                const bf16x8 b0 = *(LAS bf16x8*)(pb1 + 288 * nb + 64 * i), b1 = *(LAS bf16x8*)(pb1 + CV_BC + 288 * nb + 64 * i);
                acc[1][0] = MFMA16(a.v4, b0, acc[1][0]); acc[3][0] = MFMA16(a.v8, b0, acc[3][0]);
                acc[1][1] = MFMA16(a.v4, b1, acc[1][1]); acc[3][1] = MFMA16(a.v8, b1, acc[3][1]);
                acc[0][0] = MFMA16(a.v3, b0, acc[0][0]); acc[2][0] = MFMA16(a.v7, b0, acc[2][0]);
                acc[0][1] = MFMA16(a.v3, b1, acc[0][1]); acc[2][1] = MFMA16(a.v7, b1, acc[2][1]);
            }
        }
#pragma unroll 2
        for (int nb = 8; nb < 16; ++nb) {
#pragma unroll
            for (int i = 0; i < 4; ++i) {
                const AWin a = conv_win(pa + 256 * nb + 64 * i);
                const bf16x8 b0 = *(LAS bf16x8*)(pb2 + 288 * nb + 64 * i);
                acc2[1] = MFMA16(a.v4, b0, acc2[1]); acc2[3] = MFMA16(a.v8, b0, acc2[3]);
                acc2[0] = MFMA16(a.v3, b0, acc2[0]); acc2[2] = MFMA16(a.v7, b0, acc2[2]);
            }
        }
        STAMP_AT(ws, 1, 3);
        WAITV_BAR(0);
        const float sa = mod[d], ca = mod[D + d], ga = mod[2 * D + d], dc = bf2f(sD[d]);
        const float cA = ga * rbf(1.f + ca), cB = ga * sa;
        u16* xrow = (u16*)(ws + WS_X1T) + (size_t)d * S + 128 * col + 8 * qq;
#pragma unroll
        for (int j = 0; j < 4; ++j) {
            float y[8];
#pragma unroll
            for (int vv = 0; vv < 4; ++vv) {
                const int rho = (vv & 1) + 4 * (vv >> 1);
                const int e2 = __float_as_int(acc2[vv][j]);
                y[rho + 2] = acc[vv][0][j] + __int_as_float(__builtin_amdgcn_update_dpp(0, e2, 0x118, 0xf, 0xf, true));
                y[rho] = acc[vv][1][j] + __int_as_float(__builtin_amdgcn_update_dpp(0, e2, 0xE4, 0xf, 0xc, false));
            }
            const int ci = qq + 4 * j;
            const u32x4 kr = *(LAS u32x4*)(wl + CV_B + 16 * (18 * (15 - col) + 15 - ci));
            const lptr sp = (lptr)smem + CV_ST + col * 1040 + ci * 64;
            u32x4 o;
#pragma unroll
            for (int h = 0; h < 4; ++h) {
                const f32x4 st = *(LAS f32x4*)(sp + 16 * h);
                const float q0 = lo16(qv[j][h]), q1 = hi16(qv[j][h]);
                const float y0 = y[2 * h] + hi16(kr[3 - h]) * dc, y1 = y[2 * h + 1] + lo16(kr[3 - h]) * dc;
                const float g0 = (q0 * st[0] + st[1]) * cA + cB, g1 = (q1 * st[2] + st[3]) * cA + cB;
                o[h] = cvtpk(q0 + g0 * y0, q1 + g1 * y1);
            }
            *(u32x4*)(xrow + 32 * j) = o;
        }
    } else if (b < K1_NC + K1_NW1) {
        const int pi0 = b - K1_NC, pi = (pi0 < 48) ? (pi0 & 7) * 6 + (pi0 >> 3) : pi0;
        const u32 voff = (u32)((l >> 2) * (F * 2) + (l & 3) * 16);
        const char* gb = (const char*)wg + (size_t)(160 * w) * (F * 2) + pi * 64;
        const char* ub = (const char*)wu + (size_t)(160 * w) * (F * 2) + pi * 64;
        const u32 wb = lds0 + w * 20480;
#pragma unroll
        for (int i = 0; i < 10; ++i) { glds16s(gb + (size_t)i * 16 * F * 2, voff, wb + i * 1024); glds16s(ub + (size_t)i * 16 * F * 2, voff, wb + 10240 + i * 1024); }
        const int h = l >> 5, blk = (l >> 4) & 1, qq = (l & 15) >> 2, p = l & 3;
        const lptr rp = (lptr)smem + w * 20480 + (p >> 1) * 10240 + (8 * h + qq) * 64 + blk * 16 + (p & 1) * 8;
        const float* cbp = mod + 4 * D + 160 * w + 8 * h;
        const float* sbp = mod + 3 * D + 160 * w + 8 * h;
        float c1[2] = {0.f, 0.f}, c2[2] = {0.f, 0.f};
        u32x4* wp = (u32x4*)(ws + WS_WP) + (size_t)(2 * pi) * 40 * 64 + (size_t)(10 * w) * 64 + l;
        WAITV(0);
#pragma unroll 2
        for (int i = 0; i < 10; ++i) {
            const f32x4 cb0 = *(const f32x4*)(cbp + 16 * i), cb1 = *(const f32x4*)(cbp + 16 * i + 4), sb0 = *(const f32x4*)(sbp + 16 * i), sb1 = *(const f32x4*)(sbp + 16 * i + 4);
            float cs[8], ss[8];
#pragma unroll
            for (int j = 0; j < 4; ++j) { cs[j] = rbf(1.f + cb0[j]); cs[4 + j] = rbf(1.f + cb1[j]); ss[j] = sb0[j]; ss[4 + j] = sb1[j]; }
#pragma unroll
            for (int u = 0; u < 2; ++u) {
                const u32x2 r0 = __builtin_bit_cast(u32x2, ldtr(rp + i * 1024 + u * 32)), r1 = __builtin_bit_cast(u32x2, ldtr(rp + i * 1024 + u * 32 + 256));
                const u32 raw[4] = {r0[0], r0[1], r1[0], r1[1]};
                u32x4 o;
#pragma unroll
                for (int j = 0; j < 4; ++j) {
                    const float a0 = lo16(raw[j]), a1 = hi16(raw[j]);
                    c2[u] += a0 * ss[2 * j] + a1 * ss[2 * j + 1];
                    o[j] = cvtpk(a0 * cs[2 * j], a1 * cs[2 * j + 1]);
                    c1[u] += lo16(o[j]) + hi16(o[j]);
                }
                wp[(size_t)u * 40 * 64 + i * 64] = o;
            }
        }
        __syncthreads();
        float* red = (float*)smem;
        red[(0 * 4 + w) * 64 + l] = c1[0]; red[(1 * 4 + w) * 64 + l] = c1[1]; red[(2 * 4 + w) * 64 + l] = c2[0]; red[(3 * 4 + w) * 64 + l] = c2[1];
        __syncthreads();
        if (tid < 128) {
            const int g = tid >> 5, r = tid & 31;
            float x = 0.f;
#pragma unroll
            for (int i = 0; i < 4; ++i) x += red[(g * 4 + i) * 64 + r] + red[(g * 4 + i) * 64 + 32 + r];
            ((float*)(ws + (g < 2 ? WS_C1 : WS_C2)))[(2 * pi + (g & 1)) * 32 + r] = x;
        }
    } else {
        const int pi = b - K1_NC - K1_NW1;
        const char* src = (const char*)wd + (size_t)pi * 64 * 1280;
#pragma unroll
        for (int i = 0; i < 20; ++i) glds16s(src + (w * 20 + i) * 1024, (u32)l * 16, lds0 + (w * 20 + i) * 1024);
        WAITV_BAR(0);
        const int g = l >> 4, qq = (l & 15) >> 2, p = l & 3;
        const int ks = w >> 1;
        const lptr rp = (lptr)smem + (ks * 32 + 16 * (g >> 1) + 4 * (g & 1) + qq) * 1280 + p * 40 + (w & 1) * 640;
        u32x4* dst = (u32x4*)(ws + WS_WDP) + l;
#pragma unroll
        for (int cb = 0; cb < 4; ++cb)
#pragma unroll
            for (int n = 0; n < 5; ++n) {
                const s16x4 r0 = ldtr(rp + cb * 160 + n * 8), r1 = ldtr(rp + cb * 160 + n * 8 + 8 * 1280);
                dst[(size_t)((((w & 1) * 4 + cb) * 54 + 2 * pi + ks) * 5 + n) * 64] = __builtin_bit_cast(u32x4, cat8(r0, r1));
            }
    }
    STAMP_AT(ws, 1, 1);
}

template <int V> struct IC { static constexpr int value = V; };
constexpr int K2_LDS = 163840, K2_P = 12;
__global__ __launch_bounds__(512) void k2_gemm1(char* __restrict__ ws) {
    extern __shared__ __attribute__((aligned(16))) char smem[];
    const int b = blockIdx.x, tid = threadIdx.x, l = tid & 63, w = rfl(tid >> 6);
    const u32 lds0 = (u32)(uintptr_t)smem;
    STAMP_AT(ws, 2, 0);
    const int x = b & 7, i5 = b >> 3, cgp = x >> 1, ncb = (cgp & 1) ? 3 : 4, cb0 = (cgp * 7 + 1) >> 1;
    if (i5 >= 8 * ncb) return;
    const int tb = (x & 1) * 8 + (i5 & 7), cb = cb0 + (i5 >> 3);
    const int U = min(cb * 8 + w, 107);
    const bool live = cb * 8 + w < 108;
    const int wq = w & 3, wh = w >> 2;
    const u32 xoff = (u32)((l >> 4) * 4096 + 16 * ((l & 15) ^ (((l >> 4) << 2) | wq)));
    const char* xb = ws + WS_X1T + (size_t)(wh * 16 + wq * 4) * 4096 + tb * 256;
    const u32 xdst = lds0 + (wh * 4 + wq) * 1024;
    const char* wb = ws + WS_WP + (size_t)U * 40960;
    const u32 woff = (u32)l * 16;
    u32x4 wf[40];
#define LDW(s) wf[s] = *(const u32x4*)(wb + (s) * 1024 + woff)
#define LDX(sp) glds16s(xb + (size_t)(sp) * 131072, xoff, xdst + (sp) * 8192)
#pragma unroll
    for (int sp = 0; sp < K2_P / 2; ++sp) { LDX(sp); LDW(2 * sp); LDW(2 * sp + 1); }
    const int h = l >> 5, blk = (l >> 4) & 1, qq = (l & 15) >> 2, p = l & 3;
    lptr xr[4][2];
#pragma unroll
    for (int c = 0; c < 4; ++c)
#pragma unroll
        for (int t = 0; t < 2; ++t) xr[c][t] = (lptr)smem + 256 * (8 * h + 4 * t + qq) + 64 * (c ^ wq ^ qq) + 16 * ((2 * blk + (p >> 1)) ^ (2 * h + t)) + 8 * (p & 1);
    f32x16 acc[4];
#pragma unroll
    for (int c = 0; c < 4; ++c)
#pragma unroll
        for (int r = 0; r < 16; ++r) acc[c][r] = 0.f;
    float s1 = 0.f, s2 = 0.f;
    const bf16x2_t ones = {(__bf16)1.0f, (__bf16)1.0f};
    auto mainloop = [&](auto WH) __attribute__((always_inline)) {
#pragma unroll
        for (int sp = 0; sp < 20; ++sp) {
            constexpr int HP = K2_P / 2;
            const int after = (sp + HP <= 20) ? HP - 1 : 19 - sp;
            asm volatile("s_waitcnt vmcnt(%0)\n\ts_barrier" :: "i"(3 * after + 2) : "memory");
            if (sp == 0) STAMP_AT(ws, 2, 2);
            if (sp + HP < 20) { LDX(sp + HP); LDW(2 * (sp + HP)); LDW(2 * (sp + HP) + 1); }
#pragma unroll
            for (int e = 0; e < 2; ++e) {
                const int s = 2 * sp + e;
                bf16x8 xf[4];
#pragma unroll
                for (int c = 0; c < 4; ++c) xf[c] = cat8(ldtr(xr[c][0] + 4096 * s), ldtr(xr[c][1] + 4096 * s));
                const bf16x8 a = __builtin_bit_cast(bf16x8, wf[s]);
#pragma unroll
                for (int c = 0; c < 4; ++c) acc[c] = MFMA32(a, xf[c], acc[c]);
                if (e == WH.value) {
                    const u32x4 xs = __builtin_bit_cast(u32x4, xf[0]);
#pragma unroll
                    for (int j = 0; j < 4; ++j) {
                        const u32 xj = xs[j]; const bf16x2_t e2 = __builtin_bit_cast(bf16x2_t, xj);
                        s1 = __builtin_amdgcn_fdot2_f32_bf16(e2, ones, s1, false);
                        s2 = __builtin_amdgcn_fdot2_f32_bf16(e2, e2, s2, false);
                    }
                    asm volatile("" : "+v"(s1), "+v"(s2));
                }
            }
        }
    };
    if (wh == 0) mainloop(IC<0>{}); else mainloop(IC<1>{});
    STAMP_AT(ws, 2, 3);
    f32x2* red = (f32x2*)smem;
    red[w * 64 + l] = (f32x2){s1, s2};
    const f32x4* c1p = (const f32x4*)(ws + WS_C1) + U * 8 + h;
    const f32x4* c2p = (const f32x4*)(ws + WS_C2) + U * 8 + h;
    f32x4 c1[4], c2[4];
#pragma unroll
    for (int g = 0; g < 4; ++g) { c1[g] = c1p[2 * g]; c2[g] = c2p[2 * g]; }
    __syncthreads();
    u32x4* hp = (u32x4*)(ws + WS_HP) + ((size_t)(tb * 8 + ((l >> 4) & 1)) * 54 + (U >> 1)) * 64 + (l & 15) + 16 * (2 * (U & 1) + h);
#pragma unroll
    for (int c = 0; c < 4; ++c) {
        const int ct = c ^ wq;
        const f32x2 a0 = red[ct * 64 + (l & 31)], a1 = red[ct * 64 + 32 + (l & 31)], a2 = red[(ct + 4) * 64 + (l & 31)], a3 = red[(ct + 4) * 64 + 32 + (l & 31)];
        const float mu = (a0[0] + a1[0] + a2[0] + a3[0]) * (1.f / D);
        const float var = fmaxf((a0[1] + a1[1] + a2[1] + a3[1]) * (1.f / D) - mu * mu, 0.f);
        const float rs = rsqrtf(var + 1e-5f);
        u32x4 o;
#pragma unroll
        for (int hh = 0; hh < 2; ++hh) {
            float hv[4];
#pragma unroll
            for (int j = 0; j < 4; ++j) {
                const float g = rs * (acc[c][8 * hh + j] - mu * c1[2 * hh][j]) + c2[2 * hh][j];
                const float u = rs * (acc[c][8 * hh + 4 + j] - mu * c1[2 * hh + 1][j]) + c2[2 * hh + 1][j];
                hv[j] = fmaxf(g, 0.f) * u;
            }
            o[2 * hh] = cvtpk(hv[0], hv[1]); o[2 * hh + 1] = cvtpk(hv[2], hv[3]);
        }
        if (live) hp[(size_t)ct * 2 * 54 * 64] = o;
    }
    STAMP_AT(ws, 2, 1);
}

constexpr int K3_G = 2;
constexpr int K3_NST = 8;
constexpr int K3_STB = K3_G * 9 * 1024;
constexpr int K3_X = K3_NST * K3_STB;
constexpr int K3_LDS = K3_X + 10240;
__global__ __launch_bounds__(256) void k3_gemm2(char* __restrict__ ws, u16* __restrict__ out) {
    extern __shared__ __attribute__((aligned(16))) char smem[];
    const int b = blockIdx.x, tid = threadIdx.x, l = tid & 63, w = rfl(tid >> 6);
    const u32 lds0 = (u32)(uintptr_t)smem;
    STAMP_AT(ws, 3, 0);
    const int x = b & 7, i5 = b >> 3, tt = (x >> 1) * 8 + (i5 & 7), cb = (x & 1) * 4 + (i5 >> 3);
    const u32 voff = (u32)l * 16;
    const char* hb = ws + WS_HP + (size_t)(tt * 4 + w) * 54 * 1024;
    const char* wb = ws + WS_WDP + (size_t)cb * 54 * 5 * 1024 + w * 1024;
#define K3_ISSUE(st) do { const u32 sb_ = lds0 + ((st) % K3_NST) * K3_STB; \
        glds16s(hb + (size_t)(st) * 2048, voff, sb_ + w * 2048); glds16s(hb + (size_t)(st) * 2048 + 1024, voff, sb_ + w * 2048 + 1024); \
        glds16s(wb + (size_t)(st) * 10240, voff, sb_ + 8192 + w * 1024); glds16s(wb + (size_t)(st) * 10240 + 4096, voff, sb_ + 8192 + 4096 + w * 1024); \
        glds16s(wb + (size_t)(st) * 10240 + (w < 2 ? 8192 : 4096), voff, sb_ + 8192 + w * 1024 + (w < 2 ? 8192 : 4096)); } while (0)
    constexpr int AH = K3_NST - 1;
#pragma unroll
    for (int st = 0; st < AH; ++st) K3_ISSUE(st);
    f32x4 acc[5];
#pragma unroll
    for (int n = 0; n < 5; ++n) acc[n] = (f32x4){0, 0, 0, 0};
    const lptr hr = (lptr)smem + w * 2048 + l * 16, wr = (lptr)smem + 8192 + l * 16;
#pragma unroll
    for (int st = 0; st < 27; ++st) {
        const int after = (st + AH <= 27) ? AH - 1 : 26 - st;
        const int cnt = 5 * after + ((st + AH > 27) ? 3 : 0);
        asm volatile("s_waitcnt vmcnt(%0) lgkmcnt(0)\n\ts_barrier" :: "i"(cnt) : "memory");
        if (st == 0) STAMP_AT(ws, 3, 2);
        if (st + AH < 27) K3_ISSUE(st + AH);
        if (st + AH == 27) {
            const char* xb = ws + WS_X1T + (size_t)(cb * 80) * 4096 + tt * 128;
#pragma unroll
            for (int i = 0; i < 3; ++i) { const int pc = min(w + 4 * i, 9); glds16s(xb + (size_t)pc * 8 * 4096, (u32)((l >> 3) * 4096 + (l & 7) * 16), lds0 + K3_X + pc * 1024); }
        }
        const int so = (st % K3_NST) * K3_STB;
#pragma unroll
        for (int e = 0; e < K3_G; ++e) {
            const bf16x8 hf = *(LAS bf16x8*)(hr + so + e * 1024);
#pragma unroll
            for (int n = 0; n < 5; ++n) acc[n] = MFMA16(*(LAS bf16x8*)(wr + so + (e * 5 + n) * 1024), hf, acc[n]);
        }
    }
    STAMP_AT(ws, 3, 3);
    const int g = l >> 4, qq = (l & 15) >> 2, p = l & 3;
    const float* gbp = (const float*)(ws + WS_MOD) + 5 * D + cb * 80 + 20 * g;
    f32x4 gb[5];
#pragma unroll
    for (int n = 0; n < 5; ++n) gb[n] = *(const f32x4*)(gbp + 4 * n);
    WAITV_BAR(0);
    const lptr xr = (lptr)smem + K3_X + (20 * g + qq) * 128 + w * 32 + p * 8;
    u32 o[10];
#pragma unroll
    for (int n = 0; n < 5; ++n) {
        const u32x2 xv = __builtin_bit_cast(u32x2, ldtr(xr + n * 512));
        o[2 * n] = cvtpk(lo16(xv[0]) + gb[n][0] * acc[n][0], hi16(xv[0]) + gb[n][1] * acc[n][1]);
        o[2 * n + 1] = cvtpk(lo16(xv[1]) + gb[n][2] * acc[n][2], hi16(xv[1]) + gb[n][3] * acc[n][3]);
    }
    u32* op = (u32*)(out + (size_t)(tt * 64 + w * 16 + (l & 15)) * D + cb * 80 + 20 * g);
#pragma unroll
    for (int n = 0; n < 5; ++n) *(u32x2*)(op + 2 * n) = (u32x2){o[2 * n], o[2 * n + 1]};
    STAMP_AT(ws, 3, 1);
}

extern "C" void kernel_launch(void* const* d_in, const int* in_sizes, int n_in, void* d_out, int out_size, void* d_ws, size_t ws_size, hipStream_t stream) {
    const u16* q = (const u16*)d_in[0];
    const u16* k = (const u16*)d_in[1];
    const u16* v = (const u16*)d_in[2];
    const u16* sh = (const u16*)d_in[3];
    const u16* sk = (const u16*)d_in[4];
    const u16* sD = (const u16*)d_in[5];
    const u16* wg = (const u16*)d_in[7];
    const u16* wu = (const u16*)d_in[8];
    const u16* wd = (const u16*)d_in[9];
    const u16* c = (const u16*)d_in[10];
    const u16* wm = (const u16*)d_in[11];
    char* ws = (char*)d_ws;
    (void)hipFuncSetAttribute((const void*)k1_conv, hipFuncAttributeMaxDynamicSharedMemorySize, K1_LDS);
    (void)hipFuncSetAttribute((const void*)k2_gemm1, hipFuncAttributeMaxDynamicSharedMemorySize, K2_LDS);
    (void)hipFuncSetAttribute((const void*)k3_gemm2, hipFuncAttributeMaxDynamicSharedMemorySize, K3_LDS);
    k0_prep<<<K0_NT + K0_NM + K0_NS, 256, K0_LDS, stream>>>(q, k, v, sh, c, wm, ws);
    k1_conv<<<K1_NC + K1_NW1 + K1_NWD, 256, K1_LDS, stream>>>(sk, sD, wg, wu, wd, ws);
    k2_gemm1<<<256, 512, K2_LDS, stream>>>(ws);
    k3_gemm2<<<256, 256, K3_LDS, stream>>>(ws, (u16*)d_out);
}
```

```cpp
#define STAMP
#include <hip/hip_runtime.h>
#include <hip/hip_bf16.h>
#include <stdint.h>

typedef unsigned short u16;
typedef unsigned u32;
typedef unsigned long long u64;
typedef short bf16x8 __attribute__((ext_vector_type(8)));
typedef short s16x4 __attribute__((ext_vector_type(4)));
typedef float f32x2 __attribute__((ext_vector_type(2)));
typedef float f32x4 __attribute__((ext_vector_type(4)));
typedef float f32x16 __attribute__((ext_vector_type(16)));
typedef u32 u32x2 __attribute__((ext_vector_type(2)));
typedef u32 u32x4 __attribute__((ext_vector_type(4)));
typedef u32 u32x8 __attribute__((ext_vector_type(8)));
typedef __bf16 bf16x2_t __attribute__((ext_vector_type(2)));
#define LAS __attribute__((address_space(3)))
#define GAS __attribute__((address_space(1)))
typedef LAS char* lptr;

constexpr int S = 2048, D = 640, F = 1728;
constexpr int KVS = 2112;
constexpr size_t WS_MOD = 0, WS_ST1 = 16 << 10, WS_C1 = 32 << 10, WS_C2 = 48 << 10;
constexpr size_t WS_KVR = 1 << 20, WS_QT = 4 << 20, WS_X1T = 8 << 20, WS_WP = 12 << 20, WS_WDP = 20 << 20, WS_HP = 24 << 20;

__device__ __forceinline__ u64 rt() { return __builtin_amdgcn_s_memrealtime(); }
#define STAMP_AT(ws, kid, slot) do { if (threadIdx.x == 0) ((u64*)((char*)(ws) + (40 << 20)))[((kid) * 1024 + blockIdx.x) * 8 + (slot)] = rt(); } while (0)

__device__ __forceinline__ u32 cvtpk(float lo, float hi) { f32x2 v = {lo, hi}; return __builtin_bit_cast(u32, __builtin_convertvector(v, bf16x2_t)); }
__device__ __forceinline__ float lo16(u32 x) { return __uint_as_float(x << 16); }
__device__ __forceinline__ float hi16(u32 x) { return __uint_as_float(x & 0xffff0000u); }
__device__ __forceinline__ float rbf(float x) { return lo16(cvtpk(x, x)); }
__device__ __forceinline__ float bf2f(u16 x) { return __uint_as_float(((u32)x) << 16); }
__device__ __forceinline__ void glds16(const void* g, u32 lds_base) {
    asm volatile("s_mov_b32 m0, %1\n\ts_nop 0\n\tglobal_load_lds_dwordx4 %0, off" :: "v"(g), "s"(lds_base) : "memory");
}
__device__ __forceinline__ void glds16s(const void* sbase, u32 voff, u32 lds_base) {
    asm volatile("s_mov_b32 m0, %2\n\ts_nop 0\n\tglobal_load_lds_dwordx4 %0, %1" :: "v"(voff), "s"(sbase), "s"(lds_base) : "memory");
}
#define WAITV(n) asm volatile("s_waitcnt vmcnt(" #n ")" ::: "memory")
#define WAITV_BAR(n) asm volatile("s_waitcnt vmcnt(" #n ") lgkmcnt(0)\n\ts_barrier" ::: "memory")
#define SBAR() __builtin_amdgcn_sched_barrier(0)
__device__ __forceinline__ s16x4 ldtr(lptr p) { return __builtin_bit_cast(s16x4, __builtin_amdgcn_ds_read_tr16_b64_v4i16((LAS s16x4*)p)); }
__device__ __forceinline__ bf16x8 cat8(s16x4 a, s16x4 b) { return __builtin_shufflevector(a, b, 0, 1, 2, 3, 4, 5, 6, 7); }
template <int CTRL> __device__ __forceinline__ float dpp_f(float x) { return __int_as_float(__builtin_amdgcn_update_dpp(0, __float_as_int(x), CTRL, 0xf, 0xf, true)); }
__device__ __forceinline__ float row16_sum(float x) { x += dpp_f<0x128>(x); x += dpp_f<0x124>(x); x += dpp_f<0x122>(x); x += dpp_f<0x121>(x); return x; }
__device__ __forceinline__ int rfl(int x) { return __builtin_amdgcn_readfirstlane(x); }

constexpr int K0_NM = 120, K0_NS = 128, K0_NT = 320, K0_LDS = 25 * 1040;
__global__ __launch_bounds__(256) void k0_prep(const u16* __restrict__ q, const u16* __restrict__ k, const u16* __restrict__ v, const u16* __restrict__ sh,
                                               const u16* __restrict__ c, const u16* __restrict__ wm, char* __restrict__ ws) {
    extern __shared__ __attribute__((aligned(16))) char smem[];
    const int b = blockIdx.x, tid = threadIdx.x, l = tid & 63, w = rfl(tid >> 6);
    STAMP_AT(ws, 0, 0);
    if (b < K0_NT) {
        const int tt = b / 10, cg = b - tt * 10, t0 = tt * 64;
        const u32 lds0 = (u32)(uintptr_t)smem;
        const u32 voff = (u32)((l >> 3) * 1280 + (l & 7) * 16);
        const char* kb = (const char*)k + (size_t)(t0 - 8) * 1280 + cg * 128;
        const char* vb = (const char*)v + (size_t)t0 * 1280 + cg * 128;
        const char* qb = (const char*)q + (size_t)t0 * 1280 + cg * 128;
        const int o = l & 7, cp = (l >> 3) + 8 * w;
        const u32x4 shw = *(const u32x4*)(sh + (cg * 64 + 2 * cp) * 4);
#pragma unroll
        for (int i = 0; i < 2; ++i) {
            const int g = w + 4 * i;
            glds16s(kb + (size_t)(g + 1) * 10240, voff, lds0 + (g + 1) * 1040);
            glds16s(vb + (size_t)g * 10240, voff, lds0 + (9 + g) * 1040);
        }
        if (w == 0) {
            if (tt > 0) { if (l >= 40) glds16s(kb, voff, lds0); }
            else if (l >= 40) *(LAS u32x4*)((lptr)smem + l * 16) = (u32x4){0, 0, 0, 0};
        }
#pragma unroll
        for (int i = 0; i < 2; ++i) { const int g = w + 4 * i; glds16s(qb + (size_t)g * 10240, voff, lds0 + (17 + g) * 1040); }
        WAITV_BAR(0);
        const lptr base = (lptr)smem + o * 1040 + cp * 4;
        u32 kd[11], vd[8], qd[8];
#pragma unroll
        for (int i = 0; i < 3; ++i) kd[i] = *(LAS u32*)(base + 640 + 128 * i);
#pragma unroll
        for (int i = 0; i < 8; ++i) { kd[3 + i] = *(LAS u32*)(base + 1040 + 128 * i); vd[i] = *(LAS u32*)(base + 9 * 1040 + 128 * i); qd[i] = *(LAS u32*)(base + 17 * 1040 + 128 * i); }
        const f32x2 w0 = {lo16(shw.x), lo16(shw.z)}, w1 = {hi16(shw.x), hi16(shw.z)}, w2 = {lo16(shw.y), lo16(shw.w)}, w3 = {hi16(shw.y), hi16(shw.w)};
        f32x2 kf[11];
#pragma unroll
        for (int i = 0; i < 11; ++i) kf[i] = (f32x2){lo16(kd[i]), hi16(kd[i])};
        f32x2 kv[8];
#define R2(x) ({ const u32 u_ = cvtpk((x)[0], (x)[1]); (f32x2){lo16(u_), hi16(u_)}; })
#pragma unroll
        for (int i = 0; i < 8; ++i) {
            f32x2 a = kf[i] * w3; a = R2(a);
            f32x2 p = kf[i + 1] * w2; p = R2(p); a = a + p; a = R2(a);
            p = kf[i + 2] * w1; p = R2(p); a = a + p; a = R2(a);
            p = kf[i + 3] * w0; p = R2(p); a = a + p; a = R2(a);
            kv[i] = a * (f32x2){lo16(vd[i]), hi16(vd[i])};
        }
        u16* kvr = (u16*)(ws + WS_KVR); u16* qT = (u16*)(ws + WS_QT);
        const int ch = cg * 64 + 2 * cp, J = 255 - (tt * 8 + o);
#pragma unroll
        for (int e = 0; e < 2; ++e) {
            u32x4 r = {cvtpk(kv[7][e], kv[6][e]), cvtpk(kv[5][e], kv[4][e]), cvtpk(kv[3][e], kv[2][e]), cvtpk(kv[1][e], kv[0][e])};
            *(u32x4*)(kvr + (size_t)(ch + e) * KVS + J * 8) = r;
            const u32 sel = e ? 0x07060302u : 0x05040100u;
            u32x4 t = {__builtin_amdgcn_perm(qd[1], qd[0], sel), __builtin_amdgcn_perm(qd[3], qd[2], sel), __builtin_amdgcn_perm(qd[5], qd[4], sel), __builtin_amdgcn_perm(qd[7], qd[6], sel)};
            *(u32x4*)(qT + (size_t)(ch + e) * S + t0 + o * 8) = t;
        }
        if (tt == 0 && o < 2) *(u32x4*)(kvr + (size_t)(ch + o) * KVS + 2048) = (u32x4){0, 0, 0, 0};
    } else if (b < K0_NT + K0_NM) {
        const int mbi = b - K0_NT, mb = (mbi & 7) * 15 + (mbi >> 3);
        const int rl = l >> 2, cq = l & 3;
        const u16* wp = wm + (size_t)(w * 160 + rl) * (6 * D) + mb * 32 + cq * 8;
        u32x4 a[10]; float s[10];
#pragma unroll
        for (int i = 0; i < 10; ++i) a[i] = *(const u32x4*)(wp + (size_t)i * 16 * 6 * D);
#pragma unroll
        for (int i = 0; i < 10; ++i) { const float cv = bf2f(c[w * 160 + rl + 16 * i]); s[i] = rbf(cv / (1.f + __expf(-cv))); }
        float acc[8];
#pragma unroll
        for (int j = 0; j < 8; ++j) acc[j] = 0.f;
#pragma unroll
        for (int i = 0; i < 10; ++i)
#pragma unroll
            for (int j = 0; j < 4; ++j) { acc[2 * j] += s[i] * lo16(a[i][j]); acc[2 * j + 1] += s[i] * hi16(a[i][j]); }
        float* red = (float*)smem;
#pragma unroll
        for (int j = 0; j < 8; ++j) { float x = acc[j]; x += dpp_f<0x128>(x); x += dpp_f<0x124>(x); acc[j] = x; }
        if ((l & 12) == 0) {
#pragma unroll
            for (int j = 0; j < 8; ++j) red[(w * 4 + (l >> 4)) * 32 + cq * 8 + j] = acc[j];
        }
        __syncthreads();
        if (tid < 32) {
            float x = 0.f;
#pragma unroll
            for (int i = 0; i < 16; ++i) x += red[i * 32 + tid];
            ((float*)(ws + WS_MOD))[mb * 32 + tid] = rbf(x);
        }
    } else {
        const int t = (b - K0_NT - K0_NM) * 16 + w * 4 + (l >> 4);
        const u32x4* p = (const u32x4*)(q + (size_t)t * D) + (l & 15);
        u32x4 a[5];
#pragma unroll
        for (int i = 0; i < 5; ++i) a[i] = p[16 * i];
        float x[40], s = 0.f;
#pragma unroll
        for (int i = 0; i < 5; ++i)
#pragma unroll
            for (int j = 0; j < 4; ++j) { x[i * 8 + 2 * j] = lo16(a[i][j]); x[i * 8 + 2 * j + 1] = hi16(a[i][j]); s += x[i * 8 + 2 * j] + x[i * 8 + 2 * j + 1]; }
        const float m = row16_sum(s) * (1.f / D);
        float vs = 0.f;
#pragma unroll
        for (int i = 0; i < 40; ++i) { const float z = x[i] - m; vs += z * z; }
        const float var = row16_sum(vs) * (1.f / D);
        const float sd = rbf(sqrtf(rbf(rbf(var) + 1e-5f)));
        const float ri = 1.f / sd;
        if ((l & 15) == 0) ((f32x2*)(ws + WS_ST1))[t] = (f32x2){ri, -rbf(m) * ri};
    }
    STAMP_AT(ws, 0, 1);
}

constexpr int K1_NC = 160, K1_NW1 = 54, K1_NWD = 27;
constexpr int CV_A = 0, CV_B = 4352, CV_BC = 6656, CV_W = CV_B + 2 * CV_BC, CV_ST = 4 * CV_W, K1_LDS = CV_ST + 16 * 1040;
#define MFMA16(a, b, c) __builtin_amdgcn_mfma_f32_16x16x32_bf16(a, b, c, 0, 0, 0)
#define MFMA32(a, b, c) __builtin_amdgcn_mfma_f32_32x32x16_bf16(a, b, c, 0, 0, 0)

struct AWin { bf16x8 v3, v4, v7, v8; };
__device__ __forceinline__ AWin conv_win(lptr p) {
    const u32x4 x = *(LAS u32x4*)p, y = *(LAS u32x4*)(p + 16);
    const u32x8 d = __builtin_shufflevector(x, y, 0, 1, 2, 3, 4, 5, 6, 7);
    u32x8 f;
    f[0] = __builtin_amdgcn_alignbit(d[2], d[1], 16); f[1] = __builtin_amdgcn_alignbit(d[3], d[2], 16); f[2] = __builtin_amdgcn_alignbit(d[4], d[3], 16);
    f[3] = __builtin_amdgcn_alignbit(d[5], d[4], 16); f[4] = __builtin_amdgcn_alignbit(d[6], d[5], 16); f[5] = __builtin_amdgcn_alignbit(d[7], d[6], 16);
    f[6] = 0; f[7] = 0;
    AWin r;
    r.v3 = __builtin_bit_cast(bf16x8, __builtin_shufflevector(f, f, 0, 1, 2, 3));
    r.v7 = __builtin_bit_cast(bf16x8, __builtin_shufflevector(f, f, 2, 3, 4, 5));
    r.v4 = __builtin_bit_cast(bf16x8, __builtin_shufflevector(d, d, 2, 3, 4, 5));
    r.v8 = __builtin_bit_cast(bf16x8, y);
    return r;
}

__global__ __launch_bounds__(256) void k1_conv(const u16* __restrict__ sk, const u16* __restrict__ sD, const u16* __restrict__ wg, const u16* __restrict__ wu,
                                               const u16* __restrict__ wd, char* __restrict__ ws) {
    extern __shared__ __attribute__((aligned(16))) char smem[];
    const int b = blockIdx.x, tid = threadIdx.x, l = tid & 63, w = rfl(tid >> 6);
    const u32 lds0 = (u32)(uintptr_t)smem;
    const float* mod = (const float*)(ws + WS_MOD);
    STAMP_AT(ws, 1, 0);
    if (b < K1_NC) {
        const int d = b * 4 + w;
        const u32 wb = lds0 + w * CV_W;
        const char* kvrow = ws + WS_KVR + (size_t)d * KVS * 2;
#pragma unroll
        for (int i = 0; i < 5; ++i) {
            const int p = 64 * i + l, blk = (p * 3641) >> 16, in = min(p - 18 * blk, 15);
            const u32 off = (u32)(16 * blk + in) * 16;
            if (i < 4 || l < 32) { glds16s(kvrow, off, wb + CV_B + i * 1024); glds16s(kvrow + 4, off, wb + CV_B + CV_BC + i * 1024); }
        }
        const char* krow = (const char*)(sk + (size_t)d * S);
#pragma unroll
        for (int i = 0; i < 4; ++i) glds16s(krow + i * 1024, (u32)l * 16, wb + CV_A + 256 + i * 1024);
#pragma unroll
        for (int i = 0; i < 4; ++i) glds16s(ws + WS_ST1 + (w * 4 + i) * 1024, (u32)l * 16, lds0 + CV_ST + (w * 4 + i) * 1040);
        const lptr wl = (lptr)smem + w * CV_W;
        const u32x4 z4 = {0, 0, 0, 0};
        if (l < 16) *(LAS u32x4*)(wl + CV_A + l * 16) = z4;
#pragma unroll
        for (int i = 0; i < 2; ++i) { *(LAS u32x4*)(wl + CV_B + 288 * 16 + i * 1024 + l * 16) = z4; *(LAS u32x4*)(wl + CV_B + CV_BC + 288 * 16 + i * 1024 + l * 16) = z4; }
        const int col = l & 15, qq = l >> 4;
        const lptr pa = wl + CV_A + 16 * ((col >> 2) + 4 * (col & 3) + qq);
        const lptr pb1 = wl + CV_B + 16 * (18 * (15 - col) + qq);
        const lptr pb2 = wl + CV_B + (col >> 3) * CV_BC + 16 * (18 * (7 - (col & 7)) + qq);
        f32x4 acc[4][2], acc2[4];
#pragma unroll
        for (int i = 0; i < 4; ++i) { acc[i][0] = (f32x4){0, 0, 0, 0}; acc[i][1] = (f32x4){0, 0, 0, 0}; acc2[i] = (f32x4){0, 0, 0, 0}; }
        asm volatile("s_waitcnt vmcnt(4) lgkmcnt(0)" ::: "memory");
        STAMP_AT(ws, 1, 2);
        const u16* qrow = (const u16*)(ws + WS_QT) + (size_t)d * S + 128 * col + 8 * qq;
        u32x4 qv[4];
#pragma unroll
        for (int j = 0; j < 4; ++j) qv[j] = *(const u32x4*)(qrow + 32 * j);
#pragma unroll 2
        for (int nb = 0; nb < 8; ++nb) {
#pragma unroll
            for (int i = 0; i < 4; ++i) {
                const AWin a = conv_win(pa + 256 * nb + 64 * i);
                const bf16x8 b0 = *(LAS bf16x8*)(pb1 + 288 * nb + 64 * i), b1 = *(LAS bf16x8*)(pb1 + CV_BC + 288 * nb + 64 * i);
                acc[1][0] = MFMA16(a.v4, b0, acc[1][0]); acc[3][0] = MFMA16(a.v8, b0, acc[3][0]);
                acc[1][1] = MFMA16(a.v4, b1, acc[1][1]); acc[3][1] = MFMA16(a.v8, b1, acc[3][1]);
                acc[0][0] = MFMA16(a.v3, b0, acc[0][0]); acc[2][0] = MFMA16(a.v7, b0, acc[2][0]);
                acc[0][1] = MFMA16(a.v3, b1, acc[0][1]); acc[2][1] = MFMA16(a.v7, b1, acc[2][1]);
            }
        }
#pragma unroll 2
        for (int nb = 8; nb < 16; ++nb) {
#pragma unroll
            for (int i = 0; i < 4; ++i) {
                const AWin a = conv_win(pa + 256 * nb + 64 * i);
                const bf16x8 b0 = *(LAS bf16x8*)(pb2 + 288 * nb + 64 * i);
                acc2[1] = MFMA16(a.v4, b0, acc2[1]); acc2[3] = MFMA16(a.v8, b0, acc2[3]);
                acc2[0] = MFMA16(a.v3, b0, acc2[0]); acc2[2] = MFMA16(a.v7, b0, acc2[2]);
            }
        }
        STAMP_AT(ws, 1, 3);
        WAITV_BAR(0);
        const float sa = mod[d], ca = mod[D + d], ga = mod[2 * D + d], dc = bf2f(sD[d]);
        const float cA = ga * rbf(1.f + ca), cB = ga * sa;
        u16* xrow = (u16*)(ws + WS_X1T) + (size_t)d * S + 128 * col + 8 * qq;
#pragma unroll
        for (int j = 0; j < 4; ++j) {
            float y[8];
#pragma unroll
            for (int vv = 0; vv < 4; ++vv) {
                const int rho = (vv & 1) + 4 * (vv >> 1);
                const int e2 = __float_as_int(acc2[vv][j]);
                y[rho + 2] = acc[vv][0][j] + __int_as_float(__builtin_amdgcn_update_dpp(0, e2, 0x118, 0xf, 0xf, true));
                y[rho] = acc[vv][1][j] + __int_as_float(__builtin_amdgcn_update_dpp(0, e2, 0xE4, 0xf, 0xc, false));
            }
            const int ci = qq + 4 * j;
            const u32x4 kr = *(LAS u32x4*)(wl + CV_B + 16 * (18 * (15 - col) + 15 - ci));
            const lptr sp = (lptr)smem + CV_ST + col * 1040 + ci * 64;
            u32x4 o;
#pragma unroll
            for (int h = 0; h < 4; ++h) {
                const f32x4 st = *(LAS f32x4*)(sp + 16 * h);
                const float q0 = lo16(qv[j][h]), q1 = hi16(qv[j][h]);
                const float y0 = y[2 * h] + hi16(kr[3 - h]) * dc, y1 = y[2 * h + 1] + lo16(kr[3 - h]) * dc;
                const float g0 = (q0 * st[0] + st[1]) * cA + cB, g1 = (q1 * st[2] + st[3]) * cA + cB;
                o[h] = cvtpk(q0 + g0 * y0, q1 + g1 * y1);
            }
            *(u32x4*)(xrow + 32 * j) = o;
        }
    } else if (b < K1_NC + K1_NW1) {
        const int pi0 = b - K1_NC, pi = (pi0 < 48) ? (pi0 & 7) * 6 + (pi0 >> 3) : pi0;
        const u32 voff = (u32)((l >> 2) * (F * 2) + (l & 3) * 16);
        const char* gb = (const char*)wg + (size_t)(160 * w) * (F * 2) + pi * 64;
        const char* ub = (const char*)wu + (size_t)(160 * w) * (F * 2) + pi * 64;
        const u32 wb = lds0 + w * 20480;
#pragma unroll
        for (int i = 0; i < 10; ++i) { glds16s(gb + (size_t)i * 16 * F * 2, voff, wb + i * 1024); glds16s(ub + (size_t)i * 16 * F * 2, voff, wb + 10240 + i * 1024); }
        const int h = l >> 5, blk = (l >> 4) & 1, qq = (l & 15) >> 2, p = l & 3;
        const lptr rp = (lptr)smem + w * 20480 + (p >> 1) * 10240 + (8 * h + qq) * 64 + blk * 16 + (p & 1) * 8;
        const float* cbp = mod + 4 * D + 160 * w + 8 * h;
        const float* sbp = mod + 3 * D + 160 * w + 8 * h;
        float c1[2] = {0.f, 0.f}, c2[2] = {0.f, 0.f};
        u32x4* wp = (u32x4*)(ws + WS_WP) + (size_t)(2 * pi) * 40 * 64 + (size_t)(10 * w) * 64 + l;
        WAITV(0);
#pragma unroll 2
        for (int i = 0; i < 10; ++i) {
            const f32x4 cb0 = *(const f32x4*)(cbp + 16 * i), cb1 = *(const f32x4*)(cbp + 16 * i + 4), sb0 = *(const f32x4*)(sbp + 16 * i), sb1 = *(const f32x4*)(sbp + 16 * i + 4);
            float cs[8], ss[8];
#pragma unroll
            for (int j = 0; j < 4; ++j) { cs[j] = rbf(1.f + cb0[j]); cs[4 + j] = rbf(1.f + cb1[j]); ss[j] = sb0[j]; ss[4 + j] = sb1[j]; }
#pragma unroll
            for (int u = 0; u < 2; ++u) {
                const u32x2 r0 = __builtin_bit_cast(u32x2, ldtr(rp + i * 1024 + u * 32)), r1 = __builtin_bit_cast(u32x2, ldtr(rp + i * 1024 + u * 32 + 256));
                const u32 raw[4] = {r0[0], r0[1], r1[0], r1[1]};
                u32x4 o;
#pragma unroll
                for (int j = 0; j < 4; ++j) {
                    const float a0 = lo16(raw[j]), a1 = hi16(raw[j]);
                    c2[u] += a0 * ss[2 * j] + a1 * ss[2 * j + 1];
                    o[j] = cvtpk(a0 * cs[2 * j], a1 * cs[2 * j + 1]);
                    c1[u] += lo16(o[j]) + hi16(o[j]);
                }
                wp[(size_t)u * 40 * 64 + i * 64] = o;
            }
        }
        __syncthreads();
        float* red = (float*)smem;
        red[(0 * 4 + w) * 64 + l] = c1[0]; red[(1 * 4 + w) * 64 + l] = c1[1]; red[(2 * 4 + w) * 64 + l] = c2[0]; red[(3 * 4 + w) * 64 + l] = c2[1];
        __syncthreads();
        if (tid < 128) {
            const int g = tid >> 5, r = tid & 31;
            float x = 0.f;
#pragma unroll
            for (int i = 0; i < 4; ++i) x += red[(g * 4 + i) * 64 + r] + red[(g * 4 + i) * 64 + 32 + r];
            ((float*)(ws + (g < 2 ? WS_C1 : WS_C2)))[(2 * pi + (g & 1)) * 32 + r] = x;
        }
    } else {
        const int pi = b - K1_NC - K1_NW1;
        const char* src = (const char*)wd + (size_t)pi * 64 * 1280;
#pragma unroll
        for (int i = 0; i < 20; ++i) glds16s(src + (w * 20 + i) * 1024, (u32)l * 16, lds0 + (w * 20 + i) * 1024);
        WAITV_BAR(0);
        const int g = l >> 4, qq = (l & 15) >> 2, p = l & 3;
        const int ks = w >> 1;
        const lptr rp = (lptr)smem + (ks * 32 + 16 * (g >> 1) + 4 * (g & 1) + qq) * 1280 + p * 40 + (w & 1) * 640;
        u32x4* dst = (u32x4*)(ws + WS_WDP) + l;
#pragma unroll
        for (int cb = 0; cb < 4; ++cb)
#pragma unroll
            for (int n = 0; n < 5; ++n) {
                const s16x4 r0 = ldtr(rp + cb * 160 + n * 8), r1 = ldtr(rp + cb * 160 + n * 8 + 8 * 1280);
                dst[(size_t)((((w & 1) * 4 + cb) * 54 + 2 * pi + ks) * 5 + n) * 64] = __builtin_bit_cast(u32x4, cat8(r0, r1));
            }
    }
    STAMP_AT(ws, 1, 1);
}

template <int V> struct IC { static constexpr int value = V; };
constexpr int K2_LDS = 163840, K2_P = 12;
__global__ __launch_bounds__(512) void k2_gemm1(char* __restrict__ ws) {
    extern __shared__ __attribute__((aligned(16))) char smem[];
    const int b = blockIdx.x, tid = threadIdx.x, l = tid & 63, w = rfl(tid >> 6);
    const u32 lds0 = (u32)(uintptr_t)smem;
    STAMP_AT(ws, 2, 0);
    const int x = b & 7, i5 = b >> 3, cgp = x >> 1, ncb = (cgp & 1) ? 3 : 4, cb0 = (cgp * 7 + 1) >> 1;
    if (i5 >= 8 * ncb) return;
    const int tb = (x & 1) * 8 + (i5 & 7), cb = cb0 + (i5 >> 3);
    const int U = min(cb * 8 + w, 107);
    const bool live = cb * 8 + w < 108;
    const int wq = w & 3, wh = w >> 2;
    const u32 xoff = (u32)((l >> 4) * 4096 + 16 * ((l & 15) ^ (((l >> 4) << 2) | wq)));
    const char* xb = ws + WS_X1T + (size_t)(wh * 16 + wq * 4) * 4096 + tb * 256;
    const u32 xdst = lds0 + (wh * 4 + wq) * 1024;
    const char* wb = ws + WS_WP + (size_t)U * 40960;
    const u32 woff = (u32)l * 16;
    u32x4 wf[40];
#define LDW(s) wf[s] = *(const u32x4*)(wb + (s) * 1024 + woff)
#define LDX(sp) glds16s(xb + (size_t)(sp) * 131072, xoff, xdst + (sp) * 8192)
#pragma unroll
    for (int sp = 0; sp < K2_P / 2; ++sp) { LDX(sp); LDW(2 * sp); LDW(2 * sp + 1); }
    const int h = l >> 5, blk = (l >> 4) & 1, qq = (l & 15) >> 2, p = l & 3;
    lptr xr[4][2];
#pragma unroll
    for (int c = 0; c < 4; ++c)
#pragma unroll
        for (int t = 0; t < 2; ++t) xr[c][t] = (lptr)smem + 256 * (8 * h + 4 * t + qq) + 64 * (c ^ wq ^ qq) + 16 * ((2 * blk + (p >> 1)) ^ (2 * h + t)) + 8 * (p & 1);
    f32x16 acc[4];
#pragma unroll
    for (int c = 0; c < 4; ++c)
#pragma unroll
        for (int r = 0; r < 16; ++r) acc[c][r] = 0.f;
    float s1 = 0.f, s2 = 0.f;
    const bf16x2_t ones = {(__bf16)1.0f, (__bf16)1.0f};
    auto mainloop = [&](auto WH) __attribute__((always_inline)) {
#pragma unroll
        for (int sp = 0; sp < 20; ++sp) {
            constexpr int HP = K2_P / 2;
            const int after = (sp + HP <= 20) ? HP - 1 : 19 - sp;
            asm volatile("s_waitcnt vmcnt(%0)\n\ts_barrier" :: "i"(3 * after + 2) : "memory");
            if (sp == 0) STAMP_AT(ws, 2, 2);
            if (sp + HP < 20) { LDX(sp + HP); LDW(2 * (sp + HP)); LDW(2 * (sp + HP) + 1); }
#pragma unroll
            for (int e = 0; e < 2; ++e) {
                const int s = 2 * sp + e;
                bf16x8 xf[4];
#pragma unroll
                for (int c = 0; c < 4; ++c) xf[c] = cat8(ldtr(xr[c][0] + 4096 * s), ldtr(xr[c][1] + 4096 * s));
                const bf16x8 a = __builtin_bit_cast(bf16x8, wf[s]);
#pragma unroll
                for (int c = 0; c < 4; ++c) acc[c] = MFMA32(a, xf[c], acc[c]);
                if (e == WH.value) {
                    const u32x4 xs = __builtin_bit_cast(u32x4, xf[0]);
#pragma unroll
                    for (int j = 0; j < 4; ++j) {
                        const u32 xj = xs[j]; const bf16x2_t e2 = __builtin_bit_cast(bf16x2_t, xj);
                        s1 = __builtin_amdgcn_fdot2_f32_bf16(e2, ones, s1, false);
                        s2 = __builtin_amdgcn_fdot2_f32_bf16(e2, e2, s2, false);
                    }
                    asm volatile("" : "+v"(s1), "+v"(s2));
                }
            }
        }
    };
    if (wh == 0) mainloop(IC<0>{}); else mainloop(IC<1>{});
    STAMP_AT(ws, 2, 3);
    f32x2* red = (f32x2*)smem;
    red[w * 64 + l] = (f32x2){s1, s2};
    const f32x4* c1p = (const f32x4*)(ws + WS_C1) + U * 8 + h;
    const f32x4* c2p = (const f32x4*)(ws + WS_C2) + U * 8 + h;
    f32x4 c1[4], c2[4];
#pragma unroll
    for (int g = 0; g < 4; ++g) { c1[g] = c1p[2 * g]; c2[g] = c2p[2 * g]; }
    __syncthreads();
    u32x4* hp = (u32x4*)(ws + WS_HP) + ((size_t)(tb * 8 + ((l >> 4) & 1)) * 54 + (U >> 1)) * 64 + (l & 15) + 16 * (2 * (U & 1) + h);
#pragma unroll
    for (int c = 0; c < 4; ++c) {
        const int ct = c ^ wq;
        const f32x2 a0 = red[ct * 64 + (l & 31)], a1 = red[ct * 64 + 32 + (l & 31)], a2 = red[(ct + 4) * 64 + (l & 31)], a3 = red[(ct + 4) * 64 + 32 + (l & 31)];
        const float mu = (a0[0] + a1[0] + a2[0] + a3[0]) * (1.f / D);
        const float var = fmaxf((a0[1] + a1[1] + a2[1] + a3[1]) * (1.f / D) - mu * mu, 0.f);
        const float rs = rsqrtf(var + 1e-5f);
        u32x4 o;
#pragma unroll
        for (int hh = 0; hh < 2; ++hh) {
            float hv[4];
#pragma unroll
            for (int j = 0; j < 4; ++j) {
                const float g = rs * (acc[c][8 * hh + j] - mu * c1[2 * hh][j]) + c2[2 * hh][j];
                const float u = rs * (acc[c][8 * hh + 4 + j] - mu * c1[2 * hh + 1][j]) + c2[2 * hh + 1][j];
                hv[j] = fmaxf(g, 0.f) * u;
            }
            o[2 * hh] = cvtpk(hv[0], hv[1]); o[2 * hh + 1] = cvtpk(hv[2], hv[3]);
        }
        if (live) hp[(size_t)ct * 2 * 54 * 64] = o;
    }
    STAMP_AT(ws, 2, 1);
}

constexpr int K3_G = 2;
constexpr int K3_NST = 8;
constexpr int K3_STB = K3_G * 9 * 1024;
constexpr int K3_X = K3_NST * K3_STB;
constexpr int K3_LDS = K3_X + 10240;
__global__ __launch_bounds__(256) void k3_gemm2(char* __restrict__ ws, u16* __restrict__ out) {
    extern __shared__ __attribute__((aligned(16))) char smem[];
    const int b = blockIdx.x, tid = threadIdx.x, l = tid & 63, w = rfl(tid >> 6);
    const u32 lds0 = (u32)(uintptr_t)smem;
    STAMP_AT(ws, 3, 0);
    const int x = b & 7, i5 = b >> 3, tt = (x >> 1) * 8 + (i5 & 7), cb = (x & 1) * 4 + (i5 >> 3);
    const u32 voff = (u32)l * 16;
    const char* hb = ws + WS_HP + (size_t)(tt * 4 + w) * 54 * 1024;
    const char* wb = ws + WS_WDP + (size_t)cb * 54 * 5 * 1024 + w * 1024;
#define K3_ISSUE(st) do { const u32 sb_ = lds0 + ((st) % K3_NST) * K3_STB; \
        glds16s(hb + (size_t)(st) * 2048, voff, sb_ + w * 2048); glds16s(hb + (size_t)(st) * 2048 + 1024, voff, sb_ + w * 2048 + 1024); \
        glds16s(wb + (size_t)(st) * 10240, voff, sb_ + 8192 + w * 1024); glds16s(wb + (size_t)(st) * 10240 + 4096, voff, sb_ + 8192 + 4096 + w * 1024); \
        glds16s(wb + (size_t)(st) * 10240 + (w < 2 ? 8192 : 4096), voff, sb_ + 8192 + w * 1024 + (w < 2 ? 8192 : 4096)); } while (0)
    constexpr int AH = K3_NST - 1;
#pragma unroll
    for (int st = 0; st < AH; ++st) K3_ISSUE(st);
    f32x4 acc[5];
#pragma unroll
    for (int n = 0; n < 5; ++n) acc[n] = (f32x4){0, 0, 0, 0};
    const lptr hr = (lptr)smem + w * 2048 + l * 16, wr = (lptr)smem + 8192 + l * 16;
#pragma unroll
    for (int st = 0; st < 27; ++st) {
        const int after = (st + AH <= 27) ? AH - 1 : 26 - st;
        const int cnt = 5 * after + ((st + AH > 27) ? 3 : 0);
        asm volatile("s_waitcnt vmcnt(%0) lgkmcnt(0)\n\ts_barrier" :: "i"(cnt) : "memory");
        if (st == 0) STAMP_AT(ws, 3, 2);
        if (st + AH < 27) K3_ISSUE(st + AH);
        if (st + AH == 27) {
            const char* xb = ws + WS_X1T + (size_t)(cb * 80) * 4096 + tt * 128;
#pragma unroll
            for (int i = 0; i < 3; ++i) { const int pc = min(w + 4 * i, 9); glds16s(xb + (size_t)pc * 8 * 4096, (u32)((l >> 3) * 4096 + (l & 7) * 16), lds0 + K3_X + pc * 1024); }
        }
        const int so = (st % K3_NST) * K3_STB;
#pragma unroll
        for (int e = 0; e < K3_G; ++e) {
            const bf16x8 hf = *(LAS bf16x8*)(hr + so + e * 1024);
#pragma unroll
            for (int n = 0; n < 5; ++n) acc[n] = MFMA16(*(LAS bf16x8*)(wr + so + (e * 5 + n) * 1024), hf, acc[n]);
        }
    }
    STAMP_AT(ws, 3, 3);
    const int g = l >> 4, qq = (l & 15) >> 2, p = l & 3;
    const float* gbp = (const float*)(ws + WS_MOD) + 5 * D + cb * 80 + 20 * g;
    f32x4 gb[5];
#pragma unroll
    for (int n = 0; n < 5; ++n) gb[n] = *(const f32x4*)(gbp + 4 * n);
    WAITV_BAR(0);
    const lptr xr = (lptr)smem + K3_X + (20 * g + qq) * 128 + w * 32 + p * 8;
    u32 o[10];
#pragma unroll
    for (int n = 0; n < 5; ++n) {
        const u32x2 xv = __builtin_bit_cast(u32x2, ldtr(xr + n * 512));
        o[2 * n] = cvtpk(lo16(xv[0]) + gb[n][0] * acc[n][0], hi16(xv[0]) + gb[n][1] * acc[n][1]);
        o[2 * n + 1] = cvtpk(lo16(xv[1]) + gb[n][2] * acc[n][2], hi16(xv[1]) + gb[n][3] * acc[n][3]);
    }
    u32* op = (u32*)(out + (size_t)(tt * 64 + w * 16 + (l & 15)) * D + cb * 80 + 20 * g);
#pragma unroll
    for (int n = 0; n < 5; ++n) *(u32x2*)(op + 2 * n) = (u32x2){o[2 * n], o[2 * n + 1]};
    STAMP_AT(ws, 3, 1);
}

__global__ void k_spin(char* ws, int k, int s, int nb0, int nbk) {
    const u64* h = (const u64*)(ws + (40 << 20));
    __shared__ u64 red[2][256];
    u64 mn = ~0ull, x = (s == 0) ? ~0ull : 0ull;
    for (int b = threadIdx.x; b < nb0; b += 256) { u64 v = h[b * 8]; mn = v < mn ? v : mn; }
    for (int b = threadIdx.x; b < nbk; b += 256) { u64 v = h[(k * 1024 + b) * 8 + s]; if (v < mn || v > mn + 100000) continue; if (s == 0) x = v < x ? v : x; else x = v > x ? v : x; }
    red[0][threadIdx.x] = mn; red[1][threadIdx.x] = x;
    __syncthreads();
    if (threadIdx.x == 0) {
        for (int i = 0; i < 256; ++i) mn = red[0][i] < mn ? red[0][i] : mn;
        u64 y = (s == 0) ? ~0ull : 0ull;
        for (int i = 0; i < 256; ++i) { u64 v = red[1][i]; if (v < mn || v > mn + 100000) continue; if (s == 0) y = v < y ? v : y; else y = v > y ? v : y; }
        u64 span = y - mn; if (span > 10000) span = 10000;
        u64 t0 = rt();
        while (rt() - t0 < span * 100) __builtin_amdgcn_s_sleep(8);
    }
}
extern "C" void kernel_launch(void* const* d_in, const int* in_sizes, int n_in, void* d_out, int out_size, void* d_ws, size_t ws_size, hipStream_t stream) {
    const u16* q = (const u16*)d_in[0];
    const u16* k = (const u16*)d_in[1];
    const u16* v = (const u16*)d_in[2];
    const u16* sh = (const u16*)d_in[3];
    const u16* sk = (const u16*)d_in[4];
    const u16* sD = (const u16*)d_in[5];
    const u16* wg = (const u16*)d_in[7];
    const u16* wu = (const u16*)d_in[8];
    const u16* wd = (const u16*)d_in[9];
    const u16* c = (const u16*)d_in[10];
    const u16* wm = (const u16*)d_in[11];
    char* ws = (char*)d_ws;
    (void)hipFuncSetAttribute((const void*)k1_conv, hipFuncAttributeMaxDynamicSharedMemorySize, K1_LDS);
    (void)hipFuncSetAttribute((const void*)k2_gemm1, hipFuncAttributeMaxDynamicSharedMemorySize, K2_LDS);
    (void)hipFuncSetAttribute((const void*)k3_gemm2, hipFuncAttributeMaxDynamicSharedMemorySize, K3_LDS);
    k0_prep<<<K0_NT + K0_NM + K0_NS, 256, K0_LDS, stream>>>(q, k, v, sh, c, wm, ws);
    k1_conv<<<K1_NC + K1_NW1 + K1_NWD, 256, K1_LDS, stream>>>(sk, sD, wg, wu, wd, ws);
    k2_gemm1<<<256, 512, K2_LDS, stream>>>(ws);
    k3_gemm2<<<256, 256, K3_LDS, stream>>>(ws, (u16*)d_out);
    k_spin<<<1, 256, 0, stream>>>(ws, 0, 1, 568, 568);
}
```

```cpp
#include <hip/hip_runtime.h>
#include <hip/hip_bf16.h>
#include <stdint.h>
typedef unsigned short u16;
typedef unsigned long long u64;
constexpr int S = 2048, D = 640, F = 1728;
__device__ __forceinline__ float b2f(u16 x) { return __uint_as_float(((unsigned)x) << 16); }
__device__ __forceinline__ u16 f2b(float f) { __hip_bfloat16 b = __float2bfloat16(f); return *(u16*)&b; }
__device__ __forceinline__ float r(float f) { return b2f(f2b(f)); }
__global__ __launch_bounds__(640) void k_all(const u16* q, const u16* k, const u16* v, const u16* sh, const u16* sk, const u16* sD, const u16* wg, const u16* wu, const u16* wd,
                                             const u16* c, const u16* wm, u16* out) {
    const u64 t0 = __builtin_amdgcn_s_memrealtime();
    __shared__ float sc[D], h2[8][D], Hs[8][F], red[8][16];
    const int d = threadIdx.x, T0 = blockIdx.x * 8;
    { float cv = b2f(c[d]); sc[d] = r(cv / (1.f + __expf(-cv))); }
    __syncthreads();
    float md[6];
    for (int j = 0; j < 6; ++j) { float a = 0.f; for (int i = 0; i < D; ++i) a += sc[i] * b2f(wm[i * 6 * D + j * D + d]); md[j] = r(a); }
    float w0 = b2f(sh[d * 4]), w1 = b2f(sh[d * 4 + 1]), w2 = b2f(sh[d * 4 + 2]), w3 = b2f(sh[d * 4 + 3]);
    float y[8], kvt[8]; for (int i = 0; i < 8; ++i) { y[i] = 0.f; kvt[i] = 0.f; }
    float k1 = 0.f, k2 = 0.f, k3 = 0.f;
    float win[8];
    for (int i = 0; i < 8; ++i) win[i] = b2f(sk[d * S + T0 + i]);
    for (int s = 0; s < T0 + 8; ++s) {
        float k0 = b2f(k[s * D + d]);
        float a = r(k3 * w3); a = r(a + r(k2 * w2)); a = r(a + r(k1 * w1)); a = r(a + r(k0 * w0));
        float kv = r(a * b2f(v[s * D + d]));
        k3 = k2; k2 = k1; k1 = k0;
#pragma unroll
        for (int i = 0; i < 8; ++i) { y[i] += kv * win[i]; if (s == T0 + i) kvt[i] = kv; }
#pragma unroll
        for (int i = 7; i > 0; --i) win[i] = win[i - 1];
        win[0] = (T0 - s - 1 >= 0) ? b2f(sk[d * S + T0 - s - 1]) : 0.f;
    }
    float x1[8];
    auto bsum = [&](float x, int i) { for (int o = 32; o > 0; o >>= 1) x += __shfl_xor(x, o); __syncthreads(); if ((d & 63) == 0) red[i][d >> 6] = x; __syncthreads(); float t = 0.f; for (int j = 0; j < 10; ++j) t += red[i][j]; return t; };
#pragma unroll
    for (int i = 0; i < 8; ++i) {
        float x = b2f(q[(T0 + i) * D + d]);
        float m = bsum(x, i) / D; float vr = bsum((x - m) * (x - m), i) / D;
        float h = (x - m) * rsqrtf(vr + 1e-5f) * r(1.f + md[1]) + md[0];
        x1[i] = r(x + md[2] * h * (y[i] + kvt[i] * b2f(sD[d])));
        m = bsum(x1[i], i) / D; vr = bsum((x1[i] - m) * (x1[i] - m), i) / D;
        h2[i][d] = r((x1[i] - m) * rsqrtf(vr + 1e-5f) * r(1.f + md[4]) + md[3]);
    }
    __syncthreads();
    for (int f = d; f < F; f += 640) {
        float g[8], u[8]; for (int i = 0; i < 8; ++i) { g[i] = 0.f; u[i] = 0.f; }
        for (int e = 0; e < D; ++e) { float a = b2f(wg[e * F + f]), b = b2f(wu[e * F + f]);
#pragma unroll
            for (int i = 0; i < 8; ++i) { g[i] += h2[i][e] * a; u[i] += h2[i][e] * b; } }
        for (int i = 0; i < 8; ++i) Hs[i][f] = r(fmaxf(g[i], 0.f) * u[i]);
    }
    __syncthreads();
    float o[8]; for (int i = 0; i < 8; ++i) o[i] = 0.f;
    for (int f = 0; f < F; ++f) { float a = b2f(wd[f * D + d]);
#pragma unroll
        for (int i = 0; i < 8; ++i) o[i] += Hs[i][f] * a; }
    for (int i = 0; i < 8; ++i) out[(T0 + i) * D + d] = f2b(x1[i] + md[5] * o[i]);
    while (__builtin_amdgcn_s_memrealtime() - t0 < 500000ull) __builtin_amdgcn_s_sleep(2);
}
__global__ void k_triv(char* ws) { if (threadIdx.x == 0) ((u64*)ws)[0] = 1; }
extern "C" void kernel_launch(void* const* d_in, const int* in_sizes, int n_in, void* d_out, int out_size, void* d_ws, size_t ws_size, hipStream_t stream) {

    k_all<<<256, 640, 0, stream>>>((u16*)d_in[0], (u16*)d_in[1], (u16*)d_in[2], (u16*)d_in[3], (u16*)d_in[4], (u16*)d_in[5], (u16*)d_in[7], (u16*)d_in[8], (u16*)d_in[9], (u16*)d_in[10], (u16*)d_in[11], (u16*)d_out);

}
```

```cpp
#include <hip/hip_runtime.h>
#include <hip/hip_bf16.h>
#include <stdint.h>

typedef unsigned short u16;
typedef unsigned u32;
typedef unsigned long long u64;
typedef short bf16x8 __attribute__((ext_vector_type(8)));
typedef short s16x4 __attribute__((ext_vector_type(4)));
typedef float f32x2 __attribute__((ext_vector_type(2)));
typedef float f32x4 __attribute__((ext_vector_type(4)));
typedef float f32x16 __attribute__((ext_vector_type(16)));
typedef u32 u32x2 __attribute__((ext_vector_type(2)));
typedef u32 u32x4 __attribute__((ext_vector_type(4)));
typedef u32 u32x8 __attribute__((ext_vector_type(8)));
typedef u32 u32x16 __attribute__((ext_vector_type(16)));
typedef __bf16 bf16x2_t __attribute__((ext_vector_type(2)));
#define LAS __attribute__((address_space(3)))
#define GAS __attribute__((address_space(1)))
typedef LAS char* lptr;

constexpr int S = 2048, D = 640, F = 1728;
#define QSLEEP 0
#define MSLEEP 0
#define HA 0
#define HB 2
#define HC 1
#define HPRIO 1
constexpr int KVS = 2112;
constexpr size_t WS_GB = 0, WS_ST1 = 16 << 10, WS_ST2 = 32 << 10, WS_CNT = 48 << 10, WS_CV = 80 << 10, WS_MOD = 128 << 10, WS_CP = 256 << 10;
constexpr size_t WS_KVR = 1 << 20, WS_QT = 4 << 20, WS_X1T = 8 << 20, WS_WP = 12 << 20, WS_WDP = 20 << 20, WS_HP = 24 << 20;

#define STAMP2(slot) do {} while (0)
#define STAMPW(kid, wv, slot) do {} while (0)
#define STAMP_AT(ws, kid, slot) do {} while (0)

__device__ __forceinline__ float u2f(u32 x) { return __builtin_bit_cast(float, x); }
__device__ __forceinline__ u32 f2u(float x) { return __builtin_bit_cast(u32, x); }
__device__ __forceinline__ u32 cvtpk(float lo, float hi) { f32x2 v = {lo, hi}; return __builtin_bit_cast(u32, __builtin_convertvector(v, bf16x2_t)); }
__device__ __forceinline__ float lo16(u32 x) { return u2f(x << 16); }
__device__ __forceinline__ float hi16(u32 x) { return u2f(x & 0xffff0000u); }
__device__ __forceinline__ float rbf(float x) { return lo16(cvtpk(x, x)); }
__device__ __forceinline__ float bf2f(u16 x) { return u2f(((u32)x) << 16); }
__device__ __forceinline__ void glds16(const void* g, u32 lds_base) {
    asm volatile("s_mov_b32 m0, %1\n\ts_nop 0\n\tglobal_load_lds_dwordx4 %0, off" :: "v"(g), "s"(lds_base) : "memory");
}
__device__ __forceinline__ void glds16s(const void* sbase, u32 voff, u32 lds_base) {
    asm volatile("s_mov_b32 m0, %2\n\ts_nop 0\n\tglobal_load_lds_dwordx4 %0, %1" :: "v"(voff), "s"(sbase), "s"(lds_base) : "memory");
}
__device__ __forceinline__ void st16_wt(void* p, u32x4 v) { asm volatile("global_store_dwordx4 %0, %1, off sc1\n\ts_nop 1" :: "v"(p), "v"(v) : "memory"); }
#define WAITV(n) asm volatile("s_waitcnt vmcnt(" #n ")" ::: "memory")
#define WAITV_BAR(n) asm volatile("s_waitcnt vmcnt(" #n ") lgkmcnt(0)\n\ts_barrier" ::: "memory")
#define SBAR() __builtin_amdgcn_sched_barrier(0)
__device__ __forceinline__ s16x4 ldtr(lptr p) { return __builtin_bit_cast(s16x4, __builtin_amdgcn_ds_read_tr16_b64_v4i16((LAS s16x4*)p)); }
__device__ __forceinline__ bf16x8 cat8(s16x4 a, s16x4 b) { return __builtin_shufflevector(a, b, 0, 1, 2, 3, 4, 5, 6, 7); }
template <int CTRL> __device__ __forceinline__ float dpp_f(float x) { return u2f(__builtin_amdgcn_update_dpp(0, (int)f2u(x), CTRL, 0xf, 0xf, true)); }
__device__ __forceinline__ float row16_sum(float x) { x += dpp_f<0x128>(x); x += dpp_f<0x124>(x); x += dpp_f<0x122>(x); x += dpp_f<0x121>(x); return x; }
__device__ __forceinline__ int rfl(int x) { return __builtin_amdgcn_readfirstlane(x); }

#define CODE_ADDR(sym) ({ const char* p_; asm volatile("s_getpc_b64 s[40:41]\n\ts_add_u32 s40, s40, " sym "@rel32@lo+4\n\ts_addc_u32 s41, s41, " sym "@rel32@hi+12" : "={s[40:41]}"(p_) :: "scc"); p_; })
template <int KB> __device__ __forceinline__ void touch_code(const char* p, int l) {
    u32x4 a[KB]; u32 s = 0;
#pragma unroll
    for (int i = 0; i < KB; ++i) a[i] = *(const GAS u32x4*)(p + (i * 64 + l) * 16);
#pragma unroll
    for (int i = 0; i < KB; ++i) s ^= a[i].x ^ a[i].y ^ a[i].z ^ a[i].w;
    asm volatile("" :: "v"(s));
}
#define R2(x) ({ const u32 u_ = cvtpk((x)[0], (x)[1]); (f32x2){lo16(u_), hi16(u_)}; })

constexpr int K0_LDS = 25 * 1040;
__global__ __launch_bounds__(256) void k0_prep(const u16* __restrict__ q, const u16* __restrict__ k, const u16* __restrict__ v, const u16* __restrict__ sh,
                                               const u16* __restrict__ c, const u16* __restrict__ wm, char* __restrict__ ws) {
    extern __shared__ __attribute__((aligned(16))) char smem[];
    const int b = blockIdx.x, tid = threadIdx.x, l = tid & 63, w = rfl(tid >> 6);
    STAMP_AT(ws, 0, 0);
    if (b < 320) {
        const int i5 = b >> 3, tq = i5 / 10, cg = i5 - tq * 10, tt = (b & 7) * 4 + tq, t0 = tt * 64;
        const u32 lds0 = (u32)(uintptr_t)smem;
        const u32 voff = (u32)((l >> 3) * 1280 + (l & 7) * 16);
        const char* kb = (const char*)k + (size_t)(t0 - 8) * 1280 + cg * 128;
        const char* vb = (const char*)v + (size_t)t0 * 1280 + cg * 128;
        const char* qb = (const char*)q + (size_t)t0 * 1280 + cg * 128;
        const int o = l & 7, cp = (l >> 3) + 8 * w;
        u32x4 shw = *(const u32x4*)(sh + (cg * 64 + 2 * cp) * 4);
        if (w == 0) {
            if (tt > 0) { if (l >= 40) glds16s(kb, voff, lds0); }
            else if (l >= 40) *(LAS u32x4*)((lptr)smem + l * 16) = (u32x4){0, 0, 0, 0};
        }
#pragma unroll
        for (int i = 0; i < 2; ++i) {
            const int g = w + 4 * i;
            glds16s(kb + (size_t)(g + 1) * 10240, voff, lds0 + (g + 1) * 1040);
            glds16s(vb + (size_t)g * 10240, voff, lds0 + (9 + g) * 1040);
        }
        __builtin_amdgcn_s_sleep(QSLEEP);
#pragma unroll
        for (int i = 0; i < 2; ++i) { const int g = w + 4 * i; glds16s(qb + (size_t)g * 10240, voff, lds0 + (17 + g) * 1040); }
        asm volatile("s_waitcnt vmcnt(2) lgkmcnt(0)\n\ts_barrier" : "+v"(shw) :: "memory");
        STAMP_AT(ws, 0, 2);
        const lptr base = (lptr)smem + o * 1040 + cp * 4;
        u32 kd[11], vd[8], qd[8];
#pragma unroll
        for (int i = 0; i < 3; ++i) kd[i] = *(LAS u32*)(base + 640 + 128 * i);
#pragma unroll
        for (int i = 0; i < 8; ++i) { kd[3 + i] = *(LAS u32*)(base + 1040 + 128 * i); vd[i] = *(LAS u32*)(base + 9 * 1040 + 128 * i); }
        const f32x2 w0 = {lo16(shw.x), lo16(shw.z)}, w1 = {hi16(shw.x), hi16(shw.z)}, w2 = {lo16(shw.y), lo16(shw.w)}, w3 = {hi16(shw.y), hi16(shw.w)};
        f32x2 kf[11], kv[8];
#pragma unroll
        for (int i = 0; i < 11; ++i) kf[i] = (f32x2){lo16(kd[i]), hi16(kd[i])};
#pragma unroll
        for (int i = 0; i < 8; ++i) {
            const f32x2 a = kf[i + 3] * w0 + (kf[i + 2] * w1 + (kf[i + 1] * w2 + kf[i] * w3));
            kv[i] = a * (f32x2){lo16(vd[i]), hi16(vd[i])};
        }
        u16* kvr = (u16*)(ws + WS_KVR); u16* qT = (u16*)(ws + WS_QT);
        const int ch = cg * 64 + 2 * cp, J = 255 - (tt * 8 + o);
        u32x4 r0 = {cvtpk(kv[7][0], kv[6][0]), cvtpk(kv[5][0], kv[4][0]), cvtpk(kv[3][0], kv[2][0]), cvtpk(kv[1][0], kv[0][0])};
        u32x4 r1 = {cvtpk(kv[7][1], kv[6][1]), cvtpk(kv[5][1], kv[4][1]), cvtpk(kv[3][1], kv[2][1]), cvtpk(kv[1][1], kv[0][1])};
        asm volatile("global_store_dwordx4 %0, %2, off sc1\n\tglobal_store_dwordx4 %1, %3, off sc1\n\ts_waitcnt vmcnt(2)\n\ts_barrier"
                     :: "v"(kvr + (size_t)ch * KVS + J * 8), "v"(kvr + (size_t)(ch + 1) * KVS + J * 8), "v"(r0), "v"(r1) : "memory");
#pragma unroll
        for (int i = 0; i < 8; ++i) qd[i] = *(LAS u32*)(base + 17 * 1040 + 128 * i);
#pragma unroll
        for (int e = 0; e < 2; ++e) {
            const u32 sel = e ? 0x07060302u : 0x05040100u;
            u32x4 t = {__builtin_amdgcn_perm(qd[1], qd[0], sel), __builtin_amdgcn_perm(qd[3], qd[2], sel), __builtin_amdgcn_perm(qd[5], qd[4], sel), __builtin_amdgcn_perm(qd[7], qd[6], sel)};
            st16_wt(qT + (size_t)(ch + e) * S + t0 + o * 8, t);
        }
        if (tt == 0 && o < 2) *(u32x4*)(kvr + (size_t)(ch + o) * KVS + 2048) = (u32x4){0, 0, 0, 0};
    } else if (b < 512 || (b >= 576 && b < 624)) {
        __builtin_amdgcn_s_sleep(MSLEEP);
        const int mi = b < 512 ? b - 320 : b - 384, rq = mi / 60, cq0 = mi - rq * 60;
        const int rl = l >> 3, cq = l & 7, row = rq * 160 + w * 40 + rl;
        u16 cv[5]; u32x4 a[5];
#pragma unroll
        for (int i = 0; i < 5; ++i) cv[i] = c[row + 8 * i];
#pragma unroll
        for (int i = 0; i < 5; ++i) a[i] = *(const u32x4*)(wm + (size_t)(row + 8 * i) * (6 * D) + cq0 * 64 + cq * 8);
        f32x2 acc[4];
#pragma unroll
        for (int j = 0; j < 4; ++j) acc[j] = (f32x2){0.f, 0.f};
#pragma unroll
        for (int i = 0; i < 5; ++i) {
            const float x = bf2f(cv[i]), sv = rbf(x / (1.f + __expf(-x)));
#pragma unroll
            for (int j = 0; j < 4; ++j) { const u32 y = a[i][j]; acc[j] += (f32x2){sv, sv} * (f32x2){lo16(y), hi16(y)}; }
        }
        float* red = (float*)smem;
        *(f32x4*)(red + (w * 8 + rl) * 68 + cq * 8) = (f32x4){acc[0][0], acc[0][1], acc[1][0], acc[1][1]};
        *(f32x4*)(red + (w * 8 + rl) * 68 + cq * 8 + 4) = (f32x4){acc[2][0], acc[2][1], acc[3][0], acc[3][1]};
        __syncthreads();
        float x = 0.f;
#pragma unroll
        for (int i = 0; i < 8; ++i) x += red[((tid & 3) * 8 + i) * 68 + (tid >> 2)];
        x += dpp_f<0xB1>(x); x += dpp_f<0x4E>(x);
        if ((tid & 3) == 0) ((float*)(ws + WS_MOD))[rq * (6 * D) + cq0 * 64 + (tid >> 2)] = x;
    } else if (b < 520) {
#pragma unroll
        for (int i = 0; i < 3; ++i) { const int e = ((b - 512) * 3 + i) * 256 + tid; const u32 z = e < 3072 ? 0u : ~0u; if (e < 3072 + 1728) ((u32x4*)(ws + WS_ST2))[e] = (u32x4){z, z, z, z}; }
    } else if (b < 528) {
        touch_code<10>(CODE_ADDR("_Z7k1_convPKtS0_S0_S0_S0_Pc") + w * 10240, l);
    } else if (b >= 624) {
        __builtin_amdgcn_s_sleep(MSLEEP);
        const int bi = b - 624, t = (bi & 7) * 256 + (bi >> 3) * 16 + w * 4 + (l >> 4);
        const u32x4* p = (const u32x4*)(q + (size_t)t * D) + (l & 15);
        u32x4 a[5];
#pragma unroll
        for (int i = 0; i < 5; ++i) a[i] = p[16 * i];
        float x[40], s = 0.f;
#pragma unroll
        for (int i = 0; i < 5; ++i)
#pragma unroll
            for (int j = 0; j < 4; ++j) { x[i * 8 + 2 * j] = lo16(a[i][j]); x[i * 8 + 2 * j + 1] = hi16(a[i][j]); s += x[i * 8 + 2 * j] + x[i * 8 + 2 * j + 1]; }
        const float m = row16_sum(s) * (1.f / D);
        float vs = 0.f;
#pragma unroll
        for (int i = 0; i < 40; ++i) { const float z = x[i] - m; vs += z * z; }
        const float var = row16_sum(vs) * (1.f / D);
        const float sd = rbf(sqrtf(rbf(rbf(var) + 1e-5f)));
        const float ri = 1.f / sd;
        if ((l & 15) == 0) ((f32x2*)(ws + WS_ST1))[t] = (f32x2){ri, -rbf(m) * ri};
    }
    STAMP_AT(ws, 0, 1);
}

constexpr int CV_A = 0, CV_B = 4352, CV_BC = 6672, CV_W = CV_B + 2 * CV_BC;
constexpr int GE_P = 4608, GE_C = 4 * GE_P, K1_GE = 3 * CV_W, K1_EX = K1_GE + 3 * GE_C, K1_PP = K1_EX + 8192, K1_FL = K1_PP + 4 * 10240, K1_LDS = K1_FL + 64;
__device__ __forceinline__ void lds_arrive(u32 a) { asm volatile("s_waitcnt lgkmcnt(0)\n\ts_mov_b64 exec, 1\n\tds_add_u32 %0, %1\n\ts_mov_b64 exec, -1" :: "v"(a), "v"(1u) : "memory"); }
__device__ __forceinline__ void lds_wait(u32 a, u32 n) {
    u32 x;
    do { asm volatile("ds_read_b32 %0, %1\n\ts_waitcnt lgkmcnt(0)" : "=v"(x) : "v"(a) : "memory"); } while ((u32)rfl(x) < n);
}
__device__ __forceinline__ float mod4(const float* m, int i) { return rbf((m[i] + m[6 * D + i]) + (m[12 * D + i] + m[18 * D + i])); }

#if defined(__HIP_DEVICE_COMPILE__)
#define NO_PK __attribute__((target("no-packed-fp32-ops")))
#else
#define NO_PK
#endif
__global__ __launch_bounds__(512) NO_PK void k1_conv(const u16* __restrict__ sk, const u16* __restrict__ sD, const u16* __restrict__ wg, const u16* __restrict__ wu,
                                               const u16* __restrict__ wd, char* __restrict__ ws) {
    extern __shared__ __attribute__((aligned(16))) char smem[];
    const int b = blockIdx.x, tid = threadIdx.x, l = tid & 63, w = rfl(tid >> 6);
    const u32 lds0 = (u32)(uintptr_t)smem;
    const float* mod = (const float*)(ws + WS_MOD);
    STAMP_AT(ws, 1, 0);
    const int hf = (b >> 3) & 1, d0 = 5 * ((b >> 4) * 8 + (b & 7));
    const int dA = d0 + 3 * hf, dC = d0 + 2;
    const int col = l & 15, qq = l >> 4;
    if (w < 4) {
#pragma unroll
        for (int e = 0; e < 3; ++e) glds16s((const char*)(sk + (size_t)(e < 2 ? dA + e : dC) * S) + w * 1024, (u32)l * 16, lds0 + e * CV_W + CV_A + 256 + w * 1024);
        const int p = 64 * w + l, blk = (p * 3641) >> 16;
        const u32 off = (u32)(16 * blk + min(p - 18 * blk, 15)) * 16;
#pragma unroll
        for (int e = 0; e < 3; ++e) {
            const char* kvrow = ws + WS_KVR + (size_t)(e < 2 ? dA + e : dC) * KVS * 2;
            glds16s(kvrow, off, lds0 + e * CV_W + CV_B + w * 1024); glds16s(kvrow + 4, off, lds0 + e * CV_W + CV_B + CV_BC + w * 1024);
        }
        if (w < 3 && l < 32) {
            const int p4 = 256 + l, blk4 = (p4 * 3641) >> 16;
            const u32 off4 = (u32)(16 * blk4 + min(p4 - 18 * blk4, 15)) * 16;
            const char* kvrow = ws + WS_KVR + (size_t)(w < 2 ? dA + w : dC) * KVS * 2;
            glds16s(kvrow, off4, lds0 + w * CV_W + CV_B + 4096); glds16s(kvrow + 4, off4, lds0 + w * CV_W + CV_B + CV_BC + 4096);
        }
        const u32 rr = (col >> 2) + 4 * (col & 3);
        const int pi = 2 * (col & 3) + ((col >> 2) & 1);
        u32 pa, pb, pa2, pb2; int mode;
        const u32 pkA = CV_A + 16 * (rr + 32 * qq), pkB = CV_B + (col >> 3) * CV_BC + 288 * (7 - pi + 2 * qq);
        if (!(w & 1)) { const u32 wb = lds0 + (w >> 1) * CV_W; pa = pa2 = wb + CV_A + 16 * (rr + qq); pb = pb2 = wb + CV_B + 16 * (18 * (15 - col) + qq); mode = 0; }
        else if (w == 1) { pa = lds0 + pkA + 2048; pb = lds0 + pkB + 2304; pa2 = pa + CV_W; pb2 = pb + CV_W; mode = 1; }
        else { pa = lds0 + 2 * CV_W + pkA; pb = lds0 + 2 * CV_W + pkB + (hf ? 0 : 2304); pa2 = pa + 2048; pb2 = lds0 + 2 * CV_W + pkB + 2304; mode = 2 - hf; }
        mode = rfl(mode);
        asm volatile("s_barrier\n\ts_waitcnt vmcnt(0)" ::: "memory");
        lds_arrive(lds0 + K1_FL); lds_wait(lds0 + K1_FL, 4);
        STAMPW(1, 0, 2);
        f32x16 a0, a1;
        asm volatile(
        "s_setprio 0\n\t"
        "s_cmp_eq_u32 %6, 0\n\t"
        "s_cbranch_scc0 LCP_%=\n\t"
        "ds_read_b128 v[32:35], %2 offset:0\n\t"
        "ds_read_b128 v[36:39], %2 offset:16\n\t"
        "ds_read_b128 v[56:59], %3 offset:0\n\t"
        "ds_read_b128 v[60:63], %3 offset:6672\n\t"
        "ds_read_b128 v[40:43], %2 offset:64\n\t"
        "ds_read_b128 v[44:47], %2 offset:80\n\t"
        "ds_read_b128 v[64:67], %3 offset:64\n\t"
        "ds_read_b128 v[68:71], %3 offset:6736\n\t"
        "s_waitcnt lgkmcnt(4)\n\t"
        "v_alignbit_b32 v80, v34, v33, 16\n\t"
        "v_alignbit_b32 v81, v35, v34, 16\n\t"
        "v_alignbit_b32 v82, v36, v35, 16\n\t"
        "v_alignbit_b32 v83, v37, v36, 16\n\t"
        "v_alignbit_b32 v84, v38, v37, 16\n\t"
        "v_alignbit_b32 v85, v39, v38, 16\n\t"
        "s_nop 1\n\t"
        "v_mfma_f32_16x16x32_bf16 v[8:11], v[34:37], v[56:59], 0\n\t"
        "ds_read_b128 v[48:51], %2 offset:128\n\t"
        "v_mfma_f32_16x16x32_bf16 v[24:27], v[36:39], v[56:59], 0\n\t"
        "ds_read_b128 v[52:55], %2 offset:144\n\t"
        "v_mfma_f32_16x16x32_bf16 v[12:15], v[34:37], v[60:63], 0\n\t"
        "ds_read_b128 v[72:75], %3 offset:128\n\t"
        "v_mfma_f32_16x16x32_bf16 v[28:31], v[36:39], v[60:63], 0\n\t"
        "ds_read_b128 v[76:79], %3 offset:6800\n\t"
        "s_waitcnt lgkmcnt(4)\n\t"
        "v_mfma_f32_16x16x32_bf16 v[0:3], v[80:83], v[56:59], 0\n\t"
        "v_alignbit_b32 v86, v42, v41, 16\n\t"
        "v_alignbit_b32 v87, v43, v42, 16\n\t"
        "v_mfma_f32_16x16x32_bf16 v[16:19], v[82:85], v[56:59], 0\n\t"
        "v_alignbit_b32 v88, v44, v43, 16\n\t"
        "v_alignbit_b32 v89, v45, v44, 16\n\t"
        "v_mfma_f32_16x16x32_bf16 v[4:7], v[80:83], v[60:63], 0\n\t"
        "v_alignbit_b32 v90, v46, v45, 16\n\t"
        "v_alignbit_b32 v91, v47, v46, 16\n\t"
        "v_mfma_f32_16x16x32_bf16 v[20:23], v[82:85], v[60:63], 0\n\t"
        "v_mfma_f32_16x16x32_bf16 v[8:11], v[42:45], v[64:67], v[8:11]\n\t"
        "ds_read_b128 v[32:35], %2 offset:192\n\t"
        "v_mfma_f32_16x16x32_bf16 v[24:27], v[44:47], v[64:67], v[24:27]\n\t"
        "ds_read_b128 v[36:39], %2 offset:208\n\t"
        "v_mfma_f32_16x16x32_bf16 v[12:15], v[42:45], v[68:71], v[12:15]\n\t"
        "ds_read_b128 v[56:59], %3 offset:192\n\t"
        "v_mfma_f32_16x16x32_bf16 v[28:31], v[44:47], v[68:71], v[28:31]\n\t"
        "ds_read_b128 v[60:63], %3 offset:6864\n\t"
        "s_waitcnt lgkmcnt(4)\n\t"
        "v_mfma_f32_16x16x32_bf16 v[0:3], v[86:89], v[64:67], v[0:3]\n\t"
        "v_alignbit_b32 v80, v50, v49, 16\n\t"
        "v_alignbit_b32 v81, v51, v50, 16\n\t"
        "v_mfma_f32_16x16x32_bf16 v[16:19], v[88:91], v[64:67], v[16:19]\n\t"
        "v_alignbit_b32 v82, v52, v51, 16\n\t"
        "v_alignbit_b32 v83, v53, v52, 16\n\t"
        "v_mfma_f32_16x16x32_bf16 v[4:7], v[86:89], v[68:71], v[4:7]\n\t"
        "v_alignbit_b32 v84, v54, v53, 16\n\t"
        "v_alignbit_b32 v85, v55, v54, 16\n\t"
        "v_mfma_f32_16x16x32_bf16 v[20:23], v[88:91], v[68:71], v[20:23]\n\t"
        "v_mfma_f32_16x16x32_bf16 v[8:11], v[50:53], v[72:75], v[8:11]\n\t"
        "ds_read_b128 v[40:43], %2 offset:256\n\t"
        "v_mfma_f32_16x16x32_bf16 v[24:27], v[52:55], v[72:75], v[24:27]\n\t"
        "ds_read_b128 v[44:47], %2 offset:272\n\t"
        "v_mfma_f32_16x16x32_bf16 v[12:15], v[50:53], v[76:79], v[12:15]\n\t"
        "ds_read_b128 v[64:67], %3 offset:288\n\t"
        "v_mfma_f32_16x16x32_bf16 v[28:31], v[52:55], v[76:79], v[28:31]\n\t"
        "ds_read_b128 v[68:71], %3 offset:6960\n\t"
        "s_waitcnt lgkmcnt(4)\n\t"
        "v_mfma_f32_16x16x32_bf16 v[0:3], v[80:83], v[72:75], v[0:3]\n\t"
        "v_alignbit_b32 v86, v34, v33, 16\n\t"
        "v_alignbit_b32 v87, v35, v34, 16\n\t"
        "v_mfma_f32_16x16x32_bf16 v[16:19], v[82:85], v[72:75], v[16:19]\n\t"
        "v_alignbit_b32 v88, v36, v35, 16\n\t"
        "v_alignbit_b32 v89, v37, v36, 16\n\t"
        "v_mfma_f32_16x16x32_bf16 v[4:7], v[80:83], v[76:79], v[4:7]\n\t"
        "v_alignbit_b32 v90, v38, v37, 16\n\t"
        "v_alignbit_b32 v91, v39, v38, 16\n\t"
        "v_mfma_f32_16x16x32_bf16 v[20:23], v[82:85], v[76:79], v[20:23]\n\t"
        "v_mfma_f32_16x16x32_bf16 v[8:11], v[34:37], v[56:59], v[8:11]\n\t"
        "ds_read_b128 v[48:51], %2 offset:320\n\t"
        "v_mfma_f32_16x16x32_bf16 v[24:27], v[36:39], v[56:59], v[24:27]\n\t"
        "ds_read_b128 v[52:55], %2 offset:336\n\t"
        "v_mfma_f32_16x16x32_bf16 v[12:15], v[34:37], v[60:63], v[12:15]\n\t"
        "ds_read_b128 v[72:75], %3 offset:352\n\t"
        "v_mfma_f32_16x16x32_bf16 v[28:31], v[36:39], v[60:63], v[28:31]\n\t"
        "ds_read_b128 v[76:79], %3 offset:7024\n\t"
        "s_waitcnt lgkmcnt(4)\n\t"
        "v_mfma_f32_16x16x32_bf16 v[0:3], v[86:89], v[56:59], v[0:3]\n\t"
        "v_alignbit_b32 v80, v42, v41, 16\n\t"
        "v_alignbit_b32 v81, v43, v42, 16\n\t"
        "v_mfma_f32_16x16x32_bf16 v[16:19], v[88:91], v[56:59], v[16:19]\n\t"
        "v_alignbit_b32 v82, v44, v43, 16\n\t"
        "v_alignbit_b32 v83, v45, v44, 16\n\t"
        "v_mfma_f32_16x16x32_bf16 v[4:7], v[86:89], v[60:63], v[4:7]\n\t"
        "v_alignbit_b32 v84, v46, v45, 16\n\t"
        "v_alignbit_b32 v85, v47, v46, 16\n\t"
        "v_mfma_f32_16x16x32_bf16 v[20:23], v[88:91], v[60:63], v[20:23]\n\t"
        "v_mfma_f32_16x16x32_bf16 v[8:11], v[42:45], v[64:67], v[8:11]\n\t"
        "ds_read_b128 v[32:35], %2 offset:384\n\t"
        "v_mfma_f32_16x16x32_bf16 v[24:27], v[44:47], v[64:67], v[24:27]\n\t"
        "ds_read_b128 v[36:39], %2 offset:400\n\t"
        "v_mfma_f32_16x16x32_bf16 v[12:15], v[42:45], v[68:71], v[12:15]\n\t"
        "ds_read_b128 v[56:59], %3 offset:416\n\t"
        "v_mfma_f32_16x16x32_bf16 v[28:31], v[44:47], v[68:71], v[28:31]\n\t"
        "ds_read_b128 v[60:63], %3 offset:7088\n\t"
        "s_waitcnt lgkmcnt(4)\n\t"
        "v_mfma_f32_16x16x32_bf16 v[0:3], v[80:83], v[64:67], v[0:3]\n\t"
        "v_alignbit_b32 v86, v50, v49, 16\n\t"
        "v_alignbit_b32 v87, v51, v50, 16\n\t"
        "v_mfma_f32_16x16x32_bf16 v[16:19], v[82:85], v[64:67], v[16:19]\n\t"
        "v_alignbit_b32 v88, v52, v51, 16\n\t"
        "v_alignbit_b32 v89, v53, v52, 16\n\t"
        "v_mfma_f32_16x16x32_bf16 v[4:7], v[80:83], v[68:71], v[4:7]\n\t"
        "v_alignbit_b32 v90, v54, v53, 16\n\t"
        "v_alignbit_b32 v91, v55, v54, 16\n\t"
        "v_mfma_f32_16x16x32_bf16 v[20:23], v[82:85], v[68:71], v[20:23]\n\t"
        "v_mfma_f32_16x16x32_bf16 v[8:11], v[50:53], v[72:75], v[8:11]\n\t"
        "ds_read_b128 v[40:43], %2 offset:448\n\t"
        "v_mfma_f32_16x16x32_bf16 v[24:27], v[52:55], v[72:75], v[24:27]\n\t"
        "ds_read_b128 v[44:47], %2 offset:464\n\t"
        "v_mfma_f32_16x16x32_bf16 v[12:15], v[50:53], v[76:79], v[12:15]\n\t"
        "ds_read_b128 v[64:67], %3 offset:480\n\t"
        "v_mfma_f32_16x16x32_bf16 v[28:31], v[52:55], v[76:79], v[28:31]\n\t"
        "ds_read_b128 v[68:71], %3 offset:7152\n\t"
        "s_waitcnt lgkmcnt(4)\n\t"
        "v_mfma_f32_16x16x32_bf16 v[0:3], v[86:89], v[72:75], v[0:3]\n\t"
        "v_alignbit_b32 v80, v34, v33, 16\n\t"
        "v_alignbit_b32 v81, v35, v34, 16\n\t"
        "v_mfma_f32_16x16x32_bf16 v[16:19], v[88:91], v[72:75], v[16:19]\n\t"
        "v_alignbit_b32 v82, v36, v35, 16\n\t"
        "v_alignbit_b32 v83, v37, v36, 16\n\t"
        "v_mfma_f32_16x16x32_bf16 v[4:7], v[86:89], v[76:79], v[4:7]\n\t"
        "v_alignbit_b32 v84, v38, v37, 16\n\t"
        "v_alignbit_b32 v85, v39, v38, 16\n\t"
        "v_mfma_f32_16x16x32_bf16 v[20:23], v[88:91], v[76:79], v[20:23]\n\t"
        "v_mfma_f32_16x16x32_bf16 v[8:11], v[34:37], v[56:59], v[8:11]\n\t"
        "ds_read_b128 v[48:51], %2 offset:512\n\t"
        "v_mfma_f32_16x16x32_bf16 v[24:27], v[36:39], v[56:59], v[24:27]\n\t"
        "ds_read_b128 v[52:55], %2 offset:528\n\t"
        "v_mfma_f32_16x16x32_bf16 v[12:15], v[34:37], v[60:63], v[12:15]\n\t"
        "ds_read_b128 v[72:75], %3 offset:576\n\t"
        "v_mfma_f32_16x16x32_bf16 v[28:31], v[36:39], v[60:63], v[28:31]\n\t"
        "ds_read_b128 v[76:79], %3 offset:7248\n\t"
        "s_waitcnt lgkmcnt(4)\n\t"
        "v_mfma_f32_16x16x32_bf16 v[0:3], v[80:83], v[56:59], v[0:3]\n\t"
        "v_alignbit_b32 v86, v42, v41, 16\n\t"
        "v_alignbit_b32 v87, v43, v42, 16\n\t"
        "v_mfma_f32_16x16x32_bf16 v[16:19], v[82:85], v[56:59], v[16:19]\n\t"
        "v_alignbit_b32 v88, v44, v43, 16\n\t"
        "v_alignbit_b32 v89, v45, v44, 16\n\t"
        "v_mfma_f32_16x16x32_bf16 v[4:7], v[80:83], v[60:63], v[4:7]\n\t"
        "v_alignbit_b32 v90, v46, v45, 16\n\t"
        "v_alignbit_b32 v91, v47, v46, 16\n\t"
        "v_mfma_f32_16x16x32_bf16 v[20:23], v[82:85], v[60:63], v[20:23]\n\t"
        "v_mfma_f32_16x16x32_bf16 v[8:11], v[42:45], v[64:67], v[8:11]\n\t"
        "ds_read_b128 v[32:35], %2 offset:576\n\t"
        "v_mfma_f32_16x16x32_bf16 v[24:27], v[44:47], v[64:67], v[24:27]\n\t"
        "ds_read_b128 v[36:39], %2 offset:592\n\t"
        "v_mfma_f32_16x16x32_bf16 v[12:15], v[42:45], v[68:71], v[12:15]\n\t"
        "ds_read_b128 v[56:59], %3 offset:640\n\t"
        "v_mfma_f32_16x16x32_bf16 v[28:31], v[44:47], v[68:71], v[28:31]\n\t"
        "ds_read_b128 v[60:63], %3 offset:7312\n\t"
        "s_waitcnt lgkmcnt(4)\n\t"
        "v_mfma_f32_16x16x32_bf16 v[0:3], v[86:89], v[64:67], v[0:3]\n\t"
        "v_alignbit_b32 v80, v50, v49, 16\n\t"
        "v_alignbit_b32 v81, v51, v50, 16\n\t"
        "v_mfma_f32_16x16x32_bf16 v[16:19], v[88:91], v[64:67], v[16:19]\n\t"
        "v_alignbit_b32 v82, v52, v51, 16\n\t"
        "v_alignbit_b32 v83, v53, v52, 16\n\t"
        "v_mfma_f32_16x16x32_bf16 v[4:7], v[86:89], v[68:71], v[4:7]\n\t"
        "v_alignbit_b32 v84, v54, v53, 16\n\t"
        "v_alignbit_b32 v85, v55, v54, 16\n\t"
        "v_mfma_f32_16x16x32_bf16 v[20:23], v[88:91], v[68:71], v[20:23]\n\t"
        "v_mfma_f32_16x16x32_bf16 v[8:11], v[50:53], v[72:75], v[8:11]\n\t"
        "ds_read_b128 v[40:43], %2 offset:640\n\t"
        "v_mfma_f32_16x16x32_bf16 v[24:27], v[52:55], v[72:75], v[24:27]\n\t"
        "ds_read_b128 v[44:47], %2 offset:656\n\t"
        "v_mfma_f32_16x16x32_bf16 v[12:15], v[50:53], v[76:79], v[12:15]\n\t"
        "ds_read_b128 v[64:67], %3 offset:704\n\t"
        "v_mfma_f32_16x16x32_bf16 v[28:31], v[52:55], v[76:79], v[28:31]\n\t"
        "ds_read_b128 v[68:71], %3 offset:7376\n\t"
        "s_waitcnt lgkmcnt(4)\n\t"
        "v_mfma_f32_16x16x32_bf16 v[0:3], v[80:83], v[72:75], v[0:3]\n\t"
        "v_alignbit_b32 v86, v34, v33, 16\n\t"
        "v_alignbit_b32 v87, v35, v34, 16\n\t"
        "v_mfma_f32_16x16x32_bf16 v[16:19], v[82:85], v[72:75], v[16:19]\n\t"
        "v_alignbit_b32 v88, v36, v35, 16\n\t"
        "v_alignbit_b32 v89, v37, v36, 16\n\t"
        "v_mfma_f32_16x16x32_bf16 v[4:7], v[80:83], v[76:79], v[4:7]\n\t"
        "v_alignbit_b32 v90, v38, v37, 16\n\t"
        "v_alignbit_b32 v91, v39, v38, 16\n\t"
        "v_mfma_f32_16x16x32_bf16 v[20:23], v[82:85], v[76:79], v[20:23]\n\t"
        "v_mfma_f32_16x16x32_bf16 v[8:11], v[34:37], v[56:59], v[8:11]\n\t"
        "ds_read_b128 v[48:51], %2 offset:704\n\t"
        "v_mfma_f32_16x16x32_bf16 v[24:27], v[36:39], v[56:59], v[24:27]\n\t"
        "ds_read_b128 v[52:55], %2 offset:720\n\t"
        "v_mfma_f32_16x16x32_bf16 v[12:15], v[34:37], v[60:63], v[12:15]\n\t"
        "ds_read_b128 v[72:75], %3 offset:768\n\t"
        "v_mfma_f32_16x16x32_bf16 v[28:31], v[36:39], v[60:63], v[28:31]\n\t"
        "ds_read_b128 v[76:79], %3 offset:7440\n\t"
        "s_waitcnt lgkmcnt(4)\n\t"
        "v_mfma_f32_16x16x32_bf16 v[0:3], v[86:89], v[56:59], v[0:3]\n\t"
        "v_alignbit_b32 v80, v42, v41, 16\n\t"
        "v_alignbit_b32 v81, v43, v42, 16\n\t"
        "v_mfma_f32_16x16x32_bf16 v[16:19], v[88:91], v[56:59], v[16:19]\n\t"
        "v_alignbit_b32 v82, v44, v43, 16\n\t"
        "v_alignbit_b32 v83, v45, v44, 16\n\t"
        "v_mfma_f32_16x16x32_bf16 v[4:7], v[86:89], v[60:63], v[4:7]\n\t"
        "v_alignbit_b32 v84, v46, v45, 16\n\t"
        "v_alignbit_b32 v85, v47, v46, 16\n\t"
        "v_mfma_f32_16x16x32_bf16 v[20:23], v[88:91], v[60:63], v[20:23]\n\t"
        "v_mfma_f32_16x16x32_bf16 v[8:11], v[42:45], v[64:67], v[8:11]\n\t"
        "ds_read_b128 v[32:35], %2 offset:768\n\t"
        "v_mfma_f32_16x16x32_bf16 v[24:27], v[44:47], v[64:67], v[24:27]\n\t"
        "ds_read_b128 v[36:39], %2 offset:784\n\t"
        "v_mfma_f32_16x16x32_bf16 v[12:15], v[42:45], v[68:71], v[12:15]\n\t"
        "ds_read_b128 v[56:59], %3 offset:864\n\t"
        "v_mfma_f32_16x16x32_bf16 v[28:31], v[44:47], v[68:71], v[28:31]\n\t"
        "ds_read_b128 v[60:63], %3 offset:7536\n\t"
        "s_waitcnt lgkmcnt(4)\n\t"
        "v_mfma_f32_16x16x32_bf16 v[0:3], v[80:83], v[64:67], v[0:3]\n\t"
        "v_alignbit_b32 v86, v50, v49, 16\n\t"
        "v_alignbit_b32 v87, v51, v50, 16\n\t"
        "v_mfma_f32_16x16x32_bf16 v[16:19], v[82:85], v[64:67], v[16:19]\n\t"
        "v_alignbit_b32 v88, v52, v51, 16\n\t"
        "v_alignbit_b32 v89, v53, v52, 16\n\t"
        "v_mfma_f32_16x16x32_bf16 v[4:7], v[80:83], v[68:71], v[4:7]\n\t"
        "v_alignbit_b32 v90, v54, v53, 16\n\t"
        "v_alignbit_b32 v91, v55, v54, 16\n\t"
        "v_mfma_f32_16x16x32_bf16 v[20:23], v[82:85], v[68:71], v[20:23]\n\t"
        "v_mfma_f32_16x16x32_bf16 v[8:11], v[50:53], v[72:75], v[8:11]\n\t"
        "ds_read_b128 v[40:43], %2 offset:832\n\t"
        "v_mfma_f32_16x16x32_bf16 v[24:27], v[52:55], v[72:75], v[24:27]\n\t"
        "ds_read_b128 v[44:47], %2 offset:848\n\t"
        "v_mfma_f32_16x16x32_bf16 v[12:15], v[50:53], v[76:79], v[12:15]\n\t"
        "ds_read_b128 v[64:67], %3 offset:928\n\t"
        "v_mfma_f32_16x16x32_bf16 v[28:31], v[52:55], v[76:79], v[28:31]\n\t"
        "ds_read_b128 v[68:71], %3 offset:7600\n\t"
        "s_waitcnt lgkmcnt(4)\n\t"
        "v_mfma_f32_16x16x32_bf16 v[0:3], v[86:89], v[72:75], v[0:3]\n\t"
        "v_alignbit_b32 v80, v34, v33, 16\n\t"
        "v_alignbit_b32 v81, v35, v34, 16\n\t"
        "v_mfma_f32_16x16x32_bf16 v[16:19], v[88:91], v[72:75], v[16:19]\n\t"
        "v_alignbit_b32 v82, v36, v35, 16\n\t"
        "v_alignbit_b32 v83, v37, v36, 16\n\t"
        "v_mfma_f32_16x16x32_bf16 v[4:7], v[86:89], v[76:79], v[4:7]\n\t"
        "v_alignbit_b32 v84, v38, v37, 16\n\t"
        "v_alignbit_b32 v85, v39, v38, 16\n\t"
        "v_mfma_f32_16x16x32_bf16 v[20:23], v[88:91], v[76:79], v[20:23]\n\t"
        "v_mfma_f32_16x16x32_bf16 v[8:11], v[34:37], v[56:59], v[8:11]\n\t"
        "ds_read_b128 v[48:51], %2 offset:896\n\t"
        "v_mfma_f32_16x16x32_bf16 v[24:27], v[36:39], v[56:59], v[24:27]\n\t"
        "ds_read_b128 v[52:55], %2 offset:912\n\t"
        "v_mfma_f32_16x16x32_bf16 v[12:15], v[34:37], v[60:63], v[12:15]\n\t"
        "ds_read_b128 v[72:75], %3 offset:992\n\t"
        "v_mfma_f32_16x16x32_bf16 v[28:31], v[36:39], v[60:63], v[28:31]\n\t"
        "ds_read_b128 v[76:79], %3 offset:7664\n\t"
        "s_waitcnt lgkmcnt(4)\n\t"
        "v_mfma_f32_16x16x32_bf16 v[0:3], v[80:83], v[56:59], v[0:3]\n\t"
        "v_alignbit_b32 v86, v42, v41, 16\n\t"
        "v_alignbit_b32 v87, v43, v42, 16\n\t"
        "v_mfma_f32_16x16x32_bf16 v[16:19], v[82:85], v[56:59], v[16:19]\n\t"
        "v_alignbit_b32 v88, v44, v43, 16\n\t"
        "v_alignbit_b32 v89, v45, v44, 16\n\t"
        "v_mfma_f32_16x16x32_bf16 v[4:7], v[80:83], v[60:63], v[4:7]\n\t"
        "v_alignbit_b32 v90, v46, v45, 16\n\t"
        "v_alignbit_b32 v91, v47, v46, 16\n\t"
        "v_mfma_f32_16x16x32_bf16 v[20:23], v[82:85], v[60:63], v[20:23]\n\t"
        "v_mfma_f32_16x16x32_bf16 v[8:11], v[42:45], v[64:67], v[8:11]\n\t"
        "ds_read_b128 v[32:35], %2 offset:960\n\t"
        "v_mfma_f32_16x16x32_bf16 v[24:27], v[44:47], v[64:67], v[24:27]\n\t"
        "ds_read_b128 v[36:39], %2 offset:976\n\t"
        "v_mfma_f32_16x16x32_bf16 v[12:15], v[42:45], v[68:71], v[12:15]\n\t"
        "ds_read_b128 v[56:59], %3 offset:1056\n\t"
        "v_mfma_f32_16x16x32_bf16 v[28:31], v[44:47], v[68:71], v[28:31]\n\t"
        "ds_read_b128 v[60:63], %3 offset:7728\n\t"
        "s_waitcnt lgkmcnt(4)\n\t"
        "v_mfma_f32_16x16x32_bf16 v[0:3], v[86:89], v[64:67], v[0:3]\n\t"
        "v_alignbit_b32 v80, v50, v49, 16\n\t"
        "v_alignbit_b32 v81, v51, v50, 16\n\t"
        "v_mfma_f32_16x16x32_bf16 v[16:19], v[88:91], v[64:67], v[16:19]\n\t"
        "v_alignbit_b32 v82, v52, v51, 16\n\t"
        "v_alignbit_b32 v83, v53, v52, 16\n\t"
        "v_mfma_f32_16x16x32_bf16 v[4:7], v[86:89], v[68:71], v[4:7]\n\t"
        "v_alignbit_b32 v84, v54, v53, 16\n\t"
        "v_alignbit_b32 v85, v55, v54, 16\n\t"
        "v_mfma_f32_16x16x32_bf16 v[20:23], v[88:91], v[68:71], v[20:23]\n\t"
        "v_mfma_f32_16x16x32_bf16 v[8:11], v[50:53], v[72:75], v[8:11]\n\t"
        "ds_read_b128 v[40:43], %2 offset:1024\n\t"
        "v_mfma_f32_16x16x32_bf16 v[24:27], v[52:55], v[72:75], v[24:27]\n\t"
        "ds_read_b128 v[44:47], %2 offset:1040\n\t"
        "v_mfma_f32_16x16x32_bf16 v[12:15], v[50:53], v[76:79], v[12:15]\n\t"
        "ds_read_b128 v[64:67], %3 offset:1152\n\t"
        "v_mfma_f32_16x16x32_bf16 v[28:31], v[52:55], v[76:79], v[28:31]\n\t"
        "ds_read_b128 v[68:71], %3 offset:7824\n\t"
        "s_waitcnt lgkmcnt(4)\n\t"
        "v_mfma_f32_16x16x32_bf16 v[0:3], v[80:83], v[72:75], v[0:3]\n\t"
        "v_alignbit_b32 v86, v34, v33, 16\n\t"
        "v_alignbit_b32 v87, v35, v34, 16\n\t"
        "v_mfma_f32_16x16x32_bf16 v[16:19], v[82:85], v[72:75], v[16:19]\n\t"
        "v_alignbit_b32 v88, v36, v35, 16\n\t"
        "v_alignbit_b32 v89, v37, v36, 16\n\t"
        "v_mfma_f32_16x16x32_bf16 v[4:7], v[80:83], v[76:79], v[4:7]\n\t"
        "v_alignbit_b32 v90, v38, v37, 16\n\t"
        "v_alignbit_b32 v91, v39, v38, 16\n\t"
        "v_mfma_f32_16x16x32_bf16 v[20:23], v[82:85], v[76:79], v[20:23]\n\t"
        "v_mfma_f32_16x16x32_bf16 v[8:11], v[34:37], v[56:59], v[8:11]\n\t"
        "ds_read_b128 v[48:51], %2 offset:1088\n\t"
        "v_mfma_f32_16x16x32_bf16 v[24:27], v[36:39], v[56:59], v[24:27]\n\t"
        "ds_read_b128 v[52:55], %2 offset:1104\n\t"
        "v_mfma_f32_16x16x32_bf16 v[12:15], v[34:37], v[60:63], v[12:15]\n\t"
        "ds_read_b128 v[72:75], %3 offset:1216\n\t"
        "v_mfma_f32_16x16x32_bf16 v[28:31], v[36:39], v[60:63], v[28:31]\n\t"
        "ds_read_b128 v[76:79], %3 offset:7888\n\t"
        "s_waitcnt lgkmcnt(4)\n\t"
        "v_mfma_f32_16x16x32_bf16 v[0:3], v[86:89], v[56:59], v[0:3]\n\t"
        "v_alignbit_b32 v80, v42, v41, 16\n\t"
        "v_alignbit_b32 v81, v43, v42, 16\n\t"
        "v_mfma_f32_16x16x32_bf16 v[16:19], v[88:91], v[56:59], v[16:19]\n\t"
        "v_alignbit_b32 v82, v44, v43, 16\n\t"
        "v_alignbit_b32 v83, v45, v44, 16\n\t"
        "v_mfma_f32_16x16x32_bf16 v[4:7], v[86:89], v[60:63], v[4:7]\n\t"
        "v_alignbit_b32 v84, v46, v45, 16\n\t"
        "v_alignbit_b32 v85, v47, v46, 16\n\t"
        "v_mfma_f32_16x16x32_bf16 v[20:23], v[88:91], v[60:63], v[20:23]\n\t"
        "v_mfma_f32_16x16x32_bf16 v[8:11], v[42:45], v[64:67], v[8:11]\n\t"
        "ds_read_b128 v[32:35], %2 offset:1152\n\t"
        "v_mfma_f32_16x16x32_bf16 v[24:27], v[44:47], v[64:67], v[24:27]\n\t"
        "ds_read_b128 v[36:39], %2 offset:1168\n\t"
        "v_mfma_f32_16x16x32_bf16 v[12:15], v[42:45], v[68:71], v[12:15]\n\t"
        "ds_read_b128 v[56:59], %3 offset:1280\n\t"
        "v_mfma_f32_16x16x32_bf16 v[28:31], v[44:47], v[68:71], v[28:31]\n\t"
        "ds_read_b128 v[60:63], %3 offset:7952\n\t"
        "s_waitcnt lgkmcnt(4)\n\t"
        "v_mfma_f32_16x16x32_bf16 v[0:3], v[80:83], v[64:67], v[0:3]\n\t"
        "v_alignbit_b32 v86, v50, v49, 16\n\t"
        "v_alignbit_b32 v87, v51, v50, 16\n\t"
        "v_mfma_f32_16x16x32_bf16 v[16:19], v[82:85], v[64:67], v[16:19]\n\t"
        "v_alignbit_b32 v88, v52, v51, 16\n\t"
        "v_alignbit_b32 v89, v53, v52, 16\n\t"
        "v_mfma_f32_16x16x32_bf16 v[4:7], v[80:83], v[68:71], v[4:7]\n\t"
        "v_alignbit_b32 v90, v54, v53, 16\n\t"
        "v_alignbit_b32 v91, v55, v54, 16\n\t"
        "v_mfma_f32_16x16x32_bf16 v[20:23], v[82:85], v[68:71], v[20:23]\n\t"
        "v_mfma_f32_16x16x32_bf16 v[8:11], v[50:53], v[72:75], v[8:11]\n\t"
        "ds_read_b128 v[40:43], %2 offset:1216\n\t"
        "v_mfma_f32_16x16x32_bf16 v[24:27], v[52:55], v[72:75], v[24:27]\n\t"
        "ds_read_b128 v[44:47], %2 offset:1232\n\t"
        "v_mfma_f32_16x16x32_bf16 v[12:15], v[50:53], v[76:79], v[12:15]\n\t"
        "ds_read_b128 v[64:67], %3 offset:1344\n\t"
        "v_mfma_f32_16x16x32_bf16 v[28:31], v[52:55], v[76:79], v[28:31]\n\t"
        "ds_read_b128 v[68:71], %3 offset:8016\n\t"
        "s_waitcnt lgkmcnt(4)\n\t"
        "v_mfma_f32_16x16x32_bf16 v[0:3], v[86:89], v[72:75], v[0:3]\n\t"
        "v_alignbit_b32 v80, v34, v33, 16\n\t"
        "v_alignbit_b32 v81, v35, v34, 16\n\t"
        "v_mfma_f32_16x16x32_bf16 v[16:19], v[88:91], v[72:75], v[16:19]\n\t"
        "v_alignbit_b32 v82, v36, v35, 16\n\t"
        "v_alignbit_b32 v83, v37, v36, 16\n\t"
        "v_mfma_f32_16x16x32_bf16 v[4:7], v[86:89], v[76:79], v[4:7]\n\t"
        "v_alignbit_b32 v84, v38, v37, 16\n\t"
        "v_alignbit_b32 v85, v39, v38, 16\n\t"
        "v_mfma_f32_16x16x32_bf16 v[20:23], v[88:91], v[76:79], v[20:23]\n\t"
        "v_mfma_f32_16x16x32_bf16 v[8:11], v[34:37], v[56:59], v[8:11]\n\t"
        "ds_read_b128 v[48:51], %2 offset:1280\n\t"
        "v_mfma_f32_16x16x32_bf16 v[24:27], v[36:39], v[56:59], v[24:27]\n\t"
        "ds_read_b128 v[52:55], %2 offset:1296\n\t"
        "v_mfma_f32_16x16x32_bf16 v[12:15], v[34:37], v[60:63], v[12:15]\n\t"
        "ds_read_b128 v[72:75], %3 offset:1440\n\t"
        "v_mfma_f32_16x16x32_bf16 v[28:31], v[36:39], v[60:63], v[28:31]\n\t"
        "ds_read_b128 v[76:79], %3 offset:8112\n\t"
        "s_waitcnt lgkmcnt(4)\n\t"
        "v_mfma_f32_16x16x32_bf16 v[0:3], v[80:83], v[56:59], v[0:3]\n\t"
        "v_alignbit_b32 v86, v42, v41, 16\n\t"
        "v_alignbit_b32 v87, v43, v42, 16\n\t"
        "v_mfma_f32_16x16x32_bf16 v[16:19], v[82:85], v[56:59], v[16:19]\n\t"
        "v_alignbit_b32 v88, v44, v43, 16\n\t"
        "v_alignbit_b32 v89, v45, v44, 16\n\t"
        "v_mfma_f32_16x16x32_bf16 v[4:7], v[80:83], v[60:63], v[4:7]\n\t"
        "v_alignbit_b32 v90, v46, v45, 16\n\t"
        "v_alignbit_b32 v91, v47, v46, 16\n\t"
        "v_mfma_f32_16x16x32_bf16 v[20:23], v[82:85], v[60:63], v[20:23]\n\t"
        "v_mfma_f32_16x16x32_bf16 v[8:11], v[42:45], v[64:67], v[8:11]\n\t"
        "ds_read_b128 v[32:35], %2 offset:1344\n\t"
        "v_mfma_f32_16x16x32_bf16 v[24:27], v[44:47], v[64:67], v[24:27]\n\t"
        "ds_read_b128 v[36:39], %2 offset:1360\n\t"
        "v_mfma_f32_16x16x32_bf16 v[12:15], v[42:45], v[68:71], v[12:15]\n\t"
        "ds_read_b128 v[56:59], %3 offset:1504\n\t"
        "v_mfma_f32_16x16x32_bf16 v[28:31], v[44:47], v[68:71], v[28:31]\n\t"
        "ds_read_b128 v[60:63], %3 offset:8176\n\t"
        "s_waitcnt lgkmcnt(4)\n\t"
        "v_mfma_f32_16x16x32_bf16 v[0:3], v[86:89], v[64:67], v[0:3]\n\t"
        "v_alignbit_b32 v80, v50, v49, 16\n\t"
        "v_alignbit_b32 v81, v51, v50, 16\n\t"
        "v_mfma_f32_16x16x32_bf16 v[16:19], v[88:91], v[64:67], v[16:19]\n\t"
        "v_alignbit_b32 v82, v52, v51, 16\n\t"
        "v_alignbit_b32 v83, v53, v52, 16\n\t"
        "v_mfma_f32_16x16x32_bf16 v[4:7], v[86:89], v[68:71], v[4:7]\n\t"
        "v_alignbit_b32 v84, v54, v53, 16\n\t"
        "v_alignbit_b32 v85, v55, v54, 16\n\t"
        "v_mfma_f32_16x16x32_bf16 v[20:23], v[88:91], v[68:71], v[20:23]\n\t"
        "v_mfma_f32_16x16x32_bf16 v[8:11], v[50:53], v[72:75], v[8:11]\n\t"
        "ds_read_b128 v[40:43], %2 offset:1408\n\t"
        "v_mfma_f32_16x16x32_bf16 v[24:27], v[52:55], v[72:75], v[24:27]\n\t"
        "ds_read_b128 v[44:47], %2 offset:1424\n\t"
        "v_mfma_f32_16x16x32_bf16 v[12:15], v[50:53], v[76:79], v[12:15]\n\t"
        "ds_read_b128 v[64:67], %3 offset:1568\n\t"
        "v_mfma_f32_16x16x32_bf16 v[28:31], v[52:55], v[76:79], v[28:31]\n\t"
        "ds_read_b128 v[68:71], %3 offset:8240\n\t"
        "s_waitcnt lgkmcnt(4)\n\t"
        "v_mfma_f32_16x16x32_bf16 v[0:3], v[80:83], v[72:75], v[0:3]\n\t"
        "v_alignbit_b32 v86, v34, v33, 16\n\t"
        "v_alignbit_b32 v87, v35, v34, 16\n\t"
        "v_mfma_f32_16x16x32_bf16 v[16:19], v[82:85], v[72:75], v[16:19]\n\t"
        "v_alignbit_b32 v88, v36, v35, 16\n\t"
        "v_alignbit_b32 v89, v37, v36, 16\n\t"
        "v_mfma_f32_16x16x32_bf16 v[4:7], v[80:83], v[76:79], v[4:7]\n\t"
        "v_alignbit_b32 v90, v38, v37, 16\n\t"
        "v_alignbit_b32 v91, v39, v38, 16\n\t"
        "v_mfma_f32_16x16x32_bf16 v[20:23], v[82:85], v[76:79], v[20:23]\n\t"
        "v_mfma_f32_16x16x32_bf16 v[8:11], v[34:37], v[56:59], v[8:11]\n\t"
        "ds_read_b128 v[48:51], %2 offset:1472\n\t"
        "v_mfma_f32_16x16x32_bf16 v[24:27], v[36:39], v[56:59], v[24:27]\n\t"
        "ds_read_b128 v[52:55], %2 offset:1488\n\t"
        "v_mfma_f32_16x16x32_bf16 v[12:15], v[34:37], v[60:63], v[12:15]\n\t"
        "ds_read_b128 v[72:75], %3 offset:1632\n\t"
        "v_mfma_f32_16x16x32_bf16 v[28:31], v[36:39], v[60:63], v[28:31]\n\t"
        "ds_read_b128 v[76:79], %3 offset:8304\n\t"
        "s_waitcnt lgkmcnt(4)\n\t"
        "v_mfma_f32_16x16x32_bf16 v[0:3], v[86:89], v[56:59], v[0:3]\n\t"
        "v_alignbit_b32 v80, v42, v41, 16\n\t"
        "v_alignbit_b32 v81, v43, v42, 16\n\t"
        "v_mfma_f32_16x16x32_bf16 v[16:19], v[88:91], v[56:59], v[16:19]\n\t"
        "v_alignbit_b32 v82, v44, v43, 16\n\t"
        "v_alignbit_b32 v83, v45, v44, 16\n\t"
        "v_mfma_f32_16x16x32_bf16 v[4:7], v[86:89], v[60:63], v[4:7]\n\t"
        "v_alignbit_b32 v84, v46, v45, 16\n\t"
        "v_alignbit_b32 v85, v47, v46, 16\n\t"
        "v_mfma_f32_16x16x32_bf16 v[20:23], v[88:91], v[60:63], v[20:23]\n\t"
        "v_mfma_f32_16x16x32_bf16 v[8:11], v[42:45], v[64:67], v[8:11]\n\t"
        "ds_read_b128 v[32:35], %2 offset:1536\n\t"
        "v_mfma_f32_16x16x32_bf16 v[24:27], v[44:47], v[64:67], v[24:27]\n\t"
        "ds_read_b128 v[36:39], %2 offset:1552\n\t"
        "v_mfma_f32_16x16x32_bf16 v[12:15], v[42:45], v[68:71], v[12:15]\n\t"
        "ds_read_b128 v[56:59], %3 offset:1728\n\t"
        "v_mfma_f32_16x16x32_bf16 v[28:31], v[44:47], v[68:71], v[28:31]\n\t"
        "ds_read_b128 v[60:63], %3 offset:8400\n\t"
        "s_waitcnt lgkmcnt(4)\n\t"
        "v_mfma_f32_16x16x32_bf16 v[0:3], v[80:83], v[64:67], v[0:3]\n\t"
        "v_alignbit_b32 v86, v50, v49, 16\n\t"
        "v_alignbit_b32 v87, v51, v50, 16\n\t"
        "v_mfma_f32_16x16x32_bf16 v[16:19], v[82:85], v[64:67], v[16:19]\n\t"
        "v_alignbit_b32 v88, v52, v51, 16\n\t"
        "v_alignbit_b32 v89, v53, v52, 16\n\t"
        "v_mfma_f32_16x16x32_bf16 v[4:7], v[80:83], v[68:71], v[4:7]\n\t"
        "v_alignbit_b32 v90, v54, v53, 16\n\t"
        "v_alignbit_b32 v91, v55, v54, 16\n\t"
        "v_mfma_f32_16x16x32_bf16 v[20:23], v[82:85], v[68:71], v[20:23]\n\t"
        "v_mfma_f32_16x16x32_bf16 v[8:11], v[50:53], v[72:75], v[8:11]\n\t"
        "ds_read_b128 v[40:43], %2 offset:1600\n\t"
        "v_mfma_f32_16x16x32_bf16 v[24:27], v[52:55], v[72:75], v[24:27]\n\t"
        "ds_read_b128 v[44:47], %2 offset:1616\n\t"
        "v_mfma_f32_16x16x32_bf16 v[12:15], v[50:53], v[76:79], v[12:15]\n\t"
        "ds_read_b128 v[64:67], %3 offset:1792\n\t"
        "v_mfma_f32_16x16x32_bf16 v[28:31], v[52:55], v[76:79], v[28:31]\n\t"
        "ds_read_b128 v[68:71], %3 offset:8464\n\t"
        "s_waitcnt lgkmcnt(4)\n\t"
        "v_mfma_f32_16x16x32_bf16 v[0:3], v[86:89], v[72:75], v[0:3]\n\t"
        "v_alignbit_b32 v80, v34, v33, 16\n\t"
        "v_alignbit_b32 v81, v35, v34, 16\n\t"
        "v_mfma_f32_16x16x32_bf16 v[16:19], v[88:91], v[72:75], v[16:19]\n\t"
        "v_alignbit_b32 v82, v36, v35, 16\n\t"
        "v_alignbit_b32 v83, v37, v36, 16\n\t"
        "v_mfma_f32_16x16x32_bf16 v[4:7], v[86:89], v[76:79], v[4:7]\n\t"
        "v_alignbit_b32 v84, v38, v37, 16\n\t"
        "v_alignbit_b32 v85, v39, v38, 16\n\t"
        "v_mfma_f32_16x16x32_bf16 v[20:23], v[88:91], v[76:79], v[20:23]\n\t"
        "v_mfma_f32_16x16x32_bf16 v[8:11], v[34:37], v[56:59], v[8:11]\n\t"
        "ds_read_b128 v[48:51], %2 offset:1664\n\t"
        "v_mfma_f32_16x16x32_bf16 v[24:27], v[36:39], v[56:59], v[24:27]\n\t"
        "ds_read_b128 v[52:55], %2 offset:1680\n\t"
        "v_mfma_f32_16x16x32_bf16 v[12:15], v[34:37], v[60:63], v[12:15]\n\t"
        "ds_read_b128 v[72:75], %3 offset:1856\n\t"
        "v_mfma_f32_16x16x32_bf16 v[28:31], v[36:39], v[60:63], v[28:31]\n\t"
        "ds_read_b128 v[76:79], %3 offset:8528\n\t"
        "s_waitcnt lgkmcnt(4)\n\t"
        "v_mfma_f32_16x16x32_bf16 v[0:3], v[80:83], v[56:59], v[0:3]\n\t"
        "v_alignbit_b32 v86, v42, v41, 16\n\t"
        "v_alignbit_b32 v87, v43, v42, 16\n\t"
        "v_mfma_f32_16x16x32_bf16 v[16:19], v[82:85], v[56:59], v[16:19]\n\t"
        "v_alignbit_b32 v88, v44, v43, 16\n\t"
        "v_alignbit_b32 v89, v45, v44, 16\n\t"
        "v_mfma_f32_16x16x32_bf16 v[4:7], v[80:83], v[60:63], v[4:7]\n\t"
        "v_alignbit_b32 v90, v46, v45, 16\n\t"
        "v_alignbit_b32 v91, v47, v46, 16\n\t"
        "v_mfma_f32_16x16x32_bf16 v[20:23], v[82:85], v[60:63], v[20:23]\n\t"
        "v_mfma_f32_16x16x32_bf16 v[8:11], v[42:45], v[64:67], v[8:11]\n\t"
        "ds_read_b128 v[32:35], %2 offset:1728\n\t"
        "v_mfma_f32_16x16x32_bf16 v[24:27], v[44:47], v[64:67], v[24:27]\n\t"
        "ds_read_b128 v[36:39], %2 offset:1744\n\t"
        "v_mfma_f32_16x16x32_bf16 v[12:15], v[42:45], v[68:71], v[12:15]\n\t"
        "ds_read_b128 v[56:59], %3 offset:1920\n\t"
        "v_mfma_f32_16x16x32_bf16 v[28:31], v[44:47], v[68:71], v[28:31]\n\t"
        "ds_read_b128 v[60:63], %3 offset:8592\n\t"
        "s_waitcnt lgkmcnt(4)\n\t"
        "v_mfma_f32_16x16x32_bf16 v[0:3], v[86:89], v[64:67], v[0:3]\n\t"
        "v_alignbit_b32 v80, v50, v49, 16\n\t"
        "v_alignbit_b32 v81, v51, v50, 16\n\t"
        "v_mfma_f32_16x16x32_bf16 v[16:19], v[88:91], v[64:67], v[16:19]\n\t"
        "v_alignbit_b32 v82, v52, v51, 16\n\t"
        "v_alignbit_b32 v83, v53, v52, 16\n\t"
        "v_mfma_f32_16x16x32_bf16 v[4:7], v[86:89], v[68:71], v[4:7]\n\t"
        "v_alignbit_b32 v84, v54, v53, 16\n\t"
        "v_alignbit_b32 v85, v55, v54, 16\n\t"
        "v_mfma_f32_16x16x32_bf16 v[20:23], v[88:91], v[68:71], v[20:23]\n\t"
        "v_mfma_f32_16x16x32_bf16 v[8:11], v[50:53], v[72:75], v[8:11]\n\t"
        "ds_read_b128 v[40:43], %2 offset:1792\n\t"
        "v_mfma_f32_16x16x32_bf16 v[24:27], v[52:55], v[72:75], v[24:27]\n\t"
        "ds_read_b128 v[44:47], %2 offset:1808\n\t"
        "v_mfma_f32_16x16x32_bf16 v[12:15], v[50:53], v[76:79], v[12:15]\n\t"
        "ds_read_b128 v[64:67], %3 offset:2016\n\t"
        "v_mfma_f32_16x16x32_bf16 v[28:31], v[52:55], v[76:79], v[28:31]\n\t"
        "ds_read_b128 v[68:71], %3 offset:8688\n\t"
        "s_waitcnt lgkmcnt(4)\n\t"
        "v_mfma_f32_16x16x32_bf16 v[0:3], v[80:83], v[72:75], v[0:3]\n\t"
        "v_alignbit_b32 v86, v34, v33, 16\n\t"
        "v_alignbit_b32 v87, v35, v34, 16\n\t"
        "v_mfma_f32_16x16x32_bf16 v[16:19], v[82:85], v[72:75], v[16:19]\n\t"
        "v_alignbit_b32 v88, v36, v35, 16\n\t"
        "v_alignbit_b32 v89, v37, v36, 16\n\t"
        "v_mfma_f32_16x16x32_bf16 v[4:7], v[80:83], v[76:79], v[4:7]\n\t"
        "v_alignbit_b32 v90, v38, v37, 16\n\t"
        "v_alignbit_b32 v91, v39, v38, 16\n\t"
        "v_mfma_f32_16x16x32_bf16 v[20:23], v[82:85], v[76:79], v[20:23]\n\t"
        "v_mfma_f32_16x16x32_bf16 v[8:11], v[34:37], v[56:59], v[8:11]\n\t"
        "ds_read_b128 v[48:51], %2 offset:1856\n\t"
        "v_mfma_f32_16x16x32_bf16 v[24:27], v[36:39], v[56:59], v[24:27]\n\t"
        "ds_read_b128 v[52:55], %2 offset:1872\n\t"
        "v_mfma_f32_16x16x32_bf16 v[12:15], v[34:37], v[60:63], v[12:15]\n\t"
        "ds_read_b128 v[72:75], %3 offset:2080\n\t"
        "v_mfma_f32_16x16x32_bf16 v[28:31], v[36:39], v[60:63], v[28:31]\n\t"
        "ds_read_b128 v[76:79], %3 offset:8752\n\t"
        "s_waitcnt lgkmcnt(4)\n\t"
        "v_mfma_f32_16x16x32_bf16 v[0:3], v[86:89], v[56:59], v[0:3]\n\t"
        "v_alignbit_b32 v80, v42, v41, 16\n\t"
        "v_alignbit_b32 v81, v43, v42, 16\n\t"
        "v_mfma_f32_16x16x32_bf16 v[16:19], v[88:91], v[56:59], v[16:19]\n\t"
        "v_alignbit_b32 v82, v44, v43, 16\n\t"
        "v_alignbit_b32 v83, v45, v44, 16\n\t"
        "v_mfma_f32_16x16x32_bf16 v[4:7], v[86:89], v[60:63], v[4:7]\n\t"
        "v_alignbit_b32 v84, v46, v45, 16\n\t"
        "v_alignbit_b32 v85, v47, v46, 16\n\t"
        "v_mfma_f32_16x16x32_bf16 v[20:23], v[88:91], v[60:63], v[20:23]\n\t"
        "v_mfma_f32_16x16x32_bf16 v[8:11], v[42:45], v[64:67], v[8:11]\n\t"
        "ds_read_b128 v[32:35], %2 offset:1920\n\t"
        "v_mfma_f32_16x16x32_bf16 v[24:27], v[44:47], v[64:67], v[24:27]\n\t"
        "ds_read_b128 v[36:39], %2 offset:1936\n\t"
        "v_mfma_f32_16x16x32_bf16 v[12:15], v[42:45], v[68:71], v[12:15]\n\t"
        "ds_read_b128 v[56:59], %3 offset:2144\n\t"
        "v_mfma_f32_16x16x32_bf16 v[28:31], v[44:47], v[68:71], v[28:31]\n\t"
        "ds_read_b128 v[60:63], %3 offset:8816\n\t"
        "s_waitcnt lgkmcnt(4)\n\t"
        "v_mfma_f32_16x16x32_bf16 v[0:3], v[80:83], v[64:67], v[0:3]\n\t"
        "v_alignbit_b32 v86, v50, v49, 16\n\t"
        "v_alignbit_b32 v87, v51, v50, 16\n\t"
        "v_mfma_f32_16x16x32_bf16 v[16:19], v[82:85], v[64:67], v[16:19]\n\t"
        "v_alignbit_b32 v88, v52, v51, 16\n\t"
        "v_alignbit_b32 v89, v53, v52, 16\n\t"
        "v_mfma_f32_16x16x32_bf16 v[4:7], v[80:83], v[68:71], v[4:7]\n\t"
        "v_alignbit_b32 v90, v54, v53, 16\n\t"
        "v_alignbit_b32 v91, v55, v54, 16\n\t"
        "v_mfma_f32_16x16x32_bf16 v[20:23], v[82:85], v[68:71], v[20:23]\n\t"
        "v_mfma_f32_16x16x32_bf16 v[8:11], v[50:53], v[72:75], v[8:11]\n\t"
        "ds_read_b128 v[40:43], %2 offset:1984\n\t"
        "v_mfma_f32_16x16x32_bf16 v[24:27], v[52:55], v[72:75], v[24:27]\n\t"
        "ds_read_b128 v[44:47], %2 offset:2000\n\t"
        "v_mfma_f32_16x16x32_bf16 v[12:15], v[50:53], v[76:79], v[12:15]\n\t"
        "ds_read_b128 v[64:67], %3 offset:2208\n\t"
        "v_mfma_f32_16x16x32_bf16 v[28:31], v[52:55], v[76:79], v[28:31]\n\t"
        "ds_read_b128 v[68:71], %3 offset:8880\n\t"
        "s_waitcnt lgkmcnt(4)\n\t"
        "v_mfma_f32_16x16x32_bf16 v[0:3], v[86:89], v[72:75], v[0:3]\n\t"
        "v_alignbit_b32 v80, v34, v33, 16\n\t"
        "v_alignbit_b32 v81, v35, v34, 16\n\t"
        "v_mfma_f32_16x16x32_bf16 v[16:19], v[88:91], v[72:75], v[16:19]\n\t"
        "v_alignbit_b32 v82, v36, v35, 16\n\t"
        "v_alignbit_b32 v83, v37, v36, 16\n\t"
        "v_mfma_f32_16x16x32_bf16 v[4:7], v[86:89], v[76:79], v[4:7]\n\t"
        "v_alignbit_b32 v84, v38, v37, 16\n\t"
        "v_alignbit_b32 v85, v39, v38, 16\n\t"
        "v_mfma_f32_16x16x32_bf16 v[20:23], v[88:91], v[76:79], v[20:23]\n\t"
        "v_mfma_f32_16x16x32_bf16 v[8:11], v[34:37], v[56:59], v[8:11]\n\t"
        "v_mfma_f32_16x16x32_bf16 v[24:27], v[36:39], v[56:59], v[24:27]\n\t"
        "v_mfma_f32_16x16x32_bf16 v[12:15], v[34:37], v[60:63], v[12:15]\n\t"
        "v_mfma_f32_16x16x32_bf16 v[28:31], v[36:39], v[60:63], v[28:31]\n\t"
        "s_waitcnt lgkmcnt(0)\n\t"
        "v_mfma_f32_16x16x32_bf16 v[0:3], v[80:83], v[56:59], v[0:3]\n\t"
        "v_alignbit_b32 v86, v42, v41, 16\n\t"
        "v_alignbit_b32 v87, v43, v42, 16\n\t"
        "v_mfma_f32_16x16x32_bf16 v[16:19], v[82:85], v[56:59], v[16:19]\n\t"
        "v_alignbit_b32 v88, v44, v43, 16\n\t"
        "v_alignbit_b32 v89, v45, v44, 16\n\t"
        "v_mfma_f32_16x16x32_bf16 v[4:7], v[80:83], v[60:63], v[4:7]\n\t"
        "v_alignbit_b32 v90, v46, v45, 16\n\t"
        "v_alignbit_b32 v91, v47, v46, 16\n\t"
        "v_mfma_f32_16x16x32_bf16 v[20:23], v[82:85], v[60:63], v[20:23]\n\t"
        "v_mfma_f32_16x16x32_bf16 v[8:11], v[42:45], v[64:67], v[8:11]\n\t"
        "v_mfma_f32_16x16x32_bf16 v[24:27], v[44:47], v[64:67], v[24:27]\n\t"
        "v_mfma_f32_16x16x32_bf16 v[12:15], v[42:45], v[68:71], v[12:15]\n\t"
        "v_mfma_f32_16x16x32_bf16 v[28:31], v[44:47], v[68:71], v[28:31]\n\t"
        "v_mfma_f32_16x16x32_bf16 v[0:3], v[86:89], v[64:67], v[0:3]\n\t"
        "v_mfma_f32_16x16x32_bf16 v[16:19], v[88:91], v[64:67], v[16:19]\n\t"
        "v_mfma_f32_16x16x32_bf16 v[4:7], v[86:89], v[68:71], v[4:7]\n\t"
        "v_mfma_f32_16x16x32_bf16 v[20:23], v[88:91], v[68:71], v[20:23]\n\t"
        "s_branch LCE_%=\n\t"
        "LCP_%=:\n\t"
        "ds_read_b128 v[32:35], %2 offset:0\n\t"
        "ds_read_b128 v[36:39], %2 offset:16\n\t"
        "ds_read_b128 v[102:105], %3 offset:0\n\t"
        "ds_read_b128 v[40:43], %2 offset:32\n\t"
        "ds_read_b128 v[106:109], %3 offset:16\n\t"
        "ds_read_b128 v[44:47], %2 offset:48\n\t"
        "ds_read_b128 v[110:113], %3 offset:32\n\t"
        "s_waitcnt lgkmcnt(4)\n\t"
        "v_alignbit_b32 v68, v34, v33, 16\n\t"
        "v_alignbit_b32 v69, v35, v34, 16\n\t"
        "v_alignbit_b32 v70, v36, v35, 16\n\t"
        "v_alignbit_b32 v71, v37, v36, 16\n\t"
        "v_alignbit_b32 v72, v38, v37, 16\n\t"
        "v_alignbit_b32 v73, v39, v38, 16\n\t"
        "s_nop 1\n\t"
        "v_mfma_f32_16x16x32_bf16 v[4:7], v[34:37], v[102:105], 0\n\t"
        "ds_read_b128 v[48:51], %2 offset:64\n\t"
        "ds_read_b128 v[114:117], %3 offset:48\n\t"
        "v_mfma_f32_16x16x32_bf16 v[12:15], v[36:39], v[102:105], 0\n\t"
        "s_waitcnt lgkmcnt(4)\n\t"
        "v_alignbit_b32 v74, v40, v39, 16\n\t"
        "v_mfma_f32_16x16x32_bf16 v[0:3], v[68:71], v[102:105], 0\n\t"
        "v_alignbit_b32 v75, v41, v40, 16\n\t"
        "v_alignbit_b32 v76, v42, v41, 16\n\t"
        "v_mfma_f32_16x16x32_bf16 v[8:11], v[70:73], v[102:105], 0\n\t"
        "v_alignbit_b32 v77, v43, v42, 16\n\t"
        "v_mfma_f32_16x16x32_bf16 v[4:7], v[38:41], v[106:109], v[4:7]\n\t"
        "ds_read_b128 v[52:55], %2 offset:80\n\t"
        "ds_read_b128 v[102:105], %3 offset:64\n\t"
        "v_mfma_f32_16x16x32_bf16 v[12:15], v[40:43], v[106:109], v[12:15]\n\t"
        "s_waitcnt lgkmcnt(4)\n\t"
        "v_alignbit_b32 v78, v44, v43, 16\n\t"
        "v_mfma_f32_16x16x32_bf16 v[0:3], v[72:75], v[106:109], v[0:3]\n\t"
        "v_alignbit_b32 v79, v45, v44, 16\n\t"
        "v_alignbit_b32 v80, v46, v45, 16\n\t"
        "v_mfma_f32_16x16x32_bf16 v[8:11], v[74:77], v[106:109], v[8:11]\n\t"
        "v_alignbit_b32 v81, v47, v46, 16\n\t"
        "v_mfma_f32_16x16x32_bf16 v[4:7], v[42:45], v[110:113], v[4:7]\n\t"
        "ds_read_b128 v[56:59], %2 offset:96\n\t"
        "ds_read_b128 v[106:109], %3 offset:80\n\t"
        "v_mfma_f32_16x16x32_bf16 v[12:15], v[44:47], v[110:113], v[12:15]\n\t"
        "s_waitcnt lgkmcnt(4)\n\t"
        "v_alignbit_b32 v82, v48, v47, 16\n\t"
        "v_mfma_f32_16x16x32_bf16 v[0:3], v[76:79], v[110:113], v[0:3]\n\t"
        "v_alignbit_b32 v83, v49, v48, 16\n\t"
        "v_alignbit_b32 v84, v50, v49, 16\n\t"
        "v_mfma_f32_16x16x32_bf16 v[8:11], v[78:81], v[110:113], v[8:11]\n\t"
        "v_alignbit_b32 v85, v51, v50, 16\n\t"
        "v_mfma_f32_16x16x32_bf16 v[4:7], v[46:49], v[114:117], v[4:7]\n\t"
        "ds_read_b128 v[60:63], %2 offset:112\n\t"
        "ds_read_b128 v[110:113], %3 offset:96\n\t"
        "v_mfma_f32_16x16x32_bf16 v[12:15], v[48:51], v[114:117], v[12:15]\n\t"
        "s_waitcnt lgkmcnt(4)\n\t"
        "v_alignbit_b32 v86, v52, v51, 16\n\t"
        "v_mfma_f32_16x16x32_bf16 v[0:3], v[80:83], v[114:117], v[0:3]\n\t"
        "v_alignbit_b32 v87, v53, v52, 16\n\t"
        "v_alignbit_b32 v88, v54, v53, 16\n\t"
        "v_mfma_f32_16x16x32_bf16 v[8:11], v[82:85], v[114:117], v[8:11]\n\t"
        "v_alignbit_b32 v89, v55, v54, 16\n\t"
        "v_mfma_f32_16x16x32_bf16 v[4:7], v[50:53], v[102:105], v[4:7]\n\t"
        "ds_read_b128 v[32:35], %2 offset:128\n\t"
        "ds_read_b128 v[64:67], %2 offset:128\n\t"
        "ds_read_b128 v[114:117], %3 offset:112\n\t"
        "v_mfma_f32_16x16x32_bf16 v[12:15], v[52:55], v[102:105], v[12:15]\n\t"
        "s_waitcnt lgkmcnt(5)\n\t"
        "v_alignbit_b32 v90, v56, v55, 16\n\t"
        "v_mfma_f32_16x16x32_bf16 v[0:3], v[84:87], v[102:105], v[0:3]\n\t"
        "v_alignbit_b32 v91, v57, v56, 16\n\t"
        "v_alignbit_b32 v92, v58, v57, 16\n\t"
        "v_mfma_f32_16x16x32_bf16 v[8:11], v[86:89], v[102:105], v[8:11]\n\t"
        "v_alignbit_b32 v93, v59, v58, 16\n\t"
        "v_mfma_f32_16x16x32_bf16 v[4:7], v[54:57], v[106:109], v[4:7]\n\t"
        "ds_read_b128 v[36:39], %2 offset:144\n\t"
        "ds_read_b128 v[102:105], %3 offset:128\n\t"
        "v_mfma_f32_16x16x32_bf16 v[12:15], v[56:59], v[106:109], v[12:15]\n\t"
        "s_waitcnt lgkmcnt(5)\n\t"
        "v_alignbit_b32 v94, v60, v59, 16\n\t"
        "v_mfma_f32_16x16x32_bf16 v[0:3], v[88:91], v[106:109], v[0:3]\n\t"
        "v_alignbit_b32 v95, v61, v60, 16\n\t"
        "v_alignbit_b32 v96, v62, v61, 16\n\t"
        "v_mfma_f32_16x16x32_bf16 v[8:11], v[90:93], v[106:109], v[8:11]\n\t"
        "v_alignbit_b32 v97, v63, v62, 16\n\t"
        "v_mfma_f32_16x16x32_bf16 v[4:7], v[58:61], v[110:113], v[4:7]\n\t"
        "ds_read_b128 v[40:43], %2 offset:160\n\t"
        "ds_read_b128 v[106:109], %3 offset:144\n\t"
        "v_mfma_f32_16x16x32_bf16 v[12:15], v[60:63], v[110:113], v[12:15]\n\t"
        "s_waitcnt lgkmcnt(4)\n\t"
        "v_alignbit_b32 v98, v64, v63, 16\n\t"
        "v_mfma_f32_16x16x32_bf16 v[0:3], v[92:95], v[110:113], v[0:3]\n\t"
        "v_alignbit_b32 v99, v65, v64, 16\n\t"
        "v_alignbit_b32 v100, v66, v65, 16\n\t"
        "v_mfma_f32_16x16x32_bf16 v[8:11], v[94:97], v[110:113], v[8:11]\n\t"
        "v_alignbit_b32 v101, v67, v66, 16\n\t"
        "v_mfma_f32_16x16x32_bf16 v[4:7], v[62:65], v[114:117], v[4:7]\n\t"
        "ds_read_b128 v[44:47], %2 offset:176\n\t"
        "ds_read_b128 v[110:113], %3 offset:160\n\t"
        "v_mfma_f32_16x16x32_bf16 v[12:15], v[64:67], v[114:117], v[12:15]\n\t"
        "s_waitcnt lgkmcnt(4)\n\t"
        "v_alignbit_b32 v68, v34, v33, 16\n\t"
        "v_alignbit_b32 v69, v35, v34, 16\n\t"
        "v_alignbit_b32 v70, v36, v35, 16\n\t"
        "v_mfma_f32_16x16x32_bf16 v[0:3], v[96:99], v[114:117], v[0:3]\n\t"
        "v_alignbit_b32 v71, v37, v36, 16\n\t"
        "v_alignbit_b32 v72, v38, v37, 16\n\t"
        "v_mfma_f32_16x16x32_bf16 v[8:11], v[98:101], v[114:117], v[8:11]\n\t"
        "v_alignbit_b32 v73, v39, v38, 16\n\t"
        "v_mfma_f32_16x16x32_bf16 v[4:7], v[34:37], v[102:105], v[4:7]\n\t"
        "ds_read_b128 v[48:51], %2 offset:192\n\t"
        "ds_read_b128 v[114:117], %3 offset:176\n\t"
        "v_mfma_f32_16x16x32_bf16 v[12:15], v[36:39], v[102:105], v[12:15]\n\t"
        "s_waitcnt lgkmcnt(4)\n\t"
        "v_alignbit_b32 v74, v40, v39, 16\n\t"
        "v_mfma_f32_16x16x32_bf16 v[0:3], v[68:71], v[102:105], v[0:3]\n\t"
        "v_alignbit_b32 v75, v41, v40, 16\n\t"
        "v_alignbit_b32 v76, v42, v41, 16\n\t"
        "v_mfma_f32_16x16x32_bf16 v[8:11], v[70:73], v[102:105], v[8:11]\n\t"
        "v_alignbit_b32 v77, v43, v42, 16\n\t"
        "v_mfma_f32_16x16x32_bf16 v[4:7], v[38:41], v[106:109], v[4:7]\n\t"
        "ds_read_b128 v[52:55], %2 offset:208\n\t"
        "ds_read_b128 v[102:105], %3 offset:192\n\t"
        "v_mfma_f32_16x16x32_bf16 v[12:15], v[40:43], v[106:109], v[12:15]\n\t"
        "s_waitcnt lgkmcnt(4)\n\t"
        "v_alignbit_b32 v78, v44, v43, 16\n\t"
        "v_mfma_f32_16x16x32_bf16 v[0:3], v[72:75], v[106:109], v[0:3]\n\t"
        "v_alignbit_b32 v79, v45, v44, 16\n\t"
        "v_alignbit_b32 v80, v46, v45, 16\n\t"
        "v_mfma_f32_16x16x32_bf16 v[8:11], v[74:77], v[106:109], v[8:11]\n\t"
        "v_alignbit_b32 v81, v47, v46, 16\n\t"
        "v_mfma_f32_16x16x32_bf16 v[4:7], v[42:45], v[110:113], v[4:7]\n\t"
        "ds_read_b128 v[56:59], %2 offset:224\n\t"
        "ds_read_b128 v[106:109], %3 offset:208\n\t"
        "v_mfma_f32_16x16x32_bf16 v[12:15], v[44:47], v[110:113], v[12:15]\n\t"
        "s_waitcnt lgkmcnt(4)\n\t"
        "v_alignbit_b32 v82, v48, v47, 16\n\t"
        "v_mfma_f32_16x16x32_bf16 v[0:3], v[76:79], v[110:113], v[0:3]\n\t"
        "v_alignbit_b32 v83, v49, v48, 16\n\t"
        "v_alignbit_b32 v84, v50, v49, 16\n\t"
        "v_mfma_f32_16x16x32_bf16 v[8:11], v[78:81], v[110:113], v[8:11]\n\t"
        "v_alignbit_b32 v85, v51, v50, 16\n\t"
        "v_mfma_f32_16x16x32_bf16 v[4:7], v[46:49], v[114:117], v[4:7]\n\t"
        "ds_read_b128 v[60:63], %2 offset:240\n\t"
        "ds_read_b128 v[110:113], %3 offset:224\n\t"
        "v_mfma_f32_16x16x32_bf16 v[12:15], v[48:51], v[114:117], v[12:15]\n\t"
        "s_waitcnt lgkmcnt(4)\n\t"
        "v_alignbit_b32 v86, v52, v51, 16\n\t"
        "v_mfma_f32_16x16x32_bf16 v[0:3], v[80:83], v[114:117], v[0:3]\n\t"
        "v_alignbit_b32 v87, v53, v52, 16\n\t"
        "v_alignbit_b32 v88, v54, v53, 16\n\t"
        "v_mfma_f32_16x16x32_bf16 v[8:11], v[82:85], v[114:117], v[8:11]\n\t"
        "v_alignbit_b32 v89, v55, v54, 16\n\t"
        "v_mfma_f32_16x16x32_bf16 v[4:7], v[50:53], v[102:105], v[4:7]\n\t"
        "ds_read_b128 v[32:35], %2 offset:256\n\t"
        "ds_read_b128 v[64:67], %2 offset:256\n\t"
        "ds_read_b128 v[114:117], %3 offset:240\n\t"
        "v_mfma_f32_16x16x32_bf16 v[12:15], v[52:55], v[102:105], v[12:15]\n\t"
        "s_waitcnt lgkmcnt(5)\n\t"
        "v_alignbit_b32 v90, v56, v55, 16\n\t"
        "v_mfma_f32_16x16x32_bf16 v[0:3], v[84:87], v[102:105], v[0:3]\n\t"
        "v_alignbit_b32 v91, v57, v56, 16\n\t"
        "v_alignbit_b32 v92, v58, v57, 16\n\t"
        "v_mfma_f32_16x16x32_bf16 v[8:11], v[86:89], v[102:105], v[8:11]\n\t"
        "v_alignbit_b32 v93, v59, v58, 16\n\t"
        "v_mfma_f32_16x16x32_bf16 v[4:7], v[54:57], v[106:109], v[4:7]\n\t"
        "ds_read_b128 v[36:39], %2 offset:272\n\t"
        "ds_read_b128 v[102:105], %3 offset:288\n\t"
        "v_mfma_f32_16x16x32_bf16 v[12:15], v[56:59], v[106:109], v[12:15]\n\t"
        "s_waitcnt lgkmcnt(5)\n\t"
        "v_alignbit_b32 v94, v60, v59, 16\n\t"
        "v_mfma_f32_16x16x32_bf16 v[0:3], v[88:91], v[106:109], v[0:3]\n\t"
        "v_alignbit_b32 v95, v61, v60, 16\n\t"
        "v_alignbit_b32 v96, v62, v61, 16\n\t"
        "v_mfma_f32_16x16x32_bf16 v[8:11], v[90:93], v[106:109], v[8:11]\n\t"
        "v_alignbit_b32 v97, v63, v62, 16\n\t"
        "v_mfma_f32_16x16x32_bf16 v[4:7], v[58:61], v[110:113], v[4:7]\n\t"
        "ds_read_b128 v[40:43], %2 offset:288\n\t"
        "ds_read_b128 v[106:109], %3 offset:304\n\t"
        "v_mfma_f32_16x16x32_bf16 v[12:15], v[60:63], v[110:113], v[12:15]\n\t"
        "s_waitcnt lgkmcnt(4)\n\t"
        "v_alignbit_b32 v98, v64, v63, 16\n\t"
        "v_mfma_f32_16x16x32_bf16 v[0:3], v[92:95], v[110:113], v[0:3]\n\t"
        "v_alignbit_b32 v99, v65, v64, 16\n\t"
        "v_alignbit_b32 v100, v66, v65, 16\n\t"
        "v_mfma_f32_16x16x32_bf16 v[8:11], v[94:97], v[110:113], v[8:11]\n\t"
        "v_alignbit_b32 v101, v67, v66, 16\n\t"
        "v_mfma_f32_16x16x32_bf16 v[4:7], v[62:65], v[114:117], v[4:7]\n\t"
        "ds_read_b128 v[44:47], %2 offset:304\n\t"
        "ds_read_b128 v[110:113], %3 offset:320\n\t"
        "v_mfma_f32_16x16x32_bf16 v[12:15], v[64:67], v[114:117], v[12:15]\n\t"
        "s_waitcnt lgkmcnt(4)\n\t"
        "v_alignbit_b32 v68, v34, v33, 16\n\t"
        "v_alignbit_b32 v69, v35, v34, 16\n\t"
        "v_alignbit_b32 v70, v36, v35, 16\n\t"
        "v_mfma_f32_16x16x32_bf16 v[0:3], v[96:99], v[114:117], v[0:3]\n\t"
        "v_alignbit_b32 v71, v37, v36, 16\n\t"
        "v_alignbit_b32 v72, v38, v37, 16\n\t"
        "v_mfma_f32_16x16x32_bf16 v[8:11], v[98:101], v[114:117], v[8:11]\n\t"
        "v_alignbit_b32 v73, v39, v38, 16\n\t"
        "v_mfma_f32_16x16x32_bf16 v[4:7], v[34:37], v[102:105], v[4:7]\n\t"
        "ds_read_b128 v[48:51], %2 offset:320\n\t"
        "ds_read_b128 v[114:117], %3 offset:336\n\t"
        "v_mfma_f32_16x16x32_bf16 v[12:15], v[36:39], v[102:105], v[12:15]\n\t"
        "s_waitcnt lgkmcnt(4)\n\t"
        "v_alignbit_b32 v74, v40, v39, 16\n\t"
        "v_mfma_f32_16x16x32_bf16 v[0:3], v[68:71], v[102:105], v[0:3]\n\t"
        "v_alignbit_b32 v75, v41, v40, 16\n\t"
        "v_alignbit_b32 v76, v42, v41, 16\n\t"
        "v_mfma_f32_16x16x32_bf16 v[8:11], v[70:73], v[102:105], v[8:11]\n\t"
        "v_alignbit_b32 v77, v43, v42, 16\n\t"
        "v_mfma_f32_16x16x32_bf16 v[4:7], v[38:41], v[106:109], v[4:7]\n\t"
        "ds_read_b128 v[52:55], %2 offset:336\n\t"
        "ds_read_b128 v[102:105], %3 offset:352\n\t"
        "v_mfma_f32_16x16x32_bf16 v[12:15], v[40:43], v[106:109], v[12:15]\n\t"
        "s_waitcnt lgkmcnt(4)\n\t"
        "v_alignbit_b32 v78, v44, v43, 16\n\t"
        "v_mfma_f32_16x16x32_bf16 v[0:3], v[72:75], v[106:109], v[0:3]\n\t"
        "v_alignbit_b32 v79, v45, v44, 16\n\t"
        "v_alignbit_b32 v80, v46, v45, 16\n\t"
        "v_mfma_f32_16x16x32_bf16 v[8:11], v[74:77], v[106:109], v[8:11]\n\t"
        "v_alignbit_b32 v81, v47, v46, 16\n\t"
        "v_mfma_f32_16x16x32_bf16 v[4:7], v[42:45], v[110:113], v[4:7]\n\t"
        "ds_read_b128 v[56:59], %2 offset:352\n\t"
        "ds_read_b128 v[106:109], %3 offset:368\n\t"
        "v_mfma_f32_16x16x32_bf16 v[12:15], v[44:47], v[110:113], v[12:15]\n\t"
        "s_waitcnt lgkmcnt(4)\n\t"
        "v_alignbit_b32 v82, v48, v47, 16\n\t"
        "v_mfma_f32_16x16x32_bf16 v[0:3], v[76:79], v[110:113], v[0:3]\n\t"
        "v_alignbit_b32 v83, v49, v48, 16\n\t"
        "v_alignbit_b32 v84, v50, v49, 16\n\t"
        "v_mfma_f32_16x16x32_bf16 v[8:11], v[78:81], v[110:113], v[8:11]\n\t"
        "v_alignbit_b32 v85, v51, v50, 16\n\t"
        "v_mfma_f32_16x16x32_bf16 v[4:7], v[46:49], v[114:117], v[4:7]\n\t"
        "ds_read_b128 v[60:63], %2 offset:368\n\t"
        "ds_read_b128 v[110:113], %3 offset:384\n\t"
        "v_mfma_f32_16x16x32_bf16 v[12:15], v[48:51], v[114:117], v[12:15]\n\t"
        "s_waitcnt lgkmcnt(4)\n\t"
        "v_alignbit_b32 v86, v52, v51, 16\n\t"
        "v_mfma_f32_16x16x32_bf16 v[0:3], v[80:83], v[114:117], v[0:3]\n\t"
        "v_alignbit_b32 v87, v53, v52, 16\n\t"
        "v_alignbit_b32 v88, v54, v53, 16\n\t"
        "v_mfma_f32_16x16x32_bf16 v[8:11], v[82:85], v[114:117], v[8:11]\n\t"
        "v_alignbit_b32 v89, v55, v54, 16\n\t"
        "v_mfma_f32_16x16x32_bf16 v[4:7], v[50:53], v[102:105], v[4:7]\n\t"
        "ds_read_b128 v[32:35], %2 offset:384\n\t"
        "ds_read_b128 v[64:67], %2 offset:384\n\t"
        "ds_read_b128 v[114:117], %3 offset:400\n\t"
        "v_mfma_f32_16x16x32_bf16 v[12:15], v[52:55], v[102:105], v[12:15]\n\t"
        "s_waitcnt lgkmcnt(5)\n\t"
        "v_alignbit_b32 v90, v56, v55, 16\n\t"
        "v_mfma_f32_16x16x32_bf16 v[0:3], v[84:87], v[102:105], v[0:3]\n\t"
        "v_alignbit_b32 v91, v57, v56, 16\n\t"
        "v_alignbit_b32 v92, v58, v57, 16\n\t"
        "v_mfma_f32_16x16x32_bf16 v[8:11], v[86:89], v[102:105], v[8:11]\n\t"
        "v_alignbit_b32 v93, v59, v58, 16\n\t"
        "v_mfma_f32_16x16x32_bf16 v[4:7], v[54:57], v[106:109], v[4:7]\n\t"
        "ds_read_b128 v[36:39], %2 offset:400\n\t"
        "ds_read_b128 v[102:105], %3 offset:416\n\t"
        "v_mfma_f32_16x16x32_bf16 v[12:15], v[56:59], v[106:109], v[12:15]\n\t"
        "s_waitcnt lgkmcnt(5)\n\t"
        "v_alignbit_b32 v94, v60, v59, 16\n\t"
        "v_mfma_f32_16x16x32_bf16 v[0:3], v[88:91], v[106:109], v[0:3]\n\t"
        "v_alignbit_b32 v95, v61, v60, 16\n\t"
        "v_alignbit_b32 v96, v62, v61, 16\n\t"
        "v_mfma_f32_16x16x32_bf16 v[8:11], v[90:93], v[106:109], v[8:11]\n\t"
        "v_alignbit_b32 v97, v63, v62, 16\n\t"
        "v_mfma_f32_16x16x32_bf16 v[4:7], v[58:61], v[110:113], v[4:7]\n\t"
        "ds_read_b128 v[40:43], %2 offset:416\n\t"
        "ds_read_b128 v[106:109], %3 offset:432\n\t"
        "v_mfma_f32_16x16x32_bf16 v[12:15], v[60:63], v[110:113], v[12:15]\n\t"
        "s_waitcnt lgkmcnt(4)\n\t"
        "v_alignbit_b32 v98, v64, v63, 16\n\t"
        "v_mfma_f32_16x16x32_bf16 v[0:3], v[92:95], v[110:113], v[0:3]\n\t"
        "v_alignbit_b32 v99, v65, v64, 16\n\t"
        "v_alignbit_b32 v100, v66, v65, 16\n\t"
        "v_mfma_f32_16x16x32_bf16 v[8:11], v[94:97], v[110:113], v[8:11]\n\t"
        "v_alignbit_b32 v101, v67, v66, 16\n\t"
        "v_mfma_f32_16x16x32_bf16 v[4:7], v[62:65], v[114:117], v[4:7]\n\t"
        "ds_read_b128 v[44:47], %2 offset:432\n\t"
        "ds_read_b128 v[110:113], %3 offset:448\n\t"
        "v_mfma_f32_16x16x32_bf16 v[12:15], v[64:67], v[114:117], v[12:15]\n\t"
        "s_waitcnt lgkmcnt(4)\n\t"
        "v_alignbit_b32 v68, v34, v33, 16\n\t"
        "v_alignbit_b32 v69, v35, v34, 16\n\t"
        "v_alignbit_b32 v70, v36, v35, 16\n\t"
        "v_mfma_f32_16x16x32_bf16 v[0:3], v[96:99], v[114:117], v[0:3]\n\t"
        "v_alignbit_b32 v71, v37, v36, 16\n\t"
        "v_alignbit_b32 v72, v38, v37, 16\n\t"
        "v_mfma_f32_16x16x32_bf16 v[8:11], v[98:101], v[114:117], v[8:11]\n\t"
        "v_alignbit_b32 v73, v39, v38, 16\n\t"
        "v_mfma_f32_16x16x32_bf16 v[4:7], v[34:37], v[102:105], v[4:7]\n\t"
        "ds_read_b128 v[48:51], %2 offset:448\n\t"
        "ds_read_b128 v[114:117], %3 offset:464\n\t"
        "v_mfma_f32_16x16x32_bf16 v[12:15], v[36:39], v[102:105], v[12:15]\n\t"
        "s_waitcnt lgkmcnt(4)\n\t"
        "v_alignbit_b32 v74, v40, v39, 16\n\t"
        "v_mfma_f32_16x16x32_bf16 v[0:3], v[68:71], v[102:105], v[0:3]\n\t"
        "v_alignbit_b32 v75, v41, v40, 16\n\t"
        "v_alignbit_b32 v76, v42, v41, 16\n\t"
        "v_mfma_f32_16x16x32_bf16 v[8:11], v[70:73], v[102:105], v[8:11]\n\t"
        "v_alignbit_b32 v77, v43, v42, 16\n\t"
        "v_mfma_f32_16x16x32_bf16 v[4:7], v[38:41], v[106:109], v[4:7]\n\t"
        "ds_read_b128 v[52:55], %2 offset:464\n\t"
        "ds_read_b128 v[102:105], %3 offset:480\n\t"
        "v_mfma_f32_16x16x32_bf16 v[12:15], v[40:43], v[106:109], v[12:15]\n\t"
        "s_waitcnt lgkmcnt(4)\n\t"
        "v_alignbit_b32 v78, v44, v43, 16\n\t"
        "v_mfma_f32_16x16x32_bf16 v[0:3], v[72:75], v[106:109], v[0:3]\n\t"
        "v_alignbit_b32 v79, v45, v44, 16\n\t"
        "v_alignbit_b32 v80, v46, v45, 16\n\t"
        "v_mfma_f32_16x16x32_bf16 v[8:11], v[74:77], v[106:109], v[8:11]\n\t"
        "v_alignbit_b32 v81, v47, v46, 16\n\t"
        "v_mfma_f32_16x16x32_bf16 v[4:7], v[42:45], v[110:113], v[4:7]\n\t"
        "ds_read_b128 v[56:59], %2 offset:480\n\t"
        "ds_read_b128 v[106:109], %3 offset:496\n\t"
        "v_mfma_f32_16x16x32_bf16 v[12:15], v[44:47], v[110:113], v[12:15]\n\t"
        "s_waitcnt lgkmcnt(4)\n\t"
        "v_alignbit_b32 v82, v48, v47, 16\n\t"
        "v_mfma_f32_16x16x32_bf16 v[0:3], v[76:79], v[110:113], v[0:3]\n\t"
        "v_alignbit_b32 v83, v49, v48, 16\n\t"
        "v_alignbit_b32 v84, v50, v49, 16\n\t"
        "v_mfma_f32_16x16x32_bf16 v[8:11], v[78:81], v[110:113], v[8:11]\n\t"
        "v_alignbit_b32 v85, v51, v50, 16\n\t"
        "v_mfma_f32_16x16x32_bf16 v[4:7], v[46:49], v[114:117], v[4:7]\n\t"
        "ds_read_b128 v[60:63], %2 offset:496\n\t"
        "ds_read_b128 v[110:113], %3 offset:512\n\t"
        "v_mfma_f32_16x16x32_bf16 v[12:15], v[48:51], v[114:117], v[12:15]\n\t"
        "s_waitcnt lgkmcnt(4)\n\t"
        "v_alignbit_b32 v86, v52, v51, 16\n\t"
        "v_mfma_f32_16x16x32_bf16 v[0:3], v[80:83], v[114:117], v[0:3]\n\t"
        "v_alignbit_b32 v87, v53, v52, 16\n\t"
        "v_alignbit_b32 v88, v54, v53, 16\n\t"
        "v_mfma_f32_16x16x32_bf16 v[8:11], v[82:85], v[114:117], v[8:11]\n\t"
        "v_alignbit_b32 v89, v55, v54, 16\n\t"
        "v_mfma_f32_16x16x32_bf16 v[4:7], v[50:53], v[102:105], v[4:7]\n\t"
        "ds_read_b128 v[32:35], %4 offset:0\n\t"
        "ds_read_b128 v[64:67], %2 offset:512\n\t"
        "ds_read_b128 v[114:117], %3 offset:528\n\t"
        "v_mfma_f32_16x16x32_bf16 v[12:15], v[52:55], v[102:105], v[12:15]\n\t"
        "s_waitcnt lgkmcnt(5)\n\t"
        "v_alignbit_b32 v90, v56, v55, 16\n\t"
        "v_mfma_f32_16x16x32_bf16 v[0:3], v[84:87], v[102:105], v[0:3]\n\t"
        "v_alignbit_b32 v91, v57, v56, 16\n\t"
        "v_alignbit_b32 v92, v58, v57, 16\n\t"
        "v_mfma_f32_16x16x32_bf16 v[8:11], v[86:89], v[102:105], v[8:11]\n\t"
        "v_alignbit_b32 v93, v59, v58, 16\n\t"
        "v_mfma_f32_16x16x32_bf16 v[4:7], v[54:57], v[106:109], v[4:7]\n\t"
        "ds_read_b128 v[36:39], %4 offset:16\n\t"
        "ds_read_b128 v[102:105], %5 offset:0\n\t"
        "v_mfma_f32_16x16x32_bf16 v[12:15], v[56:59], v[106:109], v[12:15]\n\t"
        "s_waitcnt lgkmcnt(5)\n\t"
        "v_alignbit_b32 v94, v60, v59, 16\n\t"
        "v_mfma_f32_16x16x32_bf16 v[0:3], v[88:91], v[106:109], v[0:3]\n\t"
        "v_alignbit_b32 v95, v61, v60, 16\n\t"
        "v_alignbit_b32 v96, v62, v61, 16\n\t"
        "v_mfma_f32_16x16x32_bf16 v[8:11], v[90:93], v[106:109], v[8:11]\n\t"
        "v_alignbit_b32 v97, v63, v62, 16\n\t"
        "v_mfma_f32_16x16x32_bf16 v[4:7], v[58:61], v[110:113], v[4:7]\n\t"
        "ds_read_b128 v[40:43], %4 offset:32\n\t"
        "ds_read_b128 v[106:109], %5 offset:16\n\t"
        "v_mfma_f32_16x16x32_bf16 v[12:15], v[60:63], v[110:113], v[12:15]\n\t"
        "s_waitcnt lgkmcnt(4)\n\t"
        "v_alignbit_b32 v98, v64, v63, 16\n\t"
        "v_mfma_f32_16x16x32_bf16 v[0:3], v[92:95], v[110:113], v[0:3]\n\t"
        "v_alignbit_b32 v99, v65, v64, 16\n\t"
        "v_alignbit_b32 v100, v66, v65, 16\n\t"
        "v_mfma_f32_16x16x32_bf16 v[8:11], v[94:97], v[110:113], v[8:11]\n\t"
        "v_alignbit_b32 v101, v67, v66, 16\n\t"
        "v_mfma_f32_16x16x32_bf16 v[4:7], v[62:65], v[114:117], v[4:7]\n\t"
        "ds_read_b128 v[44:47], %4 offset:48\n\t"
        "ds_read_b128 v[110:113], %5 offset:32\n\t"
        "v_mfma_f32_16x16x32_bf16 v[12:15], v[64:67], v[114:117], v[12:15]\n\t"
        "s_waitcnt lgkmcnt(4)\n\t"
        "v_alignbit_b32 v68, v34, v33, 16\n\t"
        "v_alignbit_b32 v69, v35, v34, 16\n\t"
        "v_alignbit_b32 v70, v36, v35, 16\n\t"
        "v_mfma_f32_16x16x32_bf16 v[0:3], v[96:99], v[114:117], v[0:3]\n\t"
        "v_alignbit_b32 v71, v37, v36, 16\n\t"
        "v_alignbit_b32 v72, v38, v37, 16\n\t"
        "v_mfma_f32_16x16x32_bf16 v[8:11], v[98:101], v[114:117], v[8:11]\n\t"
        "v_alignbit_b32 v73, v39, v38, 16\n\t"
        "s_cmp_eq_u32 %6, 2\n\t"
        "s_cbranch_scc1 LCE_%=\n\t"
        "v_mfma_f32_16x16x32_bf16 v[20:23], v[34:37], v[102:105], 0\n\t"
        "ds_read_b128 v[48:51], %4 offset:64\n\t"
        "ds_read_b128 v[114:117], %5 offset:48\n\t"
        "v_mfma_f32_16x16x32_bf16 v[28:31], v[36:39], v[102:105], 0\n\t"
        "s_waitcnt lgkmcnt(4)\n\t"
        "v_alignbit_b32 v74, v40, v39, 16\n\t"
        "v_mfma_f32_16x16x32_bf16 v[16:19], v[68:71], v[102:105], 0\n\t"
        "v_alignbit_b32 v75, v41, v40, 16\n\t"
        "v_alignbit_b32 v76, v42, v41, 16\n\t"
        "v_mfma_f32_16x16x32_bf16 v[24:27], v[70:73], v[102:105], 0\n\t"
        "v_alignbit_b32 v77, v43, v42, 16\n\t"
        "v_mfma_f32_16x16x32_bf16 v[20:23], v[38:41], v[106:109], v[20:23]\n\t"
        "ds_read_b128 v[52:55], %4 offset:80\n\t"
        "ds_read_b128 v[102:105], %5 offset:64\n\t"
        "v_mfma_f32_16x16x32_bf16 v[28:31], v[40:43], v[106:109], v[28:31]\n\t"
        "s_waitcnt lgkmcnt(4)\n\t"
        "v_alignbit_b32 v78, v44, v43, 16\n\t"
        "v_mfma_f32_16x16x32_bf16 v[16:19], v[72:75], v[106:109], v[16:19]\n\t"
        "v_alignbit_b32 v79, v45, v44, 16\n\t"
        "v_alignbit_b32 v80, v46, v45, 16\n\t"
        "v_mfma_f32_16x16x32_bf16 v[24:27], v[74:77], v[106:109], v[24:27]\n\t"
        "v_alignbit_b32 v81, v47, v46, 16\n\t"
        "v_mfma_f32_16x16x32_bf16 v[20:23], v[42:45], v[110:113], v[20:23]\n\t"
        "ds_read_b128 v[56:59], %4 offset:96\n\t"
        "ds_read_b128 v[106:109], %5 offset:80\n\t"
        "v_mfma_f32_16x16x32_bf16 v[28:31], v[44:47], v[110:113], v[28:31]\n\t"
        "s_waitcnt lgkmcnt(4)\n\t"
        "v_alignbit_b32 v82, v48, v47, 16\n\t"
        "v_mfma_f32_16x16x32_bf16 v[16:19], v[76:79], v[110:113], v[16:19]\n\t"
        "v_alignbit_b32 v83, v49, v48, 16\n\t"
        "v_alignbit_b32 v84, v50, v49, 16\n\t"
        "v_mfma_f32_16x16x32_bf16 v[24:27], v[78:81], v[110:113], v[24:27]\n\t"
        "v_alignbit_b32 v85, v51, v50, 16\n\t"
        "v_mfma_f32_16x16x32_bf16 v[20:23], v[46:49], v[114:117], v[20:23]\n\t"
        "ds_read_b128 v[60:63], %4 offset:112\n\t"
        "ds_read_b128 v[110:113], %5 offset:96\n\t"
        "v_mfma_f32_16x16x32_bf16 v[28:31], v[48:51], v[114:117], v[28:31]\n\t"
        "s_waitcnt lgkmcnt(4)\n\t"
        "v_alignbit_b32 v86, v52, v51, 16\n\t"
        "v_mfma_f32_16x16x32_bf16 v[16:19], v[80:83], v[114:117], v[16:19]\n\t"
        "v_alignbit_b32 v87, v53, v52, 16\n\t"
        "v_alignbit_b32 v88, v54, v53, 16\n\t"
        "v_mfma_f32_16x16x32_bf16 v[24:27], v[82:85], v[114:117], v[24:27]\n\t"
        "v_alignbit_b32 v89, v55, v54, 16\n\t"
        "v_mfma_f32_16x16x32_bf16 v[20:23], v[50:53], v[102:105], v[20:23]\n\t"
        "ds_read_b128 v[32:35], %4 offset:128\n\t"
        "ds_read_b128 v[64:67], %4 offset:128\n\t"
        "ds_read_b128 v[114:117], %5 offset:112\n\t"
        "v_mfma_f32_16x16x32_bf16 v[28:31], v[52:55], v[102:105], v[28:31]\n\t"
        "s_waitcnt lgkmcnt(5)\n\t"
        "v_alignbit_b32 v90, v56, v55, 16\n\t"
        "v_mfma_f32_16x16x32_bf16 v[16:19], v[84:87], v[102:105], v[16:19]\n\t"
        "v_alignbit_b32 v91, v57, v56, 16\n\t"
        "v_alignbit_b32 v92, v58, v57, 16\n\t"
        "v_mfma_f32_16x16x32_bf16 v[24:27], v[86:89], v[102:105], v[24:27]\n\t"
        "v_alignbit_b32 v93, v59, v58, 16\n\t"
        "v_mfma_f32_16x16x32_bf16 v[20:23], v[54:57], v[106:109], v[20:23]\n\t"
        "ds_read_b128 v[36:39], %4 offset:144\n\t"
        "ds_read_b128 v[102:105], %5 offset:128\n\t"
        "v_mfma_f32_16x16x32_bf16 v[28:31], v[56:59], v[106:109], v[28:31]\n\t"
        "s_waitcnt lgkmcnt(5)\n\t"
        "v_alignbit_b32 v94, v60, v59, 16\n\t"
        "v_mfma_f32_16x16x32_bf16 v[16:19], v[88:91], v[106:109], v[16:19]\n\t"
        "v_alignbit_b32 v95, v61, v60, 16\n\t"
        "v_alignbit_b32 v96, v62, v61, 16\n\t"
        "v_mfma_f32_16x16x32_bf16 v[24:27], v[90:93], v[106:109], v[24:27]\n\t"
        "v_alignbit_b32 v97, v63, v62, 16\n\t"
        "v_mfma_f32_16x16x32_bf16 v[20:23], v[58:61], v[110:113], v[20:23]\n\t"
        "ds_read_b128 v[40:43], %4 offset:160\n\t"
        "ds_read_b128 v[106:109], %5 offset:144\n\t"
        "v_mfma_f32_16x16x32_bf16 v[28:31], v[60:63], v[110:113], v[28:31]\n\t"
        "s_waitcnt lgkmcnt(4)\n\t"
        "v_alignbit_b32 v98, v64, v63, 16\n\t"
        "v_mfma_f32_16x16x32_bf16 v[16:19], v[92:95], v[110:113], v[16:19]\n\t"
        "v_alignbit_b32 v99, v65, v64, 16\n\t"
        "v_alignbit_b32 v100, v66, v65, 16\n\t"
        "v_mfma_f32_16x16x32_bf16 v[24:27], v[94:97], v[110:113], v[24:27]\n\t"
        "v_alignbit_b32 v101, v67, v66, 16\n\t"
        "v_mfma_f32_16x16x32_bf16 v[20:23], v[62:65], v[114:117], v[20:23]\n\t"
        "ds_read_b128 v[44:47], %4 offset:176\n\t"
        "ds_read_b128 v[110:113], %5 offset:160\n\t"
        "v_mfma_f32_16x16x32_bf16 v[28:31], v[64:67], v[114:117], v[28:31]\n\t"
        "s_waitcnt lgkmcnt(4)\n\t"
        "v_alignbit_b32 v68, v34, v33, 16\n\t"
        "v_alignbit_b32 v69, v35, v34, 16\n\t"
        "v_alignbit_b32 v70, v36, v35, 16\n\t"
        "v_mfma_f32_16x16x32_bf16 v[16:19], v[96:99], v[114:117], v[16:19]\n\t"
        "v_alignbit_b32 v71, v37, v36, 16\n\t"
        "v_alignbit_b32 v72, v38, v37, 16\n\t"
        "v_mfma_f32_16x16x32_bf16 v[24:27], v[98:101], v[114:117], v[24:27]\n\t"
        "v_alignbit_b32 v73, v39, v38, 16\n\t"
        "v_mfma_f32_16x16x32_bf16 v[20:23], v[34:37], v[102:105], v[20:23]\n\t"
        "ds_read_b128 v[48:51], %4 offset:192\n\t"
        "ds_read_b128 v[114:117], %5 offset:176\n\t"
        "v_mfma_f32_16x16x32_bf16 v[28:31], v[36:39], v[102:105], v[28:31]\n\t"
        "s_waitcnt lgkmcnt(4)\n\t"
        "v_alignbit_b32 v74, v40, v39, 16\n\t"
        "v_mfma_f32_16x16x32_bf16 v[16:19], v[68:71], v[102:105], v[16:19]\n\t"
        "v_alignbit_b32 v75, v41, v40, 16\n\t"
        "v_alignbit_b32 v76, v42, v41, 16\n\t"
        "v_mfma_f32_16x16x32_bf16 v[24:27], v[70:73], v[102:105], v[24:27]\n\t"
        "v_alignbit_b32 v77, v43, v42, 16\n\t"
        "v_mfma_f32_16x16x32_bf16 v[20:23], v[38:41], v[106:109], v[20:23]\n\t"
        "ds_read_b128 v[52:55], %4 offset:208\n\t"
        "ds_read_b128 v[102:105], %5 offset:192\n\t"
        "v_mfma_f32_16x16x32_bf16 v[28:31], v[40:43], v[106:109], v[28:31]\n\t"
        "s_waitcnt lgkmcnt(4)\n\t"
        "v_alignbit_b32 v78, v44, v43, 16\n\t"
        "v_mfma_f32_16x16x32_bf16 v[16:19], v[72:75], v[106:109], v[16:19]\n\t"
        "v_alignbit_b32 v79, v45, v44, 16\n\t"
        "v_alignbit_b32 v80, v46, v45, 16\n\t"
        "v_mfma_f32_16x16x32_bf16 v[24:27], v[74:77], v[106:109], v[24:27]\n\t"
        "v_alignbit_b32 v81, v47, v46, 16\n\t"
        "v_mfma_f32_16x16x32_bf16 v[20:23], v[42:45], v[110:113], v[20:23]\n\t"
        "ds_read_b128 v[56:59], %4 offset:224\n\t"
        "ds_read_b128 v[106:109], %5 offset:208\n\t"
        "v_mfma_f32_16x16x32_bf16 v[28:31], v[44:47], v[110:113], v[28:31]\n\t"
        "s_waitcnt lgkmcnt(4)\n\t"
        "v_alignbit_b32 v82, v48, v47, 16\n\t"
        "v_mfma_f32_16x16x32_bf16 v[16:19], v[76:79], v[110:113], v[16:19]\n\t"
        "v_alignbit_b32 v83, v49, v48, 16\n\t"
        "v_alignbit_b32 v84, v50, v49, 16\n\t"
        "v_mfma_f32_16x16x32_bf16 v[24:27], v[78:81], v[110:113], v[24:27]\n\t"
        "v_alignbit_b32 v85, v51, v50, 16\n\t"
        "v_mfma_f32_16x16x32_bf16 v[20:23], v[46:49], v[114:117], v[20:23]\n\t"
        "ds_read_b128 v[60:63], %4 offset:240\n\t"
        "ds_read_b128 v[110:113], %5 offset:224\n\t"
        "v_mfma_f32_16x16x32_bf16 v[28:31], v[48:51], v[114:117], v[28:31]\n\t"
        "s_waitcnt lgkmcnt(4)\n\t"
        "v_alignbit_b32 v86, v52, v51, 16\n\t"
        "v_mfma_f32_16x16x32_bf16 v[16:19], v[80:83], v[114:117], v[16:19]\n\t"
        "v_alignbit_b32 v87, v53, v52, 16\n\t"
        "v_alignbit_b32 v88, v54, v53, 16\n\t"
        "v_mfma_f32_16x16x32_bf16 v[24:27], v[82:85], v[114:117], v[24:27]\n\t"
        "v_alignbit_b32 v89, v55, v54, 16\n\t"
        "v_mfma_f32_16x16x32_bf16 v[20:23], v[50:53], v[102:105], v[20:23]\n\t"
        "ds_read_b128 v[32:35], %4 offset:256\n\t"
        "ds_read_b128 v[64:67], %4 offset:256\n\t"
        "ds_read_b128 v[114:117], %5 offset:240\n\t"
        "v_mfma_f32_16x16x32_bf16 v[28:31], v[52:55], v[102:105], v[28:31]\n\t"
        "s_waitcnt lgkmcnt(5)\n\t"
        "v_alignbit_b32 v90, v56, v55, 16\n\t"
        "v_mfma_f32_16x16x32_bf16 v[16:19], v[84:87], v[102:105], v[16:19]\n\t"
        "v_alignbit_b32 v91, v57, v56, 16\n\t"
        "v_alignbit_b32 v92, v58, v57, 16\n\t"
        "v_mfma_f32_16x16x32_bf16 v[24:27], v[86:89], v[102:105], v[24:27]\n\t"
        "v_alignbit_b32 v93, v59, v58, 16\n\t"
        "v_mfma_f32_16x16x32_bf16 v[20:23], v[54:57], v[106:109], v[20:23]\n\t"
        "ds_read_b128 v[36:39], %4 offset:272\n\t"
        "ds_read_b128 v[102:105], %5 offset:288\n\t"
        "v_mfma_f32_16x16x32_bf16 v[28:31], v[56:59], v[106:109], v[28:31]\n\t"
        "s_waitcnt lgkmcnt(5)\n\t"
        "v_alignbit_b32 v94, v60, v59, 16\n\t"
        "v_mfma_f32_16x16x32_bf16 v[16:19], v[88:91], v[106:109], v[16:19]\n\t"
        "v_alignbit_b32 v95, v61, v60, 16\n\t"
        "v_alignbit_b32 v96, v62, v61, 16\n\t"
        "v_mfma_f32_16x16x32_bf16 v[24:27], v[90:93], v[106:109], v[24:27]\n\t"
        "v_alignbit_b32 v97, v63, v62, 16\n\t"
        "v_mfma_f32_16x16x32_bf16 v[20:23], v[58:61], v[110:113], v[20:23]\n\t"
        "ds_read_b128 v[40:43], %4 offset:288\n\t"
        "ds_read_b128 v[106:109], %5 offset:304\n\t"
        "v_mfma_f32_16x16x32_bf16 v[28:31], v[60:63], v[110:113], v[28:31]\n\t"
        "s_waitcnt lgkmcnt(4)\n\t"
        "v_alignbit_b32 v98, v64, v63, 16\n\t"
        "v_mfma_f32_16x16x32_bf16 v[16:19], v[92:95], v[110:113], v[16:19]\n\t"
        "v_alignbit_b32 v99, v65, v64, 16\n\t"
        "v_alignbit_b32 v100, v66, v65, 16\n\t"
        "v_mfma_f32_16x16x32_bf16 v[24:27], v[94:97], v[110:113], v[24:27]\n\t"
        "v_alignbit_b32 v101, v67, v66, 16\n\t"
        "v_mfma_f32_16x16x32_bf16 v[20:23], v[62:65], v[114:117], v[20:23]\n\t"
        "ds_read_b128 v[44:47], %4 offset:304\n\t"
        "ds_read_b128 v[110:113], %5 offset:320\n\t"
        "v_mfma_f32_16x16x32_bf16 v[28:31], v[64:67], v[114:117], v[28:31]\n\t"
        "s_waitcnt lgkmcnt(4)\n\t"
        "v_alignbit_b32 v68, v34, v33, 16\n\t"
        "v_alignbit_b32 v69, v35, v34, 16\n\t"
        "v_alignbit_b32 v70, v36, v35, 16\n\t"
        "v_mfma_f32_16x16x32_bf16 v[16:19], v[96:99], v[114:117], v[16:19]\n\t"
        "v_alignbit_b32 v71, v37, v36, 16\n\t"
        "v_alignbit_b32 v72, v38, v37, 16\n\t"
        "v_mfma_f32_16x16x32_bf16 v[24:27], v[98:101], v[114:117], v[24:27]\n\t"
        "v_alignbit_b32 v73, v39, v38, 16\n\t"
        "v_mfma_f32_16x16x32_bf16 v[20:23], v[34:37], v[102:105], v[20:23]\n\t"
        "ds_read_b128 v[48:51], %4 offset:320\n\t"
        "ds_read_b128 v[114:117], %5 offset:336\n\t"
        "v_mfma_f32_16x16x32_bf16 v[28:31], v[36:39], v[102:105], v[28:31]\n\t"
        "s_waitcnt lgkmcnt(4)\n\t"
        "v_alignbit_b32 v74, v40, v39, 16\n\t"
        "v_mfma_f32_16x16x32_bf16 v[16:19], v[68:71], v[102:105], v[16:19]\n\t"
        "v_alignbit_b32 v75, v41, v40, 16\n\t"
        "v_alignbit_b32 v76, v42, v41, 16\n\t"
        "v_mfma_f32_16x16x32_bf16 v[24:27], v[70:73], v[102:105], v[24:27]\n\t"
        "v_alignbit_b32 v77, v43, v42, 16\n\t"
        "v_mfma_f32_16x16x32_bf16 v[20:23], v[38:41], v[106:109], v[20:23]\n\t"
        "ds_read_b128 v[52:55], %4 offset:336\n\t"
        "ds_read_b128 v[102:105], %5 offset:352\n\t"
        "v_mfma_f32_16x16x32_bf16 v[28:31], v[40:43], v[106:109], v[28:31]\n\t"
        "s_waitcnt lgkmcnt(4)\n\t"
        "v_alignbit_b32 v78, v44, v43, 16\n\t"
        "v_mfma_f32_16x16x32_bf16 v[16:19], v[72:75], v[106:109], v[16:19]\n\t"
        "v_alignbit_b32 v79, v45, v44, 16\n\t"
        "v_alignbit_b32 v80, v46, v45, 16\n\t"
        "v_mfma_f32_16x16x32_bf16 v[24:27], v[74:77], v[106:109], v[24:27]\n\t"
        "v_alignbit_b32 v81, v47, v46, 16\n\t"
        "v_mfma_f32_16x16x32_bf16 v[20:23], v[42:45], v[110:113], v[20:23]\n\t"
        "ds_read_b128 v[56:59], %4 offset:352\n\t"
        "ds_read_b128 v[106:109], %5 offset:368\n\t"
        "v_mfma_f32_16x16x32_bf16 v[28:31], v[44:47], v[110:113], v[28:31]\n\t"
        "s_waitcnt lgkmcnt(4)\n\t"
        "v_alignbit_b32 v82, v48, v47, 16\n\t"
        "v_mfma_f32_16x16x32_bf16 v[16:19], v[76:79], v[110:113], v[16:19]\n\t"
        "v_alignbit_b32 v83, v49, v48, 16\n\t"
        "v_alignbit_b32 v84, v50, v49, 16\n\t"
        "v_mfma_f32_16x16x32_bf16 v[24:27], v[78:81], v[110:113], v[24:27]\n\t"
        "v_alignbit_b32 v85, v51, v50, 16\n\t"
        "v_mfma_f32_16x16x32_bf16 v[20:23], v[46:49], v[114:117], v[20:23]\n\t"
        "ds_read_b128 v[60:63], %4 offset:368\n\t"
        "ds_read_b128 v[110:113], %5 offset:384\n\t"
        "v_mfma_f32_16x16x32_bf16 v[28:31], v[48:51], v[114:117], v[28:31]\n\t"
        "s_waitcnt lgkmcnt(4)\n\t"
        "v_alignbit_b32 v86, v52, v51, 16\n\t"
        "v_mfma_f32_16x16x32_bf16 v[16:19], v[80:83], v[114:117], v[16:19]\n\t"
        "v_alignbit_b32 v87, v53, v52, 16\n\t"
        "v_alignbit_b32 v88, v54, v53, 16\n\t"
        "v_mfma_f32_16x16x32_bf16 v[24:27], v[82:85], v[114:117], v[24:27]\n\t"
        "v_alignbit_b32 v89, v55, v54, 16\n\t"
        "v_mfma_f32_16x16x32_bf16 v[20:23], v[50:53], v[102:105], v[20:23]\n\t"
        "ds_read_b128 v[32:35], %4 offset:384\n\t"
        "ds_read_b128 v[64:67], %4 offset:384\n\t"
        "ds_read_b128 v[114:117], %5 offset:400\n\t"
        "v_mfma_f32_16x16x32_bf16 v[28:31], v[52:55], v[102:105], v[28:31]\n\t"
        "s_waitcnt lgkmcnt(5)\n\t"
        "v_alignbit_b32 v90, v56, v55, 16\n\t"
        "v_mfma_f32_16x16x32_bf16 v[16:19], v[84:87], v[102:105], v[16:19]\n\t"
        "v_alignbit_b32 v91, v57, v56, 16\n\t"
        "v_alignbit_b32 v92, v58, v57, 16\n\t"
        "v_mfma_f32_16x16x32_bf16 v[24:27], v[86:89], v[102:105], v[24:27]\n\t"
        "v_alignbit_b32 v93, v59, v58, 16\n\t"
        "v_mfma_f32_16x16x32_bf16 v[20:23], v[54:57], v[106:109], v[20:23]\n\t"
        "ds_read_b128 v[36:39], %4 offset:400\n\t"
        "ds_read_b128 v[102:105], %5 offset:416\n\t"
        "v_mfma_f32_16x16x32_bf16 v[28:31], v[56:59], v[106:109], v[28:31]\n\t"
        "s_waitcnt lgkmcnt(5)\n\t"
        "v_alignbit_b32 v94, v60, v59, 16\n\t"
        "v_mfma_f32_16x16x32_bf16 v[16:19], v[88:91], v[106:109], v[16:19]\n\t"
        "v_alignbit_b32 v95, v61, v60, 16\n\t"
        "v_alignbit_b32 v96, v62, v61, 16\n\t"
        "v_mfma_f32_16x16x32_bf16 v[24:27], v[90:93], v[106:109], v[24:27]\n\t"
        "v_alignbit_b32 v97, v63, v62, 16\n\t"
        "v_mfma_f32_16x16x32_bf16 v[20:23], v[58:61], v[110:113], v[20:23]\n\t"
        "ds_read_b128 v[40:43], %4 offset:416\n\t"
        "ds_read_b128 v[106:109], %5 offset:432\n\t"
        "v_mfma_f32_16x16x32_bf16 v[28:31], v[60:63], v[110:113], v[28:31]\n\t"
        "s_waitcnt lgkmcnt(4)\n\t"
        "v_alignbit_b32 v98, v64, v63, 16\n\t"
        "v_mfma_f32_16x16x32_bf16 v[16:19], v[92:95], v[110:113], v[16:19]\n\t"
        "v_alignbit_b32 v99, v65, v64, 16\n\t"
        "v_alignbit_b32 v100, v66, v65, 16\n\t"
        "v_mfma_f32_16x16x32_bf16 v[24:27], v[94:97], v[110:113], v[24:27]\n\t"
        "v_alignbit_b32 v101, v67, v66, 16\n\t"
        "v_mfma_f32_16x16x32_bf16 v[20:23], v[62:65], v[114:117], v[20:23]\n\t"
        "ds_read_b128 v[44:47], %4 offset:432\n\t"
        "ds_read_b128 v[110:113], %5 offset:448\n\t"
        "v_mfma_f32_16x16x32_bf16 v[28:31], v[64:67], v[114:117], v[28:31]\n\t"
        "s_waitcnt lgkmcnt(4)\n\t"
        "v_alignbit_b32 v68, v34, v33, 16\n\t"
        "v_alignbit_b32 v69, v35, v34, 16\n\t"
        "v_alignbit_b32 v70, v36, v35, 16\n\t"
        "v_mfma_f32_16x16x32_bf16 v[16:19], v[96:99], v[114:117], v[16:19]\n\t"
        "v_alignbit_b32 v71, v37, v36, 16\n\t"
        "v_alignbit_b32 v72, v38, v37, 16\n\t"
        "v_mfma_f32_16x16x32_bf16 v[24:27], v[98:101], v[114:117], v[24:27]\n\t"
        "v_alignbit_b32 v73, v39, v38, 16\n\t"
        "v_mfma_f32_16x16x32_bf16 v[20:23], v[34:37], v[102:105], v[20:23]\n\t"
        "ds_read_b128 v[48:51], %4 offset:448\n\t"
        "ds_read_b128 v[114:117], %5 offset:464\n\t"
        "v_mfma_f32_16x16x32_bf16 v[28:31], v[36:39], v[102:105], v[28:31]\n\t"
        "s_waitcnt lgkmcnt(4)\n\t"
        "v_alignbit_b32 v74, v40, v39, 16\n\t"
        "v_mfma_f32_16x16x32_bf16 v[16:19], v[68:71], v[102:105], v[16:19]\n\t"
        "v_alignbit_b32 v75, v41, v40, 16\n\t"
        "v_alignbit_b32 v76, v42, v41, 16\n\t"
        "v_mfma_f32_16x16x32_bf16 v[24:27], v[70:73], v[102:105], v[24:27]\n\t"
        "v_alignbit_b32 v77, v43, v42, 16\n\t"
        "v_mfma_f32_16x16x32_bf16 v[20:23], v[38:41], v[106:109], v[20:23]\n\t"
        "ds_read_b128 v[52:55], %4 offset:464\n\t"
        "ds_read_b128 v[102:105], %5 offset:480\n\t"
        "v_mfma_f32_16x16x32_bf16 v[28:31], v[40:43], v[106:109], v[28:31]\n\t"
        "s_waitcnt lgkmcnt(4)\n\t"
        "v_alignbit_b32 v78, v44, v43, 16\n\t"
        "v_mfma_f32_16x16x32_bf16 v[16:19], v[72:75], v[106:109], v[16:19]\n\t"
        "v_alignbit_b32 v79, v45, v44, 16\n\t"
        "v_alignbit_b32 v80, v46, v45, 16\n\t"
        "v_mfma_f32_16x16x32_bf16 v[24:27], v[74:77], v[106:109], v[24:27]\n\t"
        "v_alignbit_b32 v81, v47, v46, 16\n\t"
        "v_mfma_f32_16x16x32_bf16 v[20:23], v[42:45], v[110:113], v[20:23]\n\t"
        "ds_read_b128 v[56:59], %4 offset:480\n\t"
        "ds_read_b128 v[106:109], %5 offset:496\n\t"
        "v_mfma_f32_16x16x32_bf16 v[28:31], v[44:47], v[110:113], v[28:31]\n\t"
        "s_waitcnt lgkmcnt(4)\n\t"
        "v_alignbit_b32 v82, v48, v47, 16\n\t"
        "v_mfma_f32_16x16x32_bf16 v[16:19], v[76:79], v[110:113], v[16:19]\n\t"
        "v_alignbit_b32 v83, v49, v48, 16\n\t"
        "v_alignbit_b32 v84, v50, v49, 16\n\t"
        "v_mfma_f32_16x16x32_bf16 v[24:27], v[78:81], v[110:113], v[24:27]\n\t"
        "v_alignbit_b32 v85, v51, v50, 16\n\t"
        "v_mfma_f32_16x16x32_bf16 v[20:23], v[46:49], v[114:117], v[20:23]\n\t"
        "ds_read_b128 v[60:63], %4 offset:496\n\t"
        "ds_read_b128 v[110:113], %5 offset:512\n\t"
        "v_mfma_f32_16x16x32_bf16 v[28:31], v[48:51], v[114:117], v[28:31]\n\t"
        "s_waitcnt lgkmcnt(4)\n\t"
        "v_alignbit_b32 v86, v52, v51, 16\n\t"
        "v_mfma_f32_16x16x32_bf16 v[16:19], v[80:83], v[114:117], v[16:19]\n\t"
        "v_alignbit_b32 v87, v53, v52, 16\n\t"
        "v_alignbit_b32 v88, v54, v53, 16\n\t"
        "v_mfma_f32_16x16x32_bf16 v[24:27], v[82:85], v[114:117], v[24:27]\n\t"
        "v_alignbit_b32 v89, v55, v54, 16\n\t"
        "v_mfma_f32_16x16x32_bf16 v[20:23], v[50:53], v[102:105], v[20:23]\n\t"
        "ds_read_b128 v[64:67], %4 offset:512\n\t"
        "ds_read_b128 v[114:117], %5 offset:528\n\t"
        "v_mfma_f32_16x16x32_bf16 v[28:31], v[52:55], v[102:105], v[28:31]\n\t"
        "s_waitcnt lgkmcnt(4)\n\t"
        "v_alignbit_b32 v90, v56, v55, 16\n\t"
        "v_mfma_f32_16x16x32_bf16 v[16:19], v[84:87], v[102:105], v[16:19]\n\t"
        "v_alignbit_b32 v91, v57, v56, 16\n\t"
        "v_alignbit_b32 v92, v58, v57, 16\n\t"
        "v_mfma_f32_16x16x32_bf16 v[24:27], v[86:89], v[102:105], v[24:27]\n\t"
        "v_alignbit_b32 v93, v59, v58, 16\n\t"
        "v_mfma_f32_16x16x32_bf16 v[20:23], v[54:57], v[106:109], v[20:23]\n\t"
        "v_mfma_f32_16x16x32_bf16 v[28:31], v[56:59], v[106:109], v[28:31]\n\t"
        "s_waitcnt lgkmcnt(2)\n\t"
        "v_alignbit_b32 v94, v60, v59, 16\n\t"
        "v_mfma_f32_16x16x32_bf16 v[16:19], v[88:91], v[106:109], v[16:19]\n\t"
        "v_alignbit_b32 v95, v61, v60, 16\n\t"
        "v_alignbit_b32 v96, v62, v61, 16\n\t"
        "v_mfma_f32_16x16x32_bf16 v[24:27], v[90:93], v[106:109], v[24:27]\n\t"
        "v_alignbit_b32 v97, v63, v62, 16\n\t"
        "v_mfma_f32_16x16x32_bf16 v[20:23], v[58:61], v[110:113], v[20:23]\n\t"
        "v_mfma_f32_16x16x32_bf16 v[28:31], v[60:63], v[110:113], v[28:31]\n\t"
        "s_waitcnt lgkmcnt(0)\n\t"
        "v_alignbit_b32 v98, v64, v63, 16\n\t"
        "v_mfma_f32_16x16x32_bf16 v[16:19], v[92:95], v[110:113], v[16:19]\n\t"
        "v_alignbit_b32 v99, v65, v64, 16\n\t"
        "v_alignbit_b32 v100, v66, v65, 16\n\t"
        "v_mfma_f32_16x16x32_bf16 v[24:27], v[94:97], v[110:113], v[24:27]\n\t"
        "v_alignbit_b32 v101, v67, v66, 16\n\t"
        "v_mfma_f32_16x16x32_bf16 v[20:23], v[62:65], v[114:117], v[20:23]\n\t"
        "v_mfma_f32_16x16x32_bf16 v[28:31], v[64:67], v[114:117], v[28:31]\n\t"
        "v_mfma_f32_16x16x32_bf16 v[16:19], v[96:99], v[114:117], v[16:19]\n\t"
        "v_mfma_f32_16x16x32_bf16 v[24:27], v[98:101], v[114:117], v[24:27]\n\t"
        "LCE_%=:\n\t"
        "s_waitcnt lgkmcnt(0)\n\t"
        "s_nop 7\n\t"
            : "={v[0:15]}"(a0), "={v[16:31]}"(a1)
            : "{v118}"(pa), "{v119}"(pb), "{v120}"(pa2), "{v121}"(pb2), "s"(mode)
            : "v32", "v33", "v34", "v35", "v36", "v37", "v38", "v39", "v40", "v41", "v42", "v43", "v44", "v45", "v46", "v47", "v48", "v49", "v50", "v51", "v52", "v53", "v54", "v55", "v56", "v57", "v58", "v59", "v60", "v61", "v62", "v63", "v64", "v65", "v66", "v67", "v68", "v69", "v70", "v71", "v72", "v73", "v74", "v75", "v76", "v77", "v78", "v79", "v80", "v81", "v82", "v83", "v84", "v85", "v86", "v87", "v88", "v89", "v90", "v91", "v92", "v93", "v94", "v95", "v96", "v97", "v98", "v99", "v100", "v101", "v102", "v103", "v104", "v105", "v106", "v107", "v108", "v109", "v110", "v111", "v112", "v113", "v114", "v115", "v116", "v117", "scc", "memory");
        STAMPW(1, 0, 3); STAMPW(1, 1, 4); STAMPW(1, 3, 5);
        if (w == 1) {
#pragma unroll
            for (int i = 0; i < 4; ++i) {
                *(LAS f32x4*)((lptr)smem + K1_EX + i * 1024 + l * 16) = (f32x4){a0[4 * i], a0[4 * i + 1], a0[4 * i + 2], a0[4 * i + 3]};
                *(LAS f32x4*)((lptr)smem + K1_EX + 4096 + i * 1024 + l * 16) = (f32x4){a1[4 * i], a1[4 * i + 1], a1[4 * i + 2], a1[4 * i + 3]};
            }
            lds_arrive(lds0 + K1_FL + 4);
            return;
        }
        lds_wait(lds0 + K1_FL + 4, 5);
        if (w != 3) {
            const int e = w >> 1;
            const float up = col >= 8 ? 1.f : 0.f;
            const lptr ex = (lptr)smem + K1_EX + e * 4096 + (qq * 16 + ((col & 7) >> 1) + 4 * (col & 1)) * 16;
            f32x4 y[8];
#pragma unroll
            for (int vv = 0; vv < 4; ++vv) {
                const int rho = (vv & 1) + 4 * (vv >> 1);
                const f32x4 e1 = *(LAS f32x4*)(ex + vv * 1024 + 128), e0 = *(LAS f32x4*)(ex + vv * 1024);
#pragma unroll
                for (int j = 0; j < 4; ++j) {
                    y[rho + 2][j] = (vv < 2 ? a0[8 * vv + j] : a1[8 * vv - 16 + j]) + up * e0[j];
                    y[rho][j] = (vv < 2 ? a0[8 * vv + 4 + j] : a1[8 * vv - 12 + j]) + up * e1[j];
                }
            }
            const lptr ge = (lptr)smem + K1_GE + e * GE_C + (18 * col + qq) * 16;
            u16* xrow = (u16*)(ws + WS_X1T) + (size_t)(dA + e) * S + 128 * col + 8 * qq;
#pragma unroll
            for (int j = 0; j < 4; ++j) {
                const f32x4 g0 = *(LAS f32x4*)(ge + 64 * j), g1 = *(LAS f32x4*)(ge + 64 * j + GE_P), e0 = *(LAS f32x4*)(ge + 64 * j + 2 * GE_P), e1 = *(LAS f32x4*)(ge + 64 * j + 3 * GE_P);
                st16_wt(xrow + 32 * j, (u32x4){cvtpk(e0[0] + g0[0] * y[0][j], e0[1] + g0[1] * y[1][j]), cvtpk(e0[2] + g0[2] * y[2][j], e0[3] + g0[3] * y[3][j]),
                                               cvtpk(e1[0] + g1[0] * y[4][j], e1[1] + g1[1] * y[5][j]), cvtpk(e1[2] + g1[2] * y[6][j], e1[3] + g1[3] * y[7][j])});
            }
        } else {
            if (hf) a0 += a1;
            const int be = col >> 3, cc = 8 * hf + pi;
            const lptr ge = (lptr)smem + K1_GE + 2 * GE_C + (18 * cc + qq) * 16 + 8 * (1 - be);
            u32 dd[4][2];
#pragma unroll
            for (int j = 0; j < 4; ++j)
#pragma unroll
                for (int h = 0; h < 2; ++h) {
                    const f32x2 g = *(LAS f32x2*)(ge + 64 * j + h * GE_P), e = *(LAS f32x2*)(ge + 64 * j + (2 + h) * GE_P);
                    dd[j][h] = cvtpk(e[0] + g[0] * a0[8 * h + j], e[1] + g[1] * a0[8 * h + 4 + j]);
                }
            u16* xrow = (u16*)(ws + WS_X1T) + (size_t)dC * S + 128 * cc + 8 * qq + 32 * (1 - be);
#pragma unroll
            for (int jp = 0; jp < 4; jp += 2) {
                const u32 s0 = be ? dd[jp + 1][0] : dd[jp][0], s1 = be ? dd[jp + 1][1] : dd[jp][1];
                const u32 k0 = be ? dd[jp][0] : dd[jp + 1][0], k1 = be ? dd[jp][1] : dd[jp + 1][1];
                const u32 r0 = __builtin_amdgcn_update_dpp(0, s0, 0x128, 0xf, 0xf, true), r1 = __builtin_amdgcn_update_dpp(0, s1, 0x128, 0xf, 0xf, true);
                st16_wt(xrow + 32 * jp, be ? (u32x4){k0, r0, k1, r1} : (u32x4){r0, k0, r1, k1});
            }
        }
        STAMPW(1, 0, 1);
        return;
    }
    const int h = w - 4;
    {
        const u32x4 z4 = {0, 0, 0, 0};
        if (h < 3) {
            const lptr wl = (lptr)smem + h * CV_W;
            if (l < 16) *(LAS u32x4*)(wl + CV_A + l * 16) = z4;
#pragma unroll
            for (int i = 0; i < 2; ++i) { *(LAS u32x4*)(wl + CV_B + 288 * 16 + i * 1024 + l * 16) = z4; *(LAS u32x4*)(wl + CV_B + CV_BC + 288 * 16 + i * 1024 + l * 16) = z4; }
        }
    }
    if (h == 3 && l < 4) *(LAS u32*)((lptr)smem + K1_FL + l * 4) = 0;
    asm volatile("s_waitcnt lgkmcnt(0)\n\ts_barrier" ::: "memory");
    __builtin_amdgcn_s_setprio(HPRIO);
    const int bl = (b >> 4) * 8 + (b & 7);
    const int un0 = h == HA ? b : h == HB ? 256 + b : h == HC ? 540 + b : (!hf && bl < 28) ? 512 + bl : 1024 + b;
    const int isw = un0 < 540, fg = un0 / 20, ks = isw ? un0 - 20 * fg : (un0 - 540) >> 2, hd = un0 & 3;
    const int ck = 64 * h + l;
    f32x4 st[4]; u32x4 qv[3], kr[3];
#pragma unroll
    for (int i = 0; i < 4; ++i) st[i] = *(const f32x4*)(ws + WS_ST1 + ck * 64 + i * 16);
#pragma unroll
    for (int e = 0; e < 3; ++e) {
        const int d = e < 2 ? dA + e : dC;
        qv[e] = *(const u32x4*)(ws + WS_QT + (size_t)d * S * 2 + ck * 16);
        kr[e] = *(const u32x4*)(ws + WS_KVR + (size_t)d * KVS * 2 + (255 - ck) * 16);
    }
    const int le = min(l >> 4, 2), ld = le < 2 ? dA + le : dC;
    float mv = mod[(l & 3) * (6 * D) + min((l >> 2) & 3, 2) * D + ld];
    const u16 dv = sD[ld];
    f32x4 mr = *(const f32x4*)(mod + ((l >> 3) & 3) * (6 * D) + (3 + (l >> 5)) * D + 32 * min(ks, 19) + 4 * (l & 7));
    asm volatile("" : "+v"(mr));
    const u32 pp = lds0 + K1_PP + h * 10240;
    if (isw) {
        const int rl = l >> 3, un = (l & 7) >> 1;
        const u32 voff0 = (u32)(rl * (F * 2) + ((un ^ ((rl >> 1) & 1)) * 32) + (l & 1) * 16), voff1 = voff0 ^ 64;
#pragma unroll
        for (int i = 0; i < 8; ++i) glds16s((const char*)(i < 4 ? wg : wu) + (size_t)(32 * ks + 8 * (i & 3)) * (F * 2) + fg * 128, (i & 1) ? voff1 : voff0, pp + i * 1024);
    } else if (un0 < 756) {
#pragma unroll
        for (int i = 0; i < 10; ++i) { const int p = 64 * i + l, row = (p * 3277) >> 16; glds16s((const char*)wd + (size_t)ks * 32 * 1280 + hd * 320, (u32)(row * 1280 + (p - 20 * row) * 16), pp + i * 1024); }
    }
    mv += dpp_f<0xB1>(mv); mv += dpp_f<0x4E>(mv); mv = rbf(mv);
    float cA[3], cS[3], cG[3], cD[3];
#pragma unroll
    for (int e = 0; e < 3; ++e) {
        cS[e] = u2f(__builtin_amdgcn_readlane((int)f2u(mv), 16 * e)); cA[e] = rbf(1.f + u2f(__builtin_amdgcn_readlane((int)f2u(mv), 16 * e + 4)));
        cG[e] = u2f(__builtin_amdgcn_readlane((int)f2u(mv), 16 * e + 8)); cD[e] = u2f(__builtin_amdgcn_readlane((int)dv << 16, 16 * e));
        cA[e] *= cG[e]; cS[e] *= cG[e];
    }
#pragma unroll
    for (int e = 0; e < 3; ++e) {
        if (e == 2 && (h >> 1) != hf) break;
        f32x2 G[4], E[4];
#pragma unroll
        for (int i = 0; i < 4; ++i) {
            const u32 qi = qv[e][i], ki = kr[e][3 - i];
            const f32x2 q2 = {lo16(qi), hi16(qi)};
            G[i] = (q2 * (f32x2){st[i][0], st[i][2]} + (f32x2){st[i][1], st[i][3]}) * cA[e] + cS[e];
            E[i] = q2 + G[i] * ((f32x2){hi16(ki), lo16(ki)} * cD[e]);
        }
        const lptr ge = (lptr)smem + K1_GE + e * GE_C + (18 * (ck >> 4) + (ck & 15)) * 16;
        *(LAS f32x4*)ge = (f32x4){G[0][0], G[0][1], G[1][0], G[1][1]};               *(LAS f32x4*)(ge + GE_P) = (f32x4){G[2][0], G[2][1], G[3][0], G[3][1]};
        *(LAS f32x4*)(ge + 2 * GE_P) = (f32x4){E[0][0], E[0][1], E[1][0], E[1][1]};  *(LAS f32x4*)(ge + 3 * GE_P) = (f32x4){E[2][0], E[2][1], E[3][0], E[3][1]};
    }
    lds_arrive(lds0 + K1_FL + 4);
    STAMPW(1, 4, 6);
    WAITV(0);
    const int g = l >> 4, q4 = (l & 15) >> 2, p4 = l & 3;
    if (isw) {
        const int sx = ((q4 >> 1) & 1) | ((g & 1) << 1);
        const lptr rp = (lptr)smem + K1_PP + h * 10240 + (8 * g + q4) * 128 + p4 * 8;
        const lptr sc = (lptr)smem + K1_PP + h * 10240 + 8192;
#pragma unroll
        for (int j = 0; j < 4; ++j) {
            float t = mr[j]; t += dpp_f<0x128>(t);
            const auto r = __builtin_amdgcn_permlane16_swap(f2u(t), f2u(t), false, false);
            mr[j] = u2f(r[0]) + u2f(r[1]);
        }
        if ((l & 24) == 0) *(LAS f32x4*)(sc + (l >> 5) * 128 + (l & 7) * 16) = mr;
        const f32x4 sbv[2] = {*(LAS f32x4*)(sc + g * 32), *(LAS f32x4*)(sc + g * 32 + 16)}, cbv[2] = {*(LAS f32x4*)(sc + 128 + g * 32), *(LAS f32x4*)(sc + 128 + g * 32 + 16)};
        f32x2 cs[4]; u32x4 sel;
#pragma unroll
        for (int j = 0; j < 4; ++j) {
            f32x2 t = {cbv[j >> 1][2 * (j & 1)], cbv[j >> 1][2 * (j & 1) + 1]}; t = R2(t); t = t + 1.f;
            const u32 c2 = cvtpk(t[0], t[1]);
            cs[j] = (f32x2){lo16(c2), hi16(c2)};
            sel[j] = (l & 1) ? cvtpk(sbv[j >> 1][2 * (j & 1)], sbv[j >> 1][2 * (j & 1) + 1]) : c2;
        }
        f32x4 dd[8];
        u32x4* wp = (u32x4*)(ws + WS_WP) + ((size_t)(2 * fg) * 20 + ks) * 256 + l;
#pragma unroll
        for (int pw = 0; pw < 2; ++pw)
#pragma unroll
            for (int u = 0; u < 4; ++u) {
                const lptr a = rp + ((2 * pw + (u >> 1)) ^ sx) * 32 + (u & 1) * 4096;
                const bf16x8 fr = cat8(ldtr(a), ldtr(a + 512));
                dd[pw * 4 + u] = __builtin_amdgcn_mfma_f32_16x16x32_bf16(__builtin_bit_cast(bf16x8, sel), fr, (f32x4){0.f, 0.f, 0.f, 0.f}, 0, 0, 0);
                const u32x4 raw = __builtin_bit_cast(u32x4, fr);
                u32x4 o;
#pragma unroll
                for (int j = 0; j < 4; ++j) { const u32 x = raw[j]; o[j] = cvtpk(lo16(x) * cs[j][0], hi16(x) * cs[j][1]); }
                st16_wt(wp + (size_t)pw * 20 * 256 + u * 64, o);
            }
        float cc[4];
#pragma unroll
        for (int i = 0; i < 4; ++i) { const float a0 = dd[i][0], a1 = dd[i][1], b0 = dd[4 + i][0], b1 = dd[4 + i][1]; const float ta = (g & 1) ? a1 : a0, tb = (g & 1) ? b1 : b0; cc[i] = (g & 2) ? tb : ta; }
        float* cp = (float*)(ws + WS_CP) + (size_t)ks * 6912 + fg * 256 + g * 64 + (l & 15);
#pragma unroll
        for (int i = 0; i < 4; ++i) cp[16 * i] = cc[i];
    } else if (un0 < 756) {
        const lptr rp = (lptr)smem + K1_PP + h * 10240 + (4 * g + q4) * 320 + p4 * 40;
        u32x4* dst = (u32x4*)(ws + WS_WDP) + l;
#pragma unroll
        for (int cb = 0; cb < 2; ++cb)
#pragma unroll
            for (int n = 0; n < 5; ++n) {
                const s16x4 r0 = ldtr(rp + cb * 160 + n * 8), r1 = ldtr(rp + cb * 160 + n * 8 + 16 * 320);
                st16_wt(dst + (size_t)(((2 * hd + cb) * 54 + ks) * 5 + n) * 64, __builtin_bit_cast(u32x4, cat8(r0, r1)));
            }
    } else if (un0 >= 756 && un0 < 766) {
        const int i = (un0 - 756) * 64 + l;
        ((float*)(ws + WS_GB))[i] = mod4(mod, 5 * D + i);
    } else if (un0 >= 1024 + 224) {
        touch_code<6>(CODE_ADDR("_Z6k2_ffnPcPt") + ((b - 224) >> 3) * (6 * 1024), l);
    }
    STAMPW(1, 4, 7);
}

constexpr int K2_LDS = 163840, K3_X = 61440;
__global__ __launch_bounds__(256) void k2_ffn(char* __restrict__ ws, u16* __restrict__ out) {
    extern __shared__ __attribute__((aligned(16))) char smem[];
    const int b = blockIdx.x, tid = threadIdx.x, l = tid & 63, w = rfl(tid >> 6);
    const u32 lds0 = (u32)(uintptr_t)smem;
    STAMP_AT(ws, 2, 0);
    const int x = b & 7, i5 = b >> 3, spare = (i5 & 7) == 0, ia = i5 - 1 - (i5 >> 3);
    const int si = x * 4 + (i5 >> 3);
    if (spare) {
        if (tid < 216) {
            const int e = si * 216 + tid;
            float y[20], z = 0.f;
#pragma unroll
            for (int i = 0; i < 20; ++i) y[i] = ((const float*)(ws + WS_CP))[i * 6912 + e];
#pragma unroll
            for (int i = 0; i < 20; ++i) z += y[i];
            __hip_atomic_store((GAS u32*)(ws + WS_CV) + e, min(f2u(z), 0xfffffffeu), __ATOMIC_RELAXED, __HIP_MEMORY_SCOPE_AGENT);
        }
        const int t0 = si * 64;
        const u32x4* p = (const u32x4*)(ws + WS_X1T + (size_t)(160 * w + (l >> 3)) * 4096 + t0 * 2) + (l & 7);
        u32x4 a[20];
#pragma unroll
        for (int i = 0; i < 20; ++i) a[i] = p[i * 8 * 256];
        float* red = (float*)smem;
        float m[8];
#pragma unroll
        for (int pass = 0; pass < 2; ++pass) {
            f32x2 s[4];
#pragma unroll
            for (int j = 0; j < 4; ++j) s[j] = (f32x2){0.f, 0.f};
#pragma unroll
            for (int i = 0; i < 20; ++i)
#pragma unroll
                for (int j = 0; j < 4; ++j) {
                    const u32 u = a[i][j]; f32x2 z = {lo16(u), hi16(u)};
                    if (pass) { z -= (f32x2){m[2 * j], m[2 * j + 1]}; s[j] += z * z; } else s[j] += z;
                }
            float* rw = red + pass * 2304 + (w * 8 + (l >> 3)) * 68 + (l & 7) * 8;
            *(f32x4*)rw = (f32x4){s[0][0], s[0][1], s[1][0], s[1][1]};
            *(f32x4*)(rw + 4) = (f32x4){s[2][0], s[2][1], s[3][0], s[3][1]};
            __syncthreads();
            float y = 0.f;
#pragma unroll
            for (int i = 0; i < 8; ++i) y += red[pass * 2304 + ((tid & 3) * 8 + i) * 68 + (tid >> 2)];
            y += dpp_f<0xB1>(y); y += dpp_f<0x4E>(y);
            y *= (1.f / D);
            if (pass == 0) {
                if ((tid & 3) == 0) red[4608 + (tid >> 2)] = y;
                __syncthreads();
#pragma unroll
                for (int j = 0; j < 8; ++j) m[j] = red[4608 + (l & 7) * 8 + j];
            } else if ((tid & 3) == 0) {
                const float mu = red[4608 + (tid >> 2)], rs = 1.f / sqrtf(y + 1e-5f);
                __hip_atomic_store((GAS u64*)(ws + WS_ST2) + t0 + (tid >> 2), (u64)f2u(mu) | ((u64)(f2u(rs) | 1u) << 32), __ATOMIC_RELAXED, __HIP_MEMORY_SCOPE_AGENT);
            }
        }
    }
    const int u1 = (x >> 1) * 28 + max(ia, 0), tb = (x & 1) * 8 + (u1 & 7), cb1 = u1 >> 3;
    const int pr = min(cb1 * 4 + w, 53);
    const int tt = (x >> 1) * 8 + (i5 & 7), cb = (x & 1) * 4 + (i5 >> 3);
    const int g = l >> 4;
    u32x16 sv, vv;
    sv[0] = WS_X1T + w * 4 * 4096 + tb * 256;            sv[1] = WS_WP + pr * 81920 + 4096;
    sv[2] = WS_CV + pr * 512;                            sv[3] = WS_ST2 + tb * 1024;
    sv[4] = lds0 + w * 1024;                             sv[5] = spare ? 2 : cb1 * 4 + w > 53;
    sv[6] = WS_HP + (tb * 8 * 54 + pr) * 1024;           sv[7] = WS_WDP + (cb * 54 + w) * 5120 + 2048;
    sv[8] = WS_HP + (tt * 4 * 54 + w) * 1024;            sv[9] = WS_CNT + tb * 2048;
    sv[10] = WS_CNT + (tt >> 1) * 2048 + cb * 256;       sv[11] = WS_X1T + (cb * 80 + w * 8) * 4096 + tt * 128;
    sv[12] = lds0 + K3_X + w * 1024;                     sv[13] = WS_GB + cb * 320;
    sv[14] = w;                                          sv[15] = (40 << 20) + (2 * 1024 + b) * 64;
#pragma unroll
    for (int i = 0; i < 16; ++i) { const u32 e = sv[i]; sv[i] = rfl(e); }
    vv[0] = lds0 + g * 2048 + (g & 1) * 128 + (l & 15) * 8;   vv[1] = vv[0] ^ 128;
    vv[2] = ((l >> 1) & 3) * 4096 + (((l >> 3) ^ ((w >> 1) & 1)) * 32) + (l & 1) * 16;
    vv[3] = l * 16; vv[4] = g * 16; vv[5] = (l & 15) * 8;
    vv[15] = l * 16 + w * 55296; vv[6] = l * 16 + ((w + 1) & 3) * 55296; vv[7] = l * 16 + ((w + 2) & 3) * 55296; vv[8] = l * 16 + ((w + 3) & 3) * 55296;
    vv[9] = (l >> 3) * 4096 + (l & 7) * 16; vv[10] = g * 80; vv[11] = l * 256; vv[12] = l; vv[13] = l; vv[14] = l;
    f32x16 av[5], gv; f32x4 gw;
    asm volatile(
        "s_add_u32 s40, s64, s48\n\t"
        "s_addc_u32 s41, s65, 0\n\t"
        "s_add_u32 s42, s64, s49\n\t"
        "s_addc_u32 s43, s65, 0\n\t"
        "s_add_u32 s44, s64, s50\n\t"
        "s_addc_u32 s45, s65, 0\n\t"
        "s_add_u32 s46, s64, s51\n\t"
        "s_addc_u32 s47, s65, 0\n\t"
        "s_add_u32 s66, s64, s54\n\t"
        "s_addc_u32 s67, s65, 0\n\t"
        "s_add_u32 s68, s64, s55\n\t"
        "s_addc_u32 s69, s65, 0\n\t"
        "s_add_u32 s70, s64, s56\n\t"
        "s_addc_u32 s71, s65, 0\n\t"
        "s_mov_b32 m0, s52\n\t"
        "s_cmp_eq_u32 s53, 0\n\t"
        "s_cbranch_scc0 LND_%=\n\t"
        "global_load_lds_dwordx4 v242, s[40:41]\n\t"
        "s_add_u32 m0, m0, 0x1000\n\t"
        "s_add_u32 s40, s40, 0x10000\n\t"
        "s_addc_u32 s41, s41, 0\n\t"
        "global_load_lds_dwordx4 v242, s[40:41]\n\t"
        "s_add_u32 m0, m0, 0x1000\n\t"
        "s_add_u32 s40, s40, 0x10000\n\t"
        "s_addc_u32 s41, s41, 0\n\t"
        "global_load_dwordx4 a[32:35], v243, s[42:43] offset:-4096\n\t"
        "global_load_dwordx4 a[36:39], v243, s[42:43] offset:-3072\n\t"
        "global_load_dwordx4 a[40:43], v243, s[42:43] offset:-2048\n\t"
        "global_load_dwordx4 a[44:47], v243, s[42:43] offset:-1024\n\t"
        "global_load_lds_dwordx4 v242, s[40:41]\n\t"
        "s_add_u32 m0, m0, 0x1000\n\t"
        "s_add_u32 s40, s40, 0x10000\n\t"
        "s_addc_u32 s41, s41, 0\n\t"
        "global_load_lds_dwordx4 v242, s[40:41]\n\t"
        "s_add_u32 m0, m0, 0x1000\n\t"
        "s_add_u32 s40, s40, 0x10000\n\t"
        "s_addc_u32 s41, s41, 0\n\t"
        "global_load_dwordx4 a[48:51], v243, s[42:43] offset:0\n\t"
        "global_load_dwordx4 a[52:55], v243, s[42:43] offset:1024\n\t"
        "global_load_dwordx4 a[56:59], v243, s[42:43] offset:2048\n\t"
        "global_load_dwordx4 a[60:63], v243, s[42:43] offset:3072\n\t"
        "s_add_u32 s42, s42, 0x2000\n\t"
        "s_addc_u32 s43, s43, 0\n\t"
        "global_load_lds_dwordx4 v242, s[40:41]\n\t"
        "s_add_u32 m0, m0, 0x1000\n\t"
        "s_add_u32 s40, s40, 0x10000\n\t"
        "s_addc_u32 s41, s41, 0\n\t"
        "global_load_lds_dwordx4 v242, s[40:41]\n\t"
        "s_add_u32 m0, m0, 0x1000\n\t"
        "s_add_u32 s40, s40, 0x10000\n\t"
        "s_addc_u32 s41, s41, 0\n\t"
        "global_load_dwordx4 a[64:67], v243, s[42:43] offset:-4096\n\t"
        "global_load_dwordx4 a[68:71], v243, s[42:43] offset:-3072\n\t"
        "global_load_dwordx4 a[72:75], v243, s[42:43] offset:-2048\n\t"
        "global_load_dwordx4 a[76:79], v243, s[42:43] offset:-1024\n\t"
        "global_load_lds_dwordx4 v242, s[40:41]\n\t"
        "s_add_u32 m0, m0, 0x1000\n\t"
        "s_add_u32 s40, s40, 0x10000\n\t"
        "s_addc_u32 s41, s41, 0\n\t"
        "global_load_lds_dwordx4 v242, s[40:41]\n\t"
        "s_add_u32 m0, m0, 0x1000\n\t"
        "s_add_u32 s40, s40, 0x10000\n\t"
        "s_addc_u32 s41, s41, 0\n\t"
        "global_load_dwordx4 a[80:83], v243, s[42:43] offset:0\n\t"
        "global_load_dwordx4 a[84:87], v243, s[42:43] offset:1024\n\t"
        "global_load_dwordx4 a[88:91], v243, s[42:43] offset:2048\n\t"
        "global_load_dwordx4 a[92:95], v243, s[42:43] offset:3072\n\t"
        "s_add_u32 s42, s42, 0x2000\n\t"
        "s_addc_u32 s43, s43, 0\n\t"
        "s_waitcnt vmcnt(22)\n\t"
        "s_barrier\n\t"
        "ds_read_b64_tr_b16 a[0:1], v240 offset:0\n\t"
        "ds_read_b64_tr_b16 a[2:3], v240 offset:1024\n\t"
        "ds_read_b64_tr_b16 a[4:5], v241 offset:0\n\t"
        "ds_read_b64_tr_b16 a[6:7], v241 offset:1024\n\t"
        "ds_read_b64_tr_b16 a[8:9], v240 offset:256\n\t"
        "ds_read_b64_tr_b16 a[10:11], v240 offset:1280\n\t"
        "ds_read_b64_tr_b16 a[12:13], v241 offset:256\n\t"
        "ds_read_b64_tr_b16 a[14:15], v241 offset:1280\n\t"
        "ds_read_b64_tr_b16 a[16:17], v240 offset:512\n\t"
        "ds_read_b64_tr_b16 a[18:19], v240 offset:1536\n\t"
        "ds_read_b64_tr_b16 a[20:21], v241 offset:512\n\t"
        "ds_read_b64_tr_b16 a[22:23], v241 offset:1536\n\t"
        "ds_read_b64_tr_b16 a[24:25], v240 offset:768\n\t"
        "ds_read_b64_tr_b16 a[26:27], v240 offset:1792\n\t"
        "s_waitcnt vmcnt(16)\n\t"
        "s_barrier\n\t"
        "s_waitcnt lgkmcnt(12)\n\t"
        "v_mfma_f32_16x16x32_bf16 v[0:3], a[32:35], a[0:3], 0\n\t"
        "ds_read_b64_tr_b16 a[28:29], v241 offset:768\n\t"
        "v_mfma_f32_16x16x32_bf16 v[4:7], a[36:39], a[0:3], 0\n\t"
        "ds_read_b64_tr_b16 a[30:31], v241 offset:1792\n\t"
        "v_mfma_f32_16x16x32_bf16 v[8:11], a[40:43], a[0:3], 0\n\t"
        "v_mfma_f32_16x16x32_bf16 v[12:15], a[44:47], a[0:3], 0\n\t"
        "s_waitcnt lgkmcnt(12)\n\t"
        "v_mfma_f32_16x16x32_bf16 v[16:19], a[32:35], a[4:7], 0\n\t"
        "ds_read_b64_tr_b16 a[0:1], v240 offset:8192\n\t"
        "v_mfma_f32_16x16x32_bf16 v[20:23], a[36:39], a[4:7], 0\n\t"
        "ds_read_b64_tr_b16 a[2:3], v240 offset:9216\n\t"
        "v_mfma_f32_16x16x32_bf16 v[24:27], a[40:43], a[4:7], 0\n\t"
        "global_load_lds_dwordx4 v242, s[40:41]\n\t"
        "s_add_u32 m0, m0, 0x1000\n\t"
        "v_mfma_f32_16x16x32_bf16 v[28:31], a[44:47], a[4:7], 0\n\t"
        "s_add_u32 s40, s40, 0x10000\n\t"
        "s_addc_u32 s41, s41, 0\n\t"
        "s_waitcnt lgkmcnt(12)\n\t"
        "v_mfma_f32_16x16x32_bf16 v[32:35], a[32:35], a[8:11], 0\n\t"
        "ds_read_b64_tr_b16 a[4:5], v241 offset:8192\n\t"
        "v_mfma_f32_16x16x32_bf16 v[36:39], a[36:39], a[8:11], 0\n\t"
        "ds_read_b64_tr_b16 a[6:7], v241 offset:9216\n\t"
        "v_mfma_f32_16x16x32_bf16 v[40:43], a[40:43], a[8:11], 0\n\t"
        "global_load_lds_dwordx4 v242, s[40:41]\n\t"
        "s_add_u32 m0, m0, 0x1000\n\t"
        "v_mfma_f32_16x16x32_bf16 v[44:47], a[44:47], a[8:11], 0\n\t"
        "s_add_u32 s40, s40, 0x10000\n\t"
        "s_addc_u32 s41, s41, 0\n\t"
        "s_waitcnt lgkmcnt(12)\n\t"
        "v_mfma_f32_16x16x32_bf16 v[48:51], a[32:35], a[12:15], 0\n\t"
        "ds_read_b64_tr_b16 a[8:9], v240 offset:8448\n\t"
        "v_mfma_f32_16x16x32_bf16 v[52:55], a[36:39], a[12:15], 0\n\t"
        "ds_read_b64_tr_b16 a[10:11], v240 offset:9472\n\t"
        "v_mfma_f32_16x16x32_bf16 v[56:59], a[40:43], a[12:15], 0\n\t"
        "global_load_dwordx4 a[96:99], v243, s[42:43] offset:-4096\n\t"
        "v_mfma_f32_16x16x32_bf16 v[60:63], a[44:47], a[12:15], 0\n\t"
        "s_waitcnt lgkmcnt(12)\n\t"
        "v_mfma_f32_16x16x32_bf16 v[64:67], a[32:35], a[16:19], 0\n\t"
        "ds_read_b64_tr_b16 a[12:13], v241 offset:8448\n\t"
        "v_mfma_f32_16x16x32_bf16 v[68:71], a[36:39], a[16:19], 0\n\t"
        "ds_read_b64_tr_b16 a[14:15], v241 offset:9472\n\t"
        "v_mfma_f32_16x16x32_bf16 v[72:75], a[40:43], a[16:19], 0\n\t"
        "global_load_dwordx4 a[100:103], v243, s[42:43] offset:-3072\n\t"
        "v_mfma_f32_16x16x32_bf16 v[76:79], a[44:47], a[16:19], 0\n\t"
        "s_waitcnt lgkmcnt(12)\n\t"
        "v_mfma_f32_16x16x32_bf16 v[80:83], a[32:35], a[20:23], 0\n\t"
        "ds_read_b64_tr_b16 a[16:17], v240 offset:8704\n\t"
        "v_mfma_f32_16x16x32_bf16 v[84:87], a[36:39], a[20:23], 0\n\t"
        "ds_read_b64_tr_b16 a[18:19], v240 offset:9728\n\t"
        "v_mfma_f32_16x16x32_bf16 v[88:91], a[40:43], a[20:23], 0\n\t"
        "global_load_dwordx4 a[104:107], v243, s[42:43] offset:-2048\n\t"
        "v_mfma_f32_16x16x32_bf16 v[92:95], a[44:47], a[20:23], 0\n\t"
        "s_waitcnt lgkmcnt(12)\n\t"
        "v_mfma_f32_16x16x32_bf16 v[96:99], a[32:35], a[24:27], 0\n\t"
        "ds_read_b64_tr_b16 a[20:21], v241 offset:8704\n\t"
        "v_mfma_f32_16x16x32_bf16 v[100:103], a[36:39], a[24:27], 0\n\t"
        "ds_read_b64_tr_b16 a[22:23], v241 offset:9728\n\t"
        "v_mfma_f32_16x16x32_bf16 v[104:107], a[40:43], a[24:27], 0\n\t"
        "global_load_dwordx4 a[108:111], v243, s[42:43] offset:-1024\n\t"
        "v_mfma_f32_16x16x32_bf16 v[108:111], a[44:47], a[24:27], 0\n\t"
        "s_waitcnt lgkmcnt(12)\n\t"
        "v_mfma_f32_16x16x32_bf16 v[112:115], a[32:35], a[28:31], 0\n\t"
        "ds_read_b64_tr_b16 a[24:25], v240 offset:8960\n\t"
        "v_mfma_f32_16x16x32_bf16 v[116:119], a[36:39], a[28:31], 0\n\t"
        "ds_read_b64_tr_b16 a[26:27], v240 offset:9984\n\t"
        "v_mfma_f32_16x16x32_bf16 v[120:123], a[40:43], a[28:31], 0\n\t"
        "v_mfma_f32_16x16x32_bf16 v[124:127], a[44:47], a[28:31], 0\n\t"
        "s_waitcnt vmcnt(16)\n\t"
        "s_barrier\n\t"
        "s_waitcnt lgkmcnt(12)\n\t"
        "v_mfma_f32_16x16x32_bf16 v[0:3], a[48:51], a[0:3], v[0:3]\n\t"
        "ds_read_b64_tr_b16 a[28:29], v241 offset:8960\n\t"
        "v_mfma_f32_16x16x32_bf16 v[4:7], a[52:55], a[0:3], v[4:7]\n\t"
        "ds_read_b64_tr_b16 a[30:31], v241 offset:9984\n\t"
        "v_mfma_f32_16x16x32_bf16 v[8:11], a[56:59], a[0:3], v[8:11]\n\t"
        "v_mfma_f32_16x16x32_bf16 v[12:15], a[60:63], a[0:3], v[12:15]\n\t"
        "s_waitcnt lgkmcnt(12)\n\t"
        "v_mfma_f32_16x16x32_bf16 v[16:19], a[48:51], a[4:7], v[16:19]\n\t"
        "ds_read_b64_tr_b16 a[0:1], v240 offset:16384\n\t"
        "v_mfma_f32_16x16x32_bf16 v[20:23], a[52:55], a[4:7], v[20:23]\n\t"
        "ds_read_b64_tr_b16 a[2:3], v240 offset:17408\n\t"
        "v_mfma_f32_16x16x32_bf16 v[24:27], a[56:59], a[4:7], v[24:27]\n\t"
        "global_load_lds_dwordx4 v242, s[40:41]\n\t"
        "s_add_u32 m0, m0, 0x1000\n\t"
        "v_mfma_f32_16x16x32_bf16 v[28:31], a[60:63], a[4:7], v[28:31]\n\t"
        "s_add_u32 s40, s40, 0x10000\n\t"
        "s_addc_u32 s41, s41, 0\n\t"
        "s_waitcnt lgkmcnt(12)\n\t"
        "v_mfma_f32_16x16x32_bf16 v[32:35], a[48:51], a[8:11], v[32:35]\n\t"
        "ds_read_b64_tr_b16 a[4:5], v241 offset:16384\n\t"
        "v_mfma_f32_16x16x32_bf16 v[36:39], a[52:55], a[8:11], v[36:39]\n\t"
        "ds_read_b64_tr_b16 a[6:7], v241 offset:17408\n\t"
        "v_mfma_f32_16x16x32_bf16 v[40:43], a[56:59], a[8:11], v[40:43]\n\t"
        "global_load_lds_dwordx4 v242, s[40:41]\n\t"
        "s_add_u32 m0, m0, 0x1000\n\t"
        "v_mfma_f32_16x16x32_bf16 v[44:47], a[60:63], a[8:11], v[44:47]\n\t"
        "s_add_u32 s40, s40, 0x10000\n\t"
        "s_addc_u32 s41, s41, 0\n\t"
        "s_waitcnt lgkmcnt(12)\n\t"
        "v_mfma_f32_16x16x32_bf16 v[48:51], a[48:51], a[12:15], v[48:51]\n\t"
        "ds_read_b64_tr_b16 a[8:9], v240 offset:16640\n\t"
        "v_mfma_f32_16x16x32_bf16 v[52:55], a[52:55], a[12:15], v[52:55]\n\t"
        "ds_read_b64_tr_b16 a[10:11], v240 offset:17664\n\t"
        "v_mfma_f32_16x16x32_bf16 v[56:59], a[56:59], a[12:15], v[56:59]\n\t"
        "global_load_dwordx4 a[32:35], v243, s[42:43] offset:0\n\t"
        "v_mfma_f32_16x16x32_bf16 v[60:63], a[60:63], a[12:15], v[60:63]\n\t"
        "s_waitcnt lgkmcnt(12)\n\t"
        "v_mfma_f32_16x16x32_bf16 v[64:67], a[48:51], a[16:19], v[64:67]\n\t"
        "ds_read_b64_tr_b16 a[12:13], v241 offset:16640\n\t"
        "v_mfma_f32_16x16x32_bf16 v[68:71], a[52:55], a[16:19], v[68:71]\n\t"
        "ds_read_b64_tr_b16 a[14:15], v241 offset:17664\n\t"
        "v_mfma_f32_16x16x32_bf16 v[72:75], a[56:59], a[16:19], v[72:75]\n\t"
        "global_load_dwordx4 a[36:39], v243, s[42:43] offset:1024\n\t"
        "v_mfma_f32_16x16x32_bf16 v[76:79], a[60:63], a[16:19], v[76:79]\n\t"
        "s_waitcnt lgkmcnt(12)\n\t"
        "v_mfma_f32_16x16x32_bf16 v[80:83], a[48:51], a[20:23], v[80:83]\n\t"
        "ds_read_b64_tr_b16 a[16:17], v240 offset:16896\n\t"
        "v_mfma_f32_16x16x32_bf16 v[84:87], a[52:55], a[20:23], v[84:87]\n\t"
        "ds_read_b64_tr_b16 a[18:19], v240 offset:17920\n\t"
        "v_mfma_f32_16x16x32_bf16 v[88:91], a[56:59], a[20:23], v[88:91]\n\t"
        "global_load_dwordx4 a[40:43], v243, s[42:43] offset:2048\n\t"
        "v_mfma_f32_16x16x32_bf16 v[92:95], a[60:63], a[20:23], v[92:95]\n\t"
        "s_waitcnt lgkmcnt(12)\n\t"
        "v_mfma_f32_16x16x32_bf16 v[96:99], a[48:51], a[24:27], v[96:99]\n\t"
        "ds_read_b64_tr_b16 a[20:21], v241 offset:16896\n\t"
        "v_mfma_f32_16x16x32_bf16 v[100:103], a[52:55], a[24:27], v[100:103]\n\t"
        "ds_read_b64_tr_b16 a[22:23], v241 offset:17920\n\t"
        "v_mfma_f32_16x16x32_bf16 v[104:107], a[56:59], a[24:27], v[104:107]\n\t"
        "global_load_dwordx4 a[44:47], v243, s[42:43] offset:3072\n\t"
        "v_mfma_f32_16x16x32_bf16 v[108:111], a[60:63], a[24:27], v[108:111]\n\t"
        "s_add_u32 s42, s42, 0x2000\n\t"
        "s_addc_u32 s43, s43, 0\n\t"
        "s_waitcnt lgkmcnt(12)\n\t"
        "v_mfma_f32_16x16x32_bf16 v[112:115], a[48:51], a[28:31], v[112:115]\n\t"
        "ds_read_b64_tr_b16 a[24:25], v240 offset:17152\n\t"
        "v_mfma_f32_16x16x32_bf16 v[116:119], a[52:55], a[28:31], v[116:119]\n\t"
        "ds_read_b64_tr_b16 a[26:27], v240 offset:18176\n\t"
        "v_mfma_f32_16x16x32_bf16 v[120:123], a[56:59], a[28:31], v[120:123]\n\t"
        "v_mfma_f32_16x16x32_bf16 v[124:127], a[60:63], a[28:31], v[124:127]\n\t"
        "s_waitcnt vmcnt(16)\n\t"
        "s_barrier\n\t"
        "s_cmp_ge_u32 s62, 2\n\t"
        "s_cbranch_scc1 LNO1_%=\n\t"
        "s_add_u32 s72, s68, 266240\n\t"
        "s_addc_u32 s73, s69, 0\n\t"
        "global_load_dwordx4 a[112:115], v243, s[72:73] offset:-2048\n\t"
        "global_load_dwordx4 a[116:119], v243, s[72:73] offset:-1024\n\t"
        "global_load_dwordx4 a[120:123], v243, s[72:73] offset:0\n\t"
        "global_load_dwordx4 a[124:127], v243, s[72:73] offset:1024\n\t"
        "global_load_dwordx4 a[128:131], v243, s[72:73] offset:2048\n\t"
        "LNO1_%=:\n\t"
        "s_waitcnt lgkmcnt(12)\n\t"
        "v_mfma_f32_16x16x32_bf16 v[0:3], a[64:67], a[0:3], v[0:3]\n\t"
        "ds_read_b64_tr_b16 a[28:29], v241 offset:17152\n\t"
        "v_mfma_f32_16x16x32_bf16 v[4:7], a[68:71], a[0:3], v[4:7]\n\t"
        "ds_read_b64_tr_b16 a[30:31], v241 offset:18176\n\t"
        "v_mfma_f32_16x16x32_bf16 v[8:11], a[72:75], a[0:3], v[8:11]\n\t"
        "v_mfma_f32_16x16x32_bf16 v[12:15], a[76:79], a[0:3], v[12:15]\n\t"
        "s_waitcnt lgkmcnt(12)\n\t"
        "v_mfma_f32_16x16x32_bf16 v[16:19], a[64:67], a[4:7], v[16:19]\n\t"
        "ds_read_b64_tr_b16 a[0:1], v240 offset:24576\n\t"
        "v_mfma_f32_16x16x32_bf16 v[20:23], a[68:71], a[4:7], v[20:23]\n\t"
        "ds_read_b64_tr_b16 a[2:3], v240 offset:25600\n\t"
        "v_mfma_f32_16x16x32_bf16 v[24:27], a[72:75], a[4:7], v[24:27]\n\t"
        "global_load_lds_dwordx4 v242, s[40:41]\n\t"
        "s_add_u32 m0, m0, 0x1000\n\t"
        "v_mfma_f32_16x16x32_bf16 v[28:31], a[76:79], a[4:7], v[28:31]\n\t"
        "s_add_u32 s40, s40, 0x10000\n\t"
        "s_addc_u32 s41, s41, 0\n\t"
        "s_waitcnt lgkmcnt(12)\n\t"
        "v_mfma_f32_16x16x32_bf16 v[32:35], a[64:67], a[8:11], v[32:35]\n\t"
        "ds_read_b64_tr_b16 a[4:5], v241 offset:24576\n\t"
        "v_mfma_f32_16x16x32_bf16 v[36:39], a[68:71], a[8:11], v[36:39]\n\t"
        "ds_read_b64_tr_b16 a[6:7], v241 offset:25600\n\t"
        "v_mfma_f32_16x16x32_bf16 v[40:43], a[72:75], a[8:11], v[40:43]\n\t"
        "global_load_lds_dwordx4 v242, s[40:41]\n\t"
        "s_add_u32 m0, m0, 0x1000\n\t"
        "v_mfma_f32_16x16x32_bf16 v[44:47], a[76:79], a[8:11], v[44:47]\n\t"
        "s_add_u32 s40, s40, 0x10000\n\t"
        "s_addc_u32 s41, s41, 0\n\t"
        "s_waitcnt lgkmcnt(12)\n\t"
        "v_mfma_f32_16x16x32_bf16 v[48:51], a[64:67], a[12:15], v[48:51]\n\t"
        "ds_read_b64_tr_b16 a[8:9], v240 offset:24832\n\t"
        "v_mfma_f32_16x16x32_bf16 v[52:55], a[68:71], a[12:15], v[52:55]\n\t"
        "ds_read_b64_tr_b16 a[10:11], v240 offset:25856\n\t"
        "v_mfma_f32_16x16x32_bf16 v[56:59], a[72:75], a[12:15], v[56:59]\n\t"
        "global_load_dwordx4 a[48:51], v243, s[42:43] offset:-4096\n\t"
        "v_mfma_f32_16x16x32_bf16 v[60:63], a[76:79], a[12:15], v[60:63]\n\t"
        "s_waitcnt lgkmcnt(12)\n\t"
        "v_mfma_f32_16x16x32_bf16 v[64:67], a[64:67], a[16:19], v[64:67]\n\t"
        "ds_read_b64_tr_b16 a[12:13], v241 offset:24832\n\t"
        "v_mfma_f32_16x16x32_bf16 v[68:71], a[68:71], a[16:19], v[68:71]\n\t"
        "ds_read_b64_tr_b16 a[14:15], v241 offset:25856\n\t"
        "v_mfma_f32_16x16x32_bf16 v[72:75], a[72:75], a[16:19], v[72:75]\n\t"
        "global_load_dwordx4 a[52:55], v243, s[42:43] offset:-3072\n\t"
        "v_mfma_f32_16x16x32_bf16 v[76:79], a[76:79], a[16:19], v[76:79]\n\t"
        "s_waitcnt lgkmcnt(12)\n\t"
        "v_mfma_f32_16x16x32_bf16 v[80:83], a[64:67], a[20:23], v[80:83]\n\t"
        "ds_read_b64_tr_b16 a[16:17], v240 offset:25088\n\t"
        "v_mfma_f32_16x16x32_bf16 v[84:87], a[68:71], a[20:23], v[84:87]\n\t"
        "ds_read_b64_tr_b16 a[18:19], v240 offset:26112\n\t"
        "v_mfma_f32_16x16x32_bf16 v[88:91], a[72:75], a[20:23], v[88:91]\n\t"
        "global_load_dwordx4 a[56:59], v243, s[42:43] offset:-2048\n\t"
        "v_mfma_f32_16x16x32_bf16 v[92:95], a[76:79], a[20:23], v[92:95]\n\t"
        "s_waitcnt lgkmcnt(12)\n\t"
        "v_mfma_f32_16x16x32_bf16 v[96:99], a[64:67], a[24:27], v[96:99]\n\t"
        "ds_read_b64_tr_b16 a[20:21], v241 offset:25088\n\t"
        "v_mfma_f32_16x16x32_bf16 v[100:103], a[68:71], a[24:27], v[100:103]\n\t"
        "ds_read_b64_tr_b16 a[22:23], v241 offset:26112\n\t"
        "v_mfma_f32_16x16x32_bf16 v[104:107], a[72:75], a[24:27], v[104:107]\n\t"
        "global_load_dwordx4 a[60:63], v243, s[42:43] offset:-1024\n\t"
        "v_mfma_f32_16x16x32_bf16 v[108:111], a[76:79], a[24:27], v[108:111]\n\t"
        "s_waitcnt lgkmcnt(12)\n\t"
        "v_mfma_f32_16x16x32_bf16 v[112:115], a[64:67], a[28:31], v[112:115]\n\t"
        "ds_read_b64_tr_b16 a[24:25], v240 offset:25344\n\t"
        "v_mfma_f32_16x16x32_bf16 v[116:119], a[68:71], a[28:31], v[116:119]\n\t"
        "ds_read_b64_tr_b16 a[26:27], v240 offset:26368\n\t"
        "v_mfma_f32_16x16x32_bf16 v[120:123], a[72:75], a[28:31], v[120:123]\n\t"
        "global_load_dwordx4 a[132:135], v243, s[68:69] offset:-2048\n\t"
        "v_mfma_f32_16x16x32_bf16 v[124:127], a[76:79], a[28:31], v[124:127]\n\t"
        "global_load_dwordx4 a[136:139], v243, s[68:69] offset:-1024\n\t"
        "s_waitcnt vmcnt(18)\n\t"
        "s_barrier\n\t"
        "s_waitcnt lgkmcnt(12)\n\t"
        "v_mfma_f32_16x16x32_bf16 v[0:3], a[80:83], a[0:3], v[0:3]\n\t"
        "ds_read_b64_tr_b16 a[28:29], v241 offset:25344\n\t"
        "v_mfma_f32_16x16x32_bf16 v[4:7], a[84:87], a[0:3], v[4:7]\n\t"
        "ds_read_b64_tr_b16 a[30:31], v241 offset:26368\n\t"
        "v_mfma_f32_16x16x32_bf16 v[8:11], a[88:91], a[0:3], v[8:11]\n\t"
        "v_mfma_f32_16x16x32_bf16 v[12:15], a[92:95], a[0:3], v[12:15]\n\t"
        "s_waitcnt lgkmcnt(12)\n\t"
        "v_mfma_f32_16x16x32_bf16 v[16:19], a[80:83], a[4:7], v[16:19]\n\t"
        "ds_read_b64_tr_b16 a[0:1], v240 offset:32768\n\t"
        "v_mfma_f32_16x16x32_bf16 v[20:23], a[84:87], a[4:7], v[20:23]\n\t"
        "ds_read_b64_tr_b16 a[2:3], v240 offset:33792\n\t"
        "v_mfma_f32_16x16x32_bf16 v[24:27], a[88:91], a[4:7], v[24:27]\n\t"
        "global_load_lds_dwordx4 v242, s[40:41]\n\t"
        "s_add_u32 m0, m0, 0x1000\n\t"
        "v_mfma_f32_16x16x32_bf16 v[28:31], a[92:95], a[4:7], v[28:31]\n\t"
        "s_add_u32 s40, s40, 0x10000\n\t"
        "s_addc_u32 s41, s41, 0\n\t"
        "s_waitcnt lgkmcnt(12)\n\t"
        "v_mfma_f32_16x16x32_bf16 v[32:35], a[80:83], a[8:11], v[32:35]\n\t"
        "ds_read_b64_tr_b16 a[4:5], v241 offset:32768\n\t"
        "v_mfma_f32_16x16x32_bf16 v[36:39], a[84:87], a[8:11], v[36:39]\n\t"
        "ds_read_b64_tr_b16 a[6:7], v241 offset:33792\n\t"
        "v_mfma_f32_16x16x32_bf16 v[40:43], a[88:91], a[8:11], v[40:43]\n\t"
        "global_load_lds_dwordx4 v242, s[40:41]\n\t"
        "s_add_u32 m0, m0, 0x1000\n\t"
        "v_mfma_f32_16x16x32_bf16 v[44:47], a[92:95], a[8:11], v[44:47]\n\t"
        "s_add_u32 s40, s40, 0x10000\n\t"
        "s_addc_u32 s41, s41, 0\n\t"
        "s_waitcnt lgkmcnt(12)\n\t"
        "v_mfma_f32_16x16x32_bf16 v[48:51], a[80:83], a[12:15], v[48:51]\n\t"
        "ds_read_b64_tr_b16 a[8:9], v240 offset:33024\n\t"
        "v_mfma_f32_16x16x32_bf16 v[52:55], a[84:87], a[12:15], v[52:55]\n\t"
        "ds_read_b64_tr_b16 a[10:11], v240 offset:34048\n\t"
        "v_mfma_f32_16x16x32_bf16 v[56:59], a[88:91], a[12:15], v[56:59]\n\t"
        "global_load_dwordx4 a[64:67], v243, s[42:43] offset:0\n\t"
        "v_mfma_f32_16x16x32_bf16 v[60:63], a[92:95], a[12:15], v[60:63]\n\t"
        "s_waitcnt lgkmcnt(12)\n\t"
        "v_mfma_f32_16x16x32_bf16 v[64:67], a[80:83], a[16:19], v[64:67]\n\t"
        "ds_read_b64_tr_b16 a[12:13], v241 offset:33024\n\t"
        "v_mfma_f32_16x16x32_bf16 v[68:71], a[84:87], a[16:19], v[68:71]\n\t"
        "ds_read_b64_tr_b16 a[14:15], v241 offset:34048\n\t"
        "v_mfma_f32_16x16x32_bf16 v[72:75], a[88:91], a[16:19], v[72:75]\n\t"
        "global_load_dwordx4 a[68:71], v243, s[42:43] offset:1024\n\t"
        "v_mfma_f32_16x16x32_bf16 v[76:79], a[92:95], a[16:19], v[76:79]\n\t"
        "s_waitcnt lgkmcnt(12)\n\t"
        "v_mfma_f32_16x16x32_bf16 v[80:83], a[80:83], a[20:23], v[80:83]\n\t"
        "ds_read_b64_tr_b16 a[16:17], v240 offset:33280\n\t"
        "v_mfma_f32_16x16x32_bf16 v[84:87], a[84:87], a[20:23], v[84:87]\n\t"
        "ds_read_b64_tr_b16 a[18:19], v240 offset:34304\n\t"
        "v_mfma_f32_16x16x32_bf16 v[88:91], a[88:91], a[20:23], v[88:91]\n\t"
        "global_load_dwordx4 a[72:75], v243, s[42:43] offset:2048\n\t"
        "v_mfma_f32_16x16x32_bf16 v[92:95], a[92:95], a[20:23], v[92:95]\n\t"
        "s_waitcnt lgkmcnt(12)\n\t"
        "v_mfma_f32_16x16x32_bf16 v[96:99], a[80:83], a[24:27], v[96:99]\n\t"
        "ds_read_b64_tr_b16 a[20:21], v241 offset:33280\n\t"
        "v_mfma_f32_16x16x32_bf16 v[100:103], a[84:87], a[24:27], v[100:103]\n\t"
        "ds_read_b64_tr_b16 a[22:23], v241 offset:34304\n\t"
        "v_mfma_f32_16x16x32_bf16 v[104:107], a[88:91], a[24:27], v[104:107]\n\t"
        "global_load_dwordx4 a[76:79], v243, s[42:43] offset:3072\n\t"
        "v_mfma_f32_16x16x32_bf16 v[108:111], a[92:95], a[24:27], v[108:111]\n\t"
        "s_add_u32 s42, s42, 0x2000\n\t"
        "s_addc_u32 s43, s43, 0\n\t"
        "s_waitcnt lgkmcnt(12)\n\t"
        "v_mfma_f32_16x16x32_bf16 v[112:115], a[80:83], a[28:31], v[112:115]\n\t"
        "ds_read_b64_tr_b16 a[24:25], v240 offset:33536\n\t"
        "v_mfma_f32_16x16x32_bf16 v[116:119], a[84:87], a[28:31], v[116:119]\n\t"
        "ds_read_b64_tr_b16 a[26:27], v240 offset:34560\n\t"
        "v_mfma_f32_16x16x32_bf16 v[120:123], a[88:91], a[28:31], v[120:123]\n\t"
        "global_load_dwordx4 a[140:143], v243, s[68:69] offset:0\n\t"
        "v_mfma_f32_16x16x32_bf16 v[124:127], a[92:95], a[28:31], v[124:127]\n\t"
        "global_load_dwordx4 a[144:147], v243, s[68:69] offset:1024\n\t"
        "s_waitcnt vmcnt(20)\n\t"
        "s_barrier\n\t"
        "s_waitcnt lgkmcnt(12)\n\t"
        "v_mfma_f32_16x16x32_bf16 v[0:3], a[96:99], a[0:3], v[0:3]\n\t"
        "ds_read_b64_tr_b16 a[28:29], v241 offset:33536\n\t"
        "v_mfma_f32_16x16x32_bf16 v[4:7], a[100:103], a[0:3], v[4:7]\n\t"
        "ds_read_b64_tr_b16 a[30:31], v241 offset:34560\n\t"
        "v_mfma_f32_16x16x32_bf16 v[8:11], a[104:107], a[0:3], v[8:11]\n\t"
        "v_mfma_f32_16x16x32_bf16 v[12:15], a[108:111], a[0:3], v[12:15]\n\t"
        "s_waitcnt lgkmcnt(12)\n\t"
        "v_mfma_f32_16x16x32_bf16 v[16:19], a[96:99], a[4:7], v[16:19]\n\t"
        "ds_read_b64_tr_b16 a[0:1], v240 offset:40960\n\t"
        "v_mfma_f32_16x16x32_bf16 v[20:23], a[100:103], a[4:7], v[20:23]\n\t"
        "ds_read_b64_tr_b16 a[2:3], v240 offset:41984\n\t"
        "v_mfma_f32_16x16x32_bf16 v[24:27], a[104:107], a[4:7], v[24:27]\n\t"
        "global_load_lds_dwordx4 v242, s[40:41]\n\t"
        "s_add_u32 m0, m0, 0x1000\n\t"
        "v_mfma_f32_16x16x32_bf16 v[28:31], a[108:111], a[4:7], v[28:31]\n\t"
        "s_add_u32 s40, s40, 0x10000\n\t"
        "s_addc_u32 s41, s41, 0\n\t"
        "s_waitcnt lgkmcnt(12)\n\t"
        "v_mfma_f32_16x16x32_bf16 v[32:35], a[96:99], a[8:11], v[32:35]\n\t"
        "ds_read_b64_tr_b16 a[4:5], v241 offset:40960\n\t"
        "v_mfma_f32_16x16x32_bf16 v[36:39], a[100:103], a[8:11], v[36:39]\n\t"
        "ds_read_b64_tr_b16 a[6:7], v241 offset:41984\n\t"
        "v_mfma_f32_16x16x32_bf16 v[40:43], a[104:107], a[8:11], v[40:43]\n\t"
        "global_load_lds_dwordx4 v242, s[40:41]\n\t"
        "s_add_u32 m0, m0, 0x1000\n\t"
        "v_mfma_f32_16x16x32_bf16 v[44:47], a[108:111], a[8:11], v[44:47]\n\t"
        "s_add_u32 s40, s40, 0x10000\n\t"
        "s_addc_u32 s41, s41, 0\n\t"
        "s_waitcnt lgkmcnt(12)\n\t"
        "v_mfma_f32_16x16x32_bf16 v[48:51], a[96:99], a[12:15], v[48:51]\n\t"
        "ds_read_b64_tr_b16 a[8:9], v240 offset:41216\n\t"
        "v_mfma_f32_16x16x32_bf16 v[52:55], a[100:103], a[12:15], v[52:55]\n\t"
        "ds_read_b64_tr_b16 a[10:11], v240 offset:42240\n\t"
        "v_mfma_f32_16x16x32_bf16 v[56:59], a[104:107], a[12:15], v[56:59]\n\t"
        "global_load_dwordx4 a[80:83], v243, s[42:43] offset:-4096\n\t"
        "v_mfma_f32_16x16x32_bf16 v[60:63], a[108:111], a[12:15], v[60:63]\n\t"
        "s_waitcnt lgkmcnt(12)\n\t"
        "v_mfma_f32_16x16x32_bf16 v[64:67], a[96:99], a[16:19], v[64:67]\n\t"
        "ds_read_b64_tr_b16 a[12:13], v241 offset:41216\n\t"
        "v_mfma_f32_16x16x32_bf16 v[68:71], a[100:103], a[16:19], v[68:71]\n\t"
        "ds_read_b64_tr_b16 a[14:15], v241 offset:42240\n\t"
        "v_mfma_f32_16x16x32_bf16 v[72:75], a[104:107], a[16:19], v[72:75]\n\t"
        "global_load_dwordx4 a[84:87], v243, s[42:43] offset:-3072\n\t"
        "v_mfma_f32_16x16x32_bf16 v[76:79], a[108:111], a[16:19], v[76:79]\n\t"
        "s_waitcnt lgkmcnt(12)\n\t"
        "v_mfma_f32_16x16x32_bf16 v[80:83], a[96:99], a[20:23], v[80:83]\n\t"
        "ds_read_b64_tr_b16 a[16:17], v240 offset:41472\n\t"
        "v_mfma_f32_16x16x32_bf16 v[84:87], a[100:103], a[20:23], v[84:87]\n\t"
        "ds_read_b64_tr_b16 a[18:19], v240 offset:42496\n\t"
        "v_mfma_f32_16x16x32_bf16 v[88:91], a[104:107], a[20:23], v[88:91]\n\t"
        "global_load_dwordx4 a[88:91], v243, s[42:43] offset:-2048\n\t"
        "v_mfma_f32_16x16x32_bf16 v[92:95], a[108:111], a[20:23], v[92:95]\n\t"
        "s_waitcnt lgkmcnt(12)\n\t"
        "v_mfma_f32_16x16x32_bf16 v[96:99], a[96:99], a[24:27], v[96:99]\n\t"
        "ds_read_b64_tr_b16 a[20:21], v241 offset:41472\n\t"
        "v_mfma_f32_16x16x32_bf16 v[100:103], a[100:103], a[24:27], v[100:103]\n\t"
        "ds_read_b64_tr_b16 a[22:23], v241 offset:42496\n\t"
        "v_mfma_f32_16x16x32_bf16 v[104:107], a[104:107], a[24:27], v[104:107]\n\t"
        "global_load_dwordx4 a[92:95], v243, s[42:43] offset:-1024\n\t"
        "v_mfma_f32_16x16x32_bf16 v[108:111], a[108:111], a[24:27], v[108:111]\n\t"
        "s_waitcnt lgkmcnt(12)\n\t"
        "v_mfma_f32_16x16x32_bf16 v[112:115], a[96:99], a[28:31], v[112:115]\n\t"
        "ds_read_b64_tr_b16 a[24:25], v240 offset:41728\n\t"
        "v_mfma_f32_16x16x32_bf16 v[116:119], a[100:103], a[28:31], v[116:119]\n\t"
        "ds_read_b64_tr_b16 a[26:27], v240 offset:42752\n\t"
        "v_mfma_f32_16x16x32_bf16 v[120:123], a[104:107], a[28:31], v[120:123]\n\t"
        "global_load_dwordx4 a[148:151], v243, s[68:69] offset:2048\n\t"
        "s_add_u32 s68, s68, 0x5000\n\t"
        "s_addc_u32 s69, s69, 0\n\t"
        "v_mfma_f32_16x16x32_bf16 v[124:127], a[108:111], a[28:31], v[124:127]\n\t"
        "s_waitcnt vmcnt(21)\n\t"
        "s_barrier\n\t"
        "s_waitcnt lgkmcnt(12)\n\t"
        "v_mfma_f32_16x16x32_bf16 v[0:3], a[32:35], a[0:3], v[0:3]\n\t"
        "ds_read_b64_tr_b16 a[28:29], v241 offset:41728\n\t"
        "v_mfma_f32_16x16x32_bf16 v[4:7], a[36:39], a[0:3], v[4:7]\n\t"
        "ds_read_b64_tr_b16 a[30:31], v241 offset:42752\n\t"
        "v_mfma_f32_16x16x32_bf16 v[8:11], a[40:43], a[0:3], v[8:11]\n\t"
        "v_mfma_f32_16x16x32_bf16 v[12:15], a[44:47], a[0:3], v[12:15]\n\t"
        "s_waitcnt lgkmcnt(12)\n\t"
        "v_mfma_f32_16x16x32_bf16 v[16:19], a[32:35], a[4:7], v[16:19]\n\t"
        "ds_read_b64_tr_b16 a[0:1], v240 offset:49152\n\t"
        "v_mfma_f32_16x16x32_bf16 v[20:23], a[36:39], a[4:7], v[20:23]\n\t"
        "ds_read_b64_tr_b16 a[2:3], v240 offset:50176\n\t"
        "v_mfma_f32_16x16x32_bf16 v[24:27], a[40:43], a[4:7], v[24:27]\n\t"
        "global_load_lds_dwordx4 v242, s[40:41]\n\t"
        "s_add_u32 m0, m0, 0x1000\n\t"
        "v_mfma_f32_16x16x32_bf16 v[28:31], a[44:47], a[4:7], v[28:31]\n\t"
        "s_add_u32 s40, s40, 0x10000\n\t"
        "s_addc_u32 s41, s41, 0\n\t"
        "s_waitcnt lgkmcnt(12)\n\t"
        "v_mfma_f32_16x16x32_bf16 v[32:35], a[32:35], a[8:11], v[32:35]\n\t"
        "ds_read_b64_tr_b16 a[4:5], v241 offset:49152\n\t"
        "v_mfma_f32_16x16x32_bf16 v[36:39], a[36:39], a[8:11], v[36:39]\n\t"
        "ds_read_b64_tr_b16 a[6:7], v241 offset:50176\n\t"
        "v_mfma_f32_16x16x32_bf16 v[40:43], a[40:43], a[8:11], v[40:43]\n\t"
        "global_load_lds_dwordx4 v242, s[40:41]\n\t"
        "s_add_u32 m0, m0, 0x1000\n\t"
        "v_mfma_f32_16x16x32_bf16 v[44:47], a[44:47], a[8:11], v[44:47]\n\t"
        "s_add_u32 s40, s40, 0x10000\n\t"
        "s_addc_u32 s41, s41, 0\n\t"
        "s_waitcnt lgkmcnt(12)\n\t"
        "v_mfma_f32_16x16x32_bf16 v[48:51], a[32:35], a[12:15], v[48:51]\n\t"
        "ds_read_b64_tr_b16 a[8:9], v240 offset:49408\n\t"
        "v_mfma_f32_16x16x32_bf16 v[52:55], a[36:39], a[12:15], v[52:55]\n\t"
        "ds_read_b64_tr_b16 a[10:11], v240 offset:50432\n\t"
        "v_mfma_f32_16x16x32_bf16 v[56:59], a[40:43], a[12:15], v[56:59]\n\t"
        "global_load_dwordx4 a[96:99], v243, s[42:43] offset:0\n\t"
        "v_mfma_f32_16x16x32_bf16 v[60:63], a[44:47], a[12:15], v[60:63]\n\t"
        "s_waitcnt lgkmcnt(12)\n\t"
        "v_mfma_f32_16x16x32_bf16 v[64:67], a[32:35], a[16:19], v[64:67]\n\t"
        "ds_read_b64_tr_b16 a[12:13], v241 offset:49408\n\t"
        "v_mfma_f32_16x16x32_bf16 v[68:71], a[36:39], a[16:19], v[68:71]\n\t"
        "ds_read_b64_tr_b16 a[14:15], v241 offset:50432\n\t"
        "v_mfma_f32_16x16x32_bf16 v[72:75], a[40:43], a[16:19], v[72:75]\n\t"
        "global_load_dwordx4 a[100:103], v243, s[42:43] offset:1024\n\t"
        "v_mfma_f32_16x16x32_bf16 v[76:79], a[44:47], a[16:19], v[76:79]\n\t"
        "s_waitcnt lgkmcnt(12)\n\t"
        "v_mfma_f32_16x16x32_bf16 v[80:83], a[32:35], a[20:23], v[80:83]\n\t"
        "ds_read_b64_tr_b16 a[16:17], v240 offset:49664\n\t"
        "v_mfma_f32_16x16x32_bf16 v[84:87], a[36:39], a[20:23], v[84:87]\n\t"
        "ds_read_b64_tr_b16 a[18:19], v240 offset:50688\n\t"
        "v_mfma_f32_16x16x32_bf16 v[88:91], a[40:43], a[20:23], v[88:91]\n\t"
        "global_load_dwordx4 a[104:107], v243, s[42:43] offset:2048\n\t"
        "v_mfma_f32_16x16x32_bf16 v[92:95], a[44:47], a[20:23], v[92:95]\n\t"
        "s_waitcnt lgkmcnt(12)\n\t"
        "v_mfma_f32_16x16x32_bf16 v[96:99], a[32:35], a[24:27], v[96:99]\n\t"
        "ds_read_b64_tr_b16 a[20:21], v241 offset:49664\n\t"
        "v_mfma_f32_16x16x32_bf16 v[100:103], a[36:39], a[24:27], v[100:103]\n\t"
        "ds_read_b64_tr_b16 a[22:23], v241 offset:50688\n\t"
        "v_mfma_f32_16x16x32_bf16 v[104:107], a[40:43], a[24:27], v[104:107]\n\t"
        "global_load_dwordx4 a[108:111], v243, s[42:43] offset:3072\n\t"
        "v_mfma_f32_16x16x32_bf16 v[108:111], a[44:47], a[24:27], v[108:111]\n\t"
        "s_add_u32 s42, s42, 0x2000\n\t"
        "s_addc_u32 s43, s43, 0\n\t"
        "s_waitcnt lgkmcnt(12)\n\t"
        "v_mfma_f32_16x16x32_bf16 v[112:115], a[32:35], a[28:31], v[112:115]\n\t"
        "ds_read_b64_tr_b16 a[24:25], v240 offset:49920\n\t"
        "v_mfma_f32_16x16x32_bf16 v[116:119], a[36:39], a[28:31], v[116:119]\n\t"
        "ds_read_b64_tr_b16 a[26:27], v240 offset:50944\n\t"
        "v_mfma_f32_16x16x32_bf16 v[120:123], a[40:43], a[28:31], v[120:123]\n\t"
        "global_load_dwordx4 a[152:155], v243, s[68:69] offset:-2048\n\t"
        "v_mfma_f32_16x16x32_bf16 v[124:127], a[44:47], a[28:31], v[124:127]\n\t"
        "s_waitcnt vmcnt(20)\n\t"
        "s_barrier\n\t"
        "s_waitcnt lgkmcnt(12)\n\t"
        "v_mfma_f32_16x16x32_bf16 v[0:3], a[48:51], a[0:3], v[0:3]\n\t"
        "ds_read_b64_tr_b16 a[28:29], v241 offset:49920\n\t"
        "v_mfma_f32_16x16x32_bf16 v[4:7], a[52:55], a[0:3], v[4:7]\n\t"
        "ds_read_b64_tr_b16 a[30:31], v241 offset:50944\n\t"
        "v_mfma_f32_16x16x32_bf16 v[8:11], a[56:59], a[0:3], v[8:11]\n\t"
        "v_mfma_f32_16x16x32_bf16 v[12:15], a[60:63], a[0:3], v[12:15]\n\t"
        "s_waitcnt lgkmcnt(12)\n\t"
        "v_mfma_f32_16x16x32_bf16 v[16:19], a[48:51], a[4:7], v[16:19]\n\t"
        "ds_read_b64_tr_b16 a[0:1], v240 offset:57344\n\t"
        "v_mfma_f32_16x16x32_bf16 v[20:23], a[52:55], a[4:7], v[20:23]\n\t"
        "ds_read_b64_tr_b16 a[2:3], v240 offset:58368\n\t"
        "v_mfma_f32_16x16x32_bf16 v[24:27], a[56:59], a[4:7], v[24:27]\n\t"
        "global_load_lds_dwordx4 v242, s[40:41]\n\t"
        "s_add_u32 m0, m0, 0x1000\n\t"
        "v_mfma_f32_16x16x32_bf16 v[28:31], a[60:63], a[4:7], v[28:31]\n\t"
        "s_add_u32 s40, s40, 0x10000\n\t"
        "s_addc_u32 s41, s41, 0\n\t"
        "s_waitcnt lgkmcnt(12)\n\t"
        "v_mfma_f32_16x16x32_bf16 v[32:35], a[48:51], a[8:11], v[32:35]\n\t"
        "ds_read_b64_tr_b16 a[4:5], v241 offset:57344\n\t"
        "v_mfma_f32_16x16x32_bf16 v[36:39], a[52:55], a[8:11], v[36:39]\n\t"
        "ds_read_b64_tr_b16 a[6:7], v241 offset:58368\n\t"
        "v_mfma_f32_16x16x32_bf16 v[40:43], a[56:59], a[8:11], v[40:43]\n\t"
        "global_load_lds_dwordx4 v242, s[40:41]\n\t"
        "s_add_u32 m0, m0, 0x1000\n\t"
        "v_mfma_f32_16x16x32_bf16 v[44:47], a[60:63], a[8:11], v[44:47]\n\t"
        "s_add_u32 s40, s40, 0x10000\n\t"
        "s_addc_u32 s41, s41, 0\n\t"
        "s_waitcnt lgkmcnt(12)\n\t"
        "v_mfma_f32_16x16x32_bf16 v[48:51], a[48:51], a[12:15], v[48:51]\n\t"
        "ds_read_b64_tr_b16 a[8:9], v240 offset:57600\n\t"
        "v_mfma_f32_16x16x32_bf16 v[52:55], a[52:55], a[12:15], v[52:55]\n\t"
        "ds_read_b64_tr_b16 a[10:11], v240 offset:58624\n\t"
        "v_mfma_f32_16x16x32_bf16 v[56:59], a[56:59], a[12:15], v[56:59]\n\t"
        "global_load_dwordx4 a[32:35], v243, s[42:43] offset:-4096\n\t"
        "v_mfma_f32_16x16x32_bf16 v[60:63], a[60:63], a[12:15], v[60:63]\n\t"
        "s_waitcnt lgkmcnt(12)\n\t"
        "v_mfma_f32_16x16x32_bf16 v[64:67], a[48:51], a[16:19], v[64:67]\n\t"
        "ds_read_b64_tr_b16 a[12:13], v241 offset:57600\n\t"
        "v_mfma_f32_16x16x32_bf16 v[68:71], a[52:55], a[16:19], v[68:71]\n\t"
        "ds_read_b64_tr_b16 a[14:15], v241 offset:58624\n\t"
        "v_mfma_f32_16x16x32_bf16 v[72:75], a[56:59], a[16:19], v[72:75]\n\t"
        "global_load_dwordx4 a[36:39], v243, s[42:43] offset:-3072\n\t"
        "v_mfma_f32_16x16x32_bf16 v[76:79], a[60:63], a[16:19], v[76:79]\n\t"
        "s_waitcnt lgkmcnt(12)\n\t"
        "v_mfma_f32_16x16x32_bf16 v[80:83], a[48:51], a[20:23], v[80:83]\n\t"
        "ds_read_b64_tr_b16 a[16:17], v240 offset:57856\n\t"
        "v_mfma_f32_16x16x32_bf16 v[84:87], a[52:55], a[20:23], v[84:87]\n\t"
        "ds_read_b64_tr_b16 a[18:19], v240 offset:58880\n\t"
        "v_mfma_f32_16x16x32_bf16 v[88:91], a[56:59], a[20:23], v[88:91]\n\t"
        "global_load_dwordx4 a[40:43], v243, s[42:43] offset:-2048\n\t"
        "v_mfma_f32_16x16x32_bf16 v[92:95], a[60:63], a[20:23], v[92:95]\n\t"
        "s_waitcnt lgkmcnt(12)\n\t"
        "v_mfma_f32_16x16x32_bf16 v[96:99], a[48:51], a[24:27], v[96:99]\n\t"
        "ds_read_b64_tr_b16 a[20:21], v241 offset:57856\n\t"
        "v_mfma_f32_16x16x32_bf16 v[100:103], a[52:55], a[24:27], v[100:103]\n\t"
        "ds_read_b64_tr_b16 a[22:23], v241 offset:58880\n\t"
        "v_mfma_f32_16x16x32_bf16 v[104:107], a[56:59], a[24:27], v[104:107]\n\t"
        "global_load_dwordx4 a[44:47], v243, s[42:43] offset:-1024\n\t"
        "v_mfma_f32_16x16x32_bf16 v[108:111], a[60:63], a[24:27], v[108:111]\n\t"
        "s_waitcnt lgkmcnt(12)\n\t"
        "v_mfma_f32_16x16x32_bf16 v[112:115], a[48:51], a[28:31], v[112:115]\n\t"
        "ds_read_b64_tr_b16 a[24:25], v240 offset:58112\n\t"
        "v_mfma_f32_16x16x32_bf16 v[116:119], a[52:55], a[28:31], v[116:119]\n\t"
        "ds_read_b64_tr_b16 a[26:27], v240 offset:59136\n\t"
        "v_mfma_f32_16x16x32_bf16 v[120:123], a[56:59], a[28:31], v[120:123]\n\t"
        "global_load_dwordx4 a[156:159], v243, s[68:69] offset:-1024\n\t"
        "v_mfma_f32_16x16x32_bf16 v[124:127], a[60:63], a[28:31], v[124:127]\n\t"
        "s_waitcnt vmcnt(19)\n\t"
        "s_barrier\n\t"
        "s_waitcnt lgkmcnt(12)\n\t"
        "v_mfma_f32_16x16x32_bf16 v[0:3], a[64:67], a[0:3], v[0:3]\n\t"
        "ds_read_b64_tr_b16 a[28:29], v241 offset:58112\n\t"
        "v_mfma_f32_16x16x32_bf16 v[4:7], a[68:71], a[0:3], v[4:7]\n\t"
        "ds_read_b64_tr_b16 a[30:31], v241 offset:59136\n\t"
        "v_mfma_f32_16x16x32_bf16 v[8:11], a[72:75], a[0:3], v[8:11]\n\t"
        "v_add_u32 v240, 0x10000, v240\n\t"
        "v_add_u32 v241, 0x10000, v241\n\t"
        "v_mfma_f32_16x16x32_bf16 v[12:15], a[76:79], a[0:3], v[12:15]\n\t"
        "s_waitcnt lgkmcnt(12)\n\t"
        "v_mfma_f32_16x16x32_bf16 v[16:19], a[64:67], a[4:7], v[16:19]\n\t"
        "ds_read_b64_tr_b16 a[0:1], v240 offset:0\n\t"
        "v_mfma_f32_16x16x32_bf16 v[20:23], a[68:71], a[4:7], v[20:23]\n\t"
        "ds_read_b64_tr_b16 a[2:3], v240 offset:1024\n\t"
        "v_mfma_f32_16x16x32_bf16 v[24:27], a[72:75], a[4:7], v[24:27]\n\t"
        "global_load_lds_dwordx4 v242, s[40:41]\n\t"
        "s_add_u32 m0, m0, 0x1000\n\t"
        "v_mfma_f32_16x16x32_bf16 v[28:31], a[76:79], a[4:7], v[28:31]\n\t"
        "s_add_u32 s40, s40, 0x10000\n\t"
        "s_addc_u32 s41, s41, 0\n\t"
        "s_waitcnt lgkmcnt(12)\n\t"
        "v_mfma_f32_16x16x32_bf16 v[32:35], a[64:67], a[8:11], v[32:35]\n\t"
        "ds_read_b64_tr_b16 a[4:5], v241 offset:0\n\t"
        "v_mfma_f32_16x16x32_bf16 v[36:39], a[68:71], a[8:11], v[36:39]\n\t"
        "ds_read_b64_tr_b16 a[6:7], v241 offset:1024\n\t"
        "v_mfma_f32_16x16x32_bf16 v[40:43], a[72:75], a[8:11], v[40:43]\n\t"
        "global_load_lds_dwordx4 v242, s[40:41]\n\t"
        "s_add_u32 m0, m0, 0x1000\n\t"
        "v_mfma_f32_16x16x32_bf16 v[44:47], a[76:79], a[8:11], v[44:47]\n\t"
        "s_add_u32 s40, s40, 0x10000\n\t"
        "s_addc_u32 s41, s41, 0\n\t"
        "s_waitcnt lgkmcnt(12)\n\t"
        "v_mfma_f32_16x16x32_bf16 v[48:51], a[64:67], a[12:15], v[48:51]\n\t"
        "ds_read_b64_tr_b16 a[8:9], v240 offset:256\n\t"
        "v_mfma_f32_16x16x32_bf16 v[52:55], a[68:71], a[12:15], v[52:55]\n\t"
        "ds_read_b64_tr_b16 a[10:11], v240 offset:1280\n\t"
        "v_mfma_f32_16x16x32_bf16 v[56:59], a[72:75], a[12:15], v[56:59]\n\t"
        "global_load_dwordx4 a[48:51], v243, s[42:43] offset:0\n\t"
        "v_mfma_f32_16x16x32_bf16 v[60:63], a[76:79], a[12:15], v[60:63]\n\t"
        "s_waitcnt lgkmcnt(12)\n\t"
        "v_mfma_f32_16x16x32_bf16 v[64:67], a[64:67], a[16:19], v[64:67]\n\t"
        "ds_read_b64_tr_b16 a[12:13], v241 offset:256\n\t"
        "v_mfma_f32_16x16x32_bf16 v[68:71], a[68:71], a[16:19], v[68:71]\n\t"
        "ds_read_b64_tr_b16 a[14:15], v241 offset:1280\n\t"
        "v_mfma_f32_16x16x32_bf16 v[72:75], a[72:75], a[16:19], v[72:75]\n\t"
        "global_load_dwordx4 a[52:55], v243, s[42:43] offset:1024\n\t"
        "v_mfma_f32_16x16x32_bf16 v[76:79], a[76:79], a[16:19], v[76:79]\n\t"
        "s_waitcnt lgkmcnt(12)\n\t"
        "v_mfma_f32_16x16x32_bf16 v[80:83], a[64:67], a[20:23], v[80:83]\n\t"
        "ds_read_b64_tr_b16 a[16:17], v240 offset:512\n\t"
        "v_mfma_f32_16x16x32_bf16 v[84:87], a[68:71], a[20:23], v[84:87]\n\t"
        "ds_read_b64_tr_b16 a[18:19], v240 offset:1536\n\t"
        "v_mfma_f32_16x16x32_bf16 v[88:91], a[72:75], a[20:23], v[88:91]\n\t"
        "global_load_dwordx4 a[56:59], v243, s[42:43] offset:2048\n\t"
        "v_mfma_f32_16x16x32_bf16 v[92:95], a[76:79], a[20:23], v[92:95]\n\t"
        "s_waitcnt lgkmcnt(12)\n\t"
        "v_mfma_f32_16x16x32_bf16 v[96:99], a[64:67], a[24:27], v[96:99]\n\t"
        "ds_read_b64_tr_b16 a[20:21], v241 offset:512\n\t"
        "v_mfma_f32_16x16x32_bf16 v[100:103], a[68:71], a[24:27], v[100:103]\n\t"
        "ds_read_b64_tr_b16 a[22:23], v241 offset:1536\n\t"
        "v_mfma_f32_16x16x32_bf16 v[104:107], a[72:75], a[24:27], v[104:107]\n\t"
        "global_load_dwordx4 a[60:63], v243, s[42:43] offset:3072\n\t"
        "v_mfma_f32_16x16x32_bf16 v[108:111], a[76:79], a[24:27], v[108:111]\n\t"
        "s_add_u32 s42, s42, 0x2000\n\t"
        "s_addc_u32 s43, s43, 0\n\t"
        "s_waitcnt lgkmcnt(12)\n\t"
        "v_mfma_f32_16x16x32_bf16 v[112:115], a[64:67], a[28:31], v[112:115]\n\t"
        "ds_read_b64_tr_b16 a[24:25], v240 offset:768\n\t"
        "v_mfma_f32_16x16x32_bf16 v[116:119], a[68:71], a[28:31], v[116:119]\n\t"
        "ds_read_b64_tr_b16 a[26:27], v240 offset:1792\n\t"
        "v_mfma_f32_16x16x32_bf16 v[120:123], a[72:75], a[28:31], v[120:123]\n\t"
        "global_load_dwordx4 a[160:163], v243, s[68:69] offset:0\n\t"
        "v_mfma_f32_16x16x32_bf16 v[124:127], a[76:79], a[28:31], v[124:127]\n\t"
        "s_waitcnt vmcnt(19)\n\t"
        "s_barrier\n\t"
        "s_waitcnt lgkmcnt(12)\n\t"
        "v_mfma_f32_16x16x32_bf16 v[0:3], a[80:83], a[0:3], v[0:3]\n\t"
        "ds_read_b64_tr_b16 a[28:29], v241 offset:768\n\t"
        "v_mfma_f32_16x16x32_bf16 v[4:7], a[84:87], a[0:3], v[4:7]\n\t"
        "ds_read_b64_tr_b16 a[30:31], v241 offset:1792\n\t"
        "v_mfma_f32_16x16x32_bf16 v[8:11], a[88:91], a[0:3], v[8:11]\n\t"
        "v_mfma_f32_16x16x32_bf16 v[12:15], a[92:95], a[0:3], v[12:15]\n\t"
        "s_waitcnt lgkmcnt(12)\n\t"
        "v_mfma_f32_16x16x32_bf16 v[16:19], a[80:83], a[4:7], v[16:19]\n\t"
        "ds_read_b64_tr_b16 a[0:1], v240 offset:8192\n\t"
        "v_mfma_f32_16x16x32_bf16 v[20:23], a[84:87], a[4:7], v[20:23]\n\t"
        "ds_read_b64_tr_b16 a[2:3], v240 offset:9216\n\t"
        "v_mfma_f32_16x16x32_bf16 v[24:27], a[88:91], a[4:7], v[24:27]\n\t"
        "global_load_lds_dwordx4 v242, s[40:41]\n\t"
        "s_add_u32 m0, m0, 0x1000\n\t"
        "v_mfma_f32_16x16x32_bf16 v[28:31], a[92:95], a[4:7], v[28:31]\n\t"
        "s_add_u32 s40, s40, 0x10000\n\t"
        "s_addc_u32 s41, s41, 0\n\t"
        "s_waitcnt lgkmcnt(12)\n\t"
        "v_mfma_f32_16x16x32_bf16 v[32:35], a[80:83], a[8:11], v[32:35]\n\t"
        "ds_read_b64_tr_b16 a[4:5], v241 offset:8192\n\t"
        "v_mfma_f32_16x16x32_bf16 v[36:39], a[84:87], a[8:11], v[36:39]\n\t"
        "ds_read_b64_tr_b16 a[6:7], v241 offset:9216\n\t"
        "v_mfma_f32_16x16x32_bf16 v[40:43], a[88:91], a[8:11], v[40:43]\n\t"
        "global_load_lds_dwordx4 v242, s[40:41]\n\t"
        "s_add_u32 m0, m0, 0x1000\n\t"
        "v_mfma_f32_16x16x32_bf16 v[44:47], a[92:95], a[8:11], v[44:47]\n\t"
        "s_add_u32 s40, s40, 0x10000\n\t"
        "s_addc_u32 s41, s41, 0\n\t"
        "s_waitcnt lgkmcnt(12)\n\t"
        "v_mfma_f32_16x16x32_bf16 v[48:51], a[80:83], a[12:15], v[48:51]\n\t"
        "ds_read_b64_tr_b16 a[8:9], v240 offset:8448\n\t"
        "v_mfma_f32_16x16x32_bf16 v[52:55], a[84:87], a[12:15], v[52:55]\n\t"
        "ds_read_b64_tr_b16 a[10:11], v240 offset:9472\n\t"
        "v_mfma_f32_16x16x32_bf16 v[56:59], a[88:91], a[12:15], v[56:59]\n\t"
        "global_load_dwordx4 a[64:67], v243, s[42:43] offset:-4096\n\t"
        "v_mfma_f32_16x16x32_bf16 v[60:63], a[92:95], a[12:15], v[60:63]\n\t"
        "s_waitcnt lgkmcnt(12)\n\t"
        "v_mfma_f32_16x16x32_bf16 v[64:67], a[80:83], a[16:19], v[64:67]\n\t"
        "ds_read_b64_tr_b16 a[12:13], v241 offset:8448\n\t"
        "v_mfma_f32_16x16x32_bf16 v[68:71], a[84:87], a[16:19], v[68:71]\n\t"
        "ds_read_b64_tr_b16 a[14:15], v241 offset:9472\n\t"
        "v_mfma_f32_16x16x32_bf16 v[72:75], a[88:91], a[16:19], v[72:75]\n\t"
        "global_load_dwordx4 a[68:71], v243, s[42:43] offset:-3072\n\t"
        "v_mfma_f32_16x16x32_bf16 v[76:79], a[92:95], a[16:19], v[76:79]\n\t"
        "s_waitcnt lgkmcnt(12)\n\t"
        "v_mfma_f32_16x16x32_bf16 v[80:83], a[80:83], a[20:23], v[80:83]\n\t"
        "ds_read_b64_tr_b16 a[16:17], v240 offset:8704\n\t"
        "v_mfma_f32_16x16x32_bf16 v[84:87], a[84:87], a[20:23], v[84:87]\n\t"
        "ds_read_b64_tr_b16 a[18:19], v240 offset:9728\n\t"
        "v_mfma_f32_16x16x32_bf16 v[88:91], a[88:91], a[20:23], v[88:91]\n\t"
        "global_load_dwordx4 a[72:75], v243, s[42:43] offset:-2048\n\t"
        "v_mfma_f32_16x16x32_bf16 v[92:95], a[92:95], a[20:23], v[92:95]\n\t"
        "s_waitcnt lgkmcnt(12)\n\t"
        "v_mfma_f32_16x16x32_bf16 v[96:99], a[80:83], a[24:27], v[96:99]\n\t"
        "ds_read_b64_tr_b16 a[20:21], v241 offset:8704\n\t"
        "v_mfma_f32_16x16x32_bf16 v[100:103], a[84:87], a[24:27], v[100:103]\n\t"
        "ds_read_b64_tr_b16 a[22:23], v241 offset:9728\n\t"
        "v_mfma_f32_16x16x32_bf16 v[104:107], a[88:91], a[24:27], v[104:107]\n\t"
        "global_load_dwordx4 a[76:79], v243, s[42:43] offset:-1024\n\t"
        "v_mfma_f32_16x16x32_bf16 v[108:111], a[92:95], a[24:27], v[108:111]\n\t"
        "s_waitcnt lgkmcnt(12)\n\t"
        "v_mfma_f32_16x16x32_bf16 v[112:115], a[80:83], a[28:31], v[112:115]\n\t"
        "ds_read_b64_tr_b16 a[24:25], v240 offset:8960\n\t"
        "v_mfma_f32_16x16x32_bf16 v[116:119], a[84:87], a[28:31], v[116:119]\n\t"
        "ds_read_b64_tr_b16 a[26:27], v240 offset:9984\n\t"
        "v_mfma_f32_16x16x32_bf16 v[120:123], a[88:91], a[28:31], v[120:123]\n\t"
        "global_load_dwordx4 a[164:167], v243, s[68:69] offset:1024\n\t"
        "v_mfma_f32_16x16x32_bf16 v[124:127], a[92:95], a[28:31], v[124:127]\n\t"
        "s_waitcnt vmcnt(19)\n\t"
        "s_barrier\n\t"
        "s_waitcnt lgkmcnt(12)\n\t"
        "v_mfma_f32_16x16x32_bf16 v[0:3], a[96:99], a[0:3], v[0:3]\n\t"
        "ds_read_b64_tr_b16 a[28:29], v241 offset:8960\n\t"
        "v_mfma_f32_16x16x32_bf16 v[4:7], a[100:103], a[0:3], v[4:7]\n\t"
        "ds_read_b64_tr_b16 a[30:31], v241 offset:9984\n\t"
        "v_mfma_f32_16x16x32_bf16 v[8:11], a[104:107], a[0:3], v[8:11]\n\t"
        "v_mfma_f32_16x16x32_bf16 v[12:15], a[108:111], a[0:3], v[12:15]\n\t"
        "s_waitcnt lgkmcnt(12)\n\t"
        "v_mfma_f32_16x16x32_bf16 v[16:19], a[96:99], a[4:7], v[16:19]\n\t"
        "ds_read_b64_tr_b16 a[0:1], v240 offset:16384\n\t"
        "v_mfma_f32_16x16x32_bf16 v[20:23], a[100:103], a[4:7], v[20:23]\n\t"
        "ds_read_b64_tr_b16 a[2:3], v240 offset:17408\n\t"
        "v_mfma_f32_16x16x32_bf16 v[24:27], a[104:107], a[4:7], v[24:27]\n\t"
        "global_load_lds_dwordx4 v242, s[40:41]\n\t"
        "s_add_u32 m0, m0, 0x1000\n\t"
        "v_mfma_f32_16x16x32_bf16 v[28:31], a[108:111], a[4:7], v[28:31]\n\t"
        "s_add_u32 s40, s40, 0x10000\n\t"
        "s_addc_u32 s41, s41, 0\n\t"
        "s_waitcnt lgkmcnt(12)\n\t"
        "v_mfma_f32_16x16x32_bf16 v[32:35], a[96:99], a[8:11], v[32:35]\n\t"
        "ds_read_b64_tr_b16 a[4:5], v241 offset:16384\n\t"
        "v_mfma_f32_16x16x32_bf16 v[36:39], a[100:103], a[8:11], v[36:39]\n\t"
        "ds_read_b64_tr_b16 a[6:7], v241 offset:17408\n\t"
        "v_mfma_f32_16x16x32_bf16 v[40:43], a[104:107], a[8:11], v[40:43]\n\t"
        "global_load_lds_dwordx4 v242, s[40:41]\n\t"
        "s_add_u32 m0, m0, 0x1000\n\t"
        "v_mfma_f32_16x16x32_bf16 v[44:47], a[108:111], a[8:11], v[44:47]\n\t"
        "s_add_u32 s40, s40, 0x10000\n\t"
        "s_addc_u32 s41, s41, 0\n\t"
        "s_waitcnt lgkmcnt(12)\n\t"
        "v_mfma_f32_16x16x32_bf16 v[48:51], a[96:99], a[12:15], v[48:51]\n\t"
        "ds_read_b64_tr_b16 a[8:9], v240 offset:16640\n\t"
        "v_mfma_f32_16x16x32_bf16 v[52:55], a[100:103], a[12:15], v[52:55]\n\t"
        "ds_read_b64_tr_b16 a[10:11], v240 offset:17664\n\t"
        "v_mfma_f32_16x16x32_bf16 v[56:59], a[104:107], a[12:15], v[56:59]\n\t"
        "global_load_dwordx4 a[80:83], v243, s[42:43] offset:0\n\t"
        "v_mfma_f32_16x16x32_bf16 v[60:63], a[108:111], a[12:15], v[60:63]\n\t"
        "s_waitcnt lgkmcnt(12)\n\t"
        "v_mfma_f32_16x16x32_bf16 v[64:67], a[96:99], a[16:19], v[64:67]\n\t"
        "ds_read_b64_tr_b16 a[12:13], v241 offset:16640\n\t"
        "v_mfma_f32_16x16x32_bf16 v[68:71], a[100:103], a[16:19], v[68:71]\n\t"
        "ds_read_b64_tr_b16 a[14:15], v241 offset:17664\n\t"
        "v_mfma_f32_16x16x32_bf16 v[72:75], a[104:107], a[16:19], v[72:75]\n\t"
        "global_load_dwordx4 a[84:87], v243, s[42:43] offset:1024\n\t"
        "v_mfma_f32_16x16x32_bf16 v[76:79], a[108:111], a[16:19], v[76:79]\n\t"
        "s_waitcnt lgkmcnt(12)\n\t"
        "v_mfma_f32_16x16x32_bf16 v[80:83], a[96:99], a[20:23], v[80:83]\n\t"
        "ds_read_b64_tr_b16 a[16:17], v240 offset:16896\n\t"
        "v_mfma_f32_16x16x32_bf16 v[84:87], a[100:103], a[20:23], v[84:87]\n\t"
        "ds_read_b64_tr_b16 a[18:19], v240 offset:17920\n\t"
        "v_mfma_f32_16x16x32_bf16 v[88:91], a[104:107], a[20:23], v[88:91]\n\t"
        "global_load_dwordx4 a[88:91], v243, s[42:43] offset:2048\n\t"
        "v_mfma_f32_16x16x32_bf16 v[92:95], a[108:111], a[20:23], v[92:95]\n\t"
        "s_waitcnt lgkmcnt(12)\n\t"
        "v_mfma_f32_16x16x32_bf16 v[96:99], a[96:99], a[24:27], v[96:99]\n\t"
        "ds_read_b64_tr_b16 a[20:21], v241 offset:16896\n\t"
        "v_mfma_f32_16x16x32_bf16 v[100:103], a[100:103], a[24:27], v[100:103]\n\t"
        "ds_read_b64_tr_b16 a[22:23], v241 offset:17920\n\t"
        "v_mfma_f32_16x16x32_bf16 v[104:107], a[104:107], a[24:27], v[104:107]\n\t"
        "global_load_dwordx4 a[92:95], v243, s[42:43] offset:3072\n\t"
        "v_mfma_f32_16x16x32_bf16 v[108:111], a[108:111], a[24:27], v[108:111]\n\t"
        "s_add_u32 s42, s42, 0x2000\n\t"
        "s_addc_u32 s43, s43, 0\n\t"
        "s_waitcnt lgkmcnt(12)\n\t"
        "v_mfma_f32_16x16x32_bf16 v[112:115], a[96:99], a[28:31], v[112:115]\n\t"
        "ds_read_b64_tr_b16 a[24:25], v240 offset:17152\n\t"
        "v_mfma_f32_16x16x32_bf16 v[116:119], a[100:103], a[28:31], v[116:119]\n\t"
        "ds_read_b64_tr_b16 a[26:27], v240 offset:18176\n\t"
        "v_mfma_f32_16x16x32_bf16 v[120:123], a[104:107], a[28:31], v[120:123]\n\t"
        "global_load_dwordx4 a[168:171], v243, s[68:69] offset:2048\n\t"
        "s_add_u32 s68, s68, 0x5000\n\t"
        "s_addc_u32 s69, s69, 0\n\t"
        "v_mfma_f32_16x16x32_bf16 v[124:127], a[108:111], a[28:31], v[124:127]\n\t"
        "s_waitcnt vmcnt(19)\n\t"
        "s_barrier\n\t"
        "s_waitcnt lgkmcnt(12)\n\t"
        "v_mfma_f32_16x16x32_bf16 v[0:3], a[32:35], a[0:3], v[0:3]\n\t"
        "ds_read_b64_tr_b16 a[28:29], v241 offset:17152\n\t"
        "v_mfma_f32_16x16x32_bf16 v[4:7], a[36:39], a[0:3], v[4:7]\n\t"
        "ds_read_b64_tr_b16 a[30:31], v241 offset:18176\n\t"
        "v_mfma_f32_16x16x32_bf16 v[8:11], a[40:43], a[0:3], v[8:11]\n\t"
        "v_mfma_f32_16x16x32_bf16 v[12:15], a[44:47], a[0:3], v[12:15]\n\t"
        "s_waitcnt lgkmcnt(12)\n\t"
        "v_mfma_f32_16x16x32_bf16 v[16:19], a[32:35], a[4:7], v[16:19]\n\t"
        "ds_read_b64_tr_b16 a[0:1], v240 offset:24576\n\t"
        "v_mfma_f32_16x16x32_bf16 v[20:23], a[36:39], a[4:7], v[20:23]\n\t"
        "ds_read_b64_tr_b16 a[2:3], v240 offset:25600\n\t"
        "v_mfma_f32_16x16x32_bf16 v[24:27], a[40:43], a[4:7], v[24:27]\n\t"
        "global_load_lds_dwordx4 v242, s[40:41]\n\t"
        "s_add_u32 m0, m0, 0x1000\n\t"
        "v_mfma_f32_16x16x32_bf16 v[28:31], a[44:47], a[4:7], v[28:31]\n\t"
        "s_add_u32 s40, s40, 0x10000\n\t"
        "s_addc_u32 s41, s41, 0\n\t"
        "s_waitcnt lgkmcnt(12)\n\t"
        "v_mfma_f32_16x16x32_bf16 v[32:35], a[32:35], a[8:11], v[32:35]\n\t"
        "ds_read_b64_tr_b16 a[4:5], v241 offset:24576\n\t"
        "v_mfma_f32_16x16x32_bf16 v[36:39], a[36:39], a[8:11], v[36:39]\n\t"
        "ds_read_b64_tr_b16 a[6:7], v241 offset:25600\n\t"
        "v_mfma_f32_16x16x32_bf16 v[40:43], a[40:43], a[8:11], v[40:43]\n\t"
        "global_load_lds_dwordx4 v242, s[40:41]\n\t"
        "s_add_u32 m0, m0, 0x1000\n\t"
        "v_mfma_f32_16x16x32_bf16 v[44:47], a[44:47], a[8:11], v[44:47]\n\t"
        "s_add_u32 s40, s40, 0x10000\n\t"
        "s_addc_u32 s41, s41, 0\n\t"
        "s_waitcnt lgkmcnt(12)\n\t"
        "v_mfma_f32_16x16x32_bf16 v[48:51], a[32:35], a[12:15], v[48:51]\n\t"
        "ds_read_b64_tr_b16 a[8:9], v240 offset:24832\n\t"
        "v_mfma_f32_16x16x32_bf16 v[52:55], a[36:39], a[12:15], v[52:55]\n\t"
        "ds_read_b64_tr_b16 a[10:11], v240 offset:25856\n\t"
        "v_mfma_f32_16x16x32_bf16 v[56:59], a[40:43], a[12:15], v[56:59]\n\t"
        "global_load_dwordx4 a[96:99], v243, s[42:43] offset:-4096\n\t"
        "v_mfma_f32_16x16x32_bf16 v[60:63], a[44:47], a[12:15], v[60:63]\n\t"
        "s_waitcnt lgkmcnt(12)\n\t"
        "v_mfma_f32_16x16x32_bf16 v[64:67], a[32:35], a[16:19], v[64:67]\n\t"
        "ds_read_b64_tr_b16 a[12:13], v241 offset:24832\n\t"
        "v_mfma_f32_16x16x32_bf16 v[68:71], a[36:39], a[16:19], v[68:71]\n\t"
        "ds_read_b64_tr_b16 a[14:15], v241 offset:25856\n\t"
        "v_mfma_f32_16x16x32_bf16 v[72:75], a[40:43], a[16:19], v[72:75]\n\t"
        "global_load_dwordx4 a[100:103], v243, s[42:43] offset:-3072\n\t"
        "v_mfma_f32_16x16x32_bf16 v[76:79], a[44:47], a[16:19], v[76:79]\n\t"
        "s_waitcnt lgkmcnt(12)\n\t"
        "v_mfma_f32_16x16x32_bf16 v[80:83], a[32:35], a[20:23], v[80:83]\n\t"
        "ds_read_b64_tr_b16 a[16:17], v240 offset:25088\n\t"
        "v_mfma_f32_16x16x32_bf16 v[84:87], a[36:39], a[20:23], v[84:87]\n\t"
        "ds_read_b64_tr_b16 a[18:19], v240 offset:26112\n\t"
        "v_mfma_f32_16x16x32_bf16 v[88:91], a[40:43], a[20:23], v[88:91]\n\t"
        "global_load_dwordx4 a[104:107], v243, s[42:43] offset:-2048\n\t"
        "v_mfma_f32_16x16x32_bf16 v[92:95], a[44:47], a[20:23], v[92:95]\n\t"
        "s_waitcnt lgkmcnt(12)\n\t"
        "v_mfma_f32_16x16x32_bf16 v[96:99], a[32:35], a[24:27], v[96:99]\n\t"
        "ds_read_b64_tr_b16 a[20:21], v241 offset:25088\n\t"
        "v_mfma_f32_16x16x32_bf16 v[100:103], a[36:39], a[24:27], v[100:103]\n\t"
        "ds_read_b64_tr_b16 a[22:23], v241 offset:26112\n\t"
        "v_mfma_f32_16x16x32_bf16 v[104:107], a[40:43], a[24:27], v[104:107]\n\t"
        "global_load_dwordx4 a[108:111], v243, s[42:43] offset:-1024\n\t"
        "v_mfma_f32_16x16x32_bf16 v[108:111], a[44:47], a[24:27], v[108:111]\n\t"
        "s_waitcnt lgkmcnt(12)\n\t"
        "v_mfma_f32_16x16x32_bf16 v[112:115], a[32:35], a[28:31], v[112:115]\n\t"
        "ds_read_b64_tr_b16 a[24:25], v240 offset:25344\n\t"
        "v_mfma_f32_16x16x32_bf16 v[116:119], a[36:39], a[28:31], v[116:119]\n\t"
        "ds_read_b64_tr_b16 a[26:27], v240 offset:26368\n\t"
        "v_mfma_f32_16x16x32_bf16 v[120:123], a[40:43], a[28:31], v[120:123]\n\t"
        "global_load_dwordx4 a[172:175], v243, s[68:69] offset:-2048\n\t"
        "v_mfma_f32_16x16x32_bf16 v[124:127], a[44:47], a[28:31], v[124:127]\n\t"
        "s_waitcnt vmcnt(19)\n\t"
        "s_barrier\n\t"
        "s_waitcnt lgkmcnt(12)\n\t"
        "v_mfma_f32_16x16x32_bf16 v[0:3], a[48:51], a[0:3], v[0:3]\n\t"
        "ds_read_b64_tr_b16 a[28:29], v241 offset:25344\n\t"
        "v_mfma_f32_16x16x32_bf16 v[4:7], a[52:55], a[0:3], v[4:7]\n\t"
        "ds_read_b64_tr_b16 a[30:31], v241 offset:26368\n\t"
        "v_mfma_f32_16x16x32_bf16 v[8:11], a[56:59], a[0:3], v[8:11]\n\t"
        "v_mfma_f32_16x16x32_bf16 v[12:15], a[60:63], a[0:3], v[12:15]\n\t"
        "s_waitcnt lgkmcnt(12)\n\t"
        "v_mfma_f32_16x16x32_bf16 v[16:19], a[48:51], a[4:7], v[16:19]\n\t"
        "ds_read_b64_tr_b16 a[0:1], v240 offset:32768\n\t"
        "v_mfma_f32_16x16x32_bf16 v[20:23], a[52:55], a[4:7], v[20:23]\n\t"
        "ds_read_b64_tr_b16 a[2:3], v240 offset:33792\n\t"
        "v_mfma_f32_16x16x32_bf16 v[24:27], a[56:59], a[4:7], v[24:27]\n\t"
        "global_load_lds_dwordx4 v242, s[40:41]\n\t"
        "s_add_u32 m0, m0, 0x1000\n\t"
        "v_mfma_f32_16x16x32_bf16 v[28:31], a[60:63], a[4:7], v[28:31]\n\t"
        "s_add_u32 s40, s40, 0x10000\n\t"
        "s_addc_u32 s41, s41, 0\n\t"
        "s_waitcnt lgkmcnt(12)\n\t"
        "v_mfma_f32_16x16x32_bf16 v[32:35], a[48:51], a[8:11], v[32:35]\n\t"
        "ds_read_b64_tr_b16 a[4:5], v241 offset:32768\n\t"
        "v_mfma_f32_16x16x32_bf16 v[36:39], a[52:55], a[8:11], v[36:39]\n\t"
        "ds_read_b64_tr_b16 a[6:7], v241 offset:33792\n\t"
        "v_mfma_f32_16x16x32_bf16 v[40:43], a[56:59], a[8:11], v[40:43]\n\t"
        "global_load_lds_dwordx4 v242, s[40:41]\n\t"
        "s_add_u32 m0, m0, 0x1000\n\t"
        "v_mfma_f32_16x16x32_bf16 v[44:47], a[60:63], a[8:11], v[44:47]\n\t"
        "s_add_u32 s40, s40, 0x10000\n\t"
        "s_addc_u32 s41, s41, 0\n\t"
        "s_waitcnt lgkmcnt(12)\n\t"
        "v_mfma_f32_16x16x32_bf16 v[48:51], a[48:51], a[12:15], v[48:51]\n\t"
        "ds_read_b64_tr_b16 a[8:9], v240 offset:33024\n\t"
        "v_mfma_f32_16x16x32_bf16 v[52:55], a[52:55], a[12:15], v[52:55]\n\t"
        "ds_read_b64_tr_b16 a[10:11], v240 offset:34048\n\t"
        "v_mfma_f32_16x16x32_bf16 v[56:59], a[56:59], a[12:15], v[56:59]\n\t"
        "global_load_dwordx4 a[32:35], v243, s[42:43] offset:0\n\t"
        "v_mfma_f32_16x16x32_bf16 v[60:63], a[60:63], a[12:15], v[60:63]\n\t"
        "s_waitcnt lgkmcnt(12)\n\t"
        "v_mfma_f32_16x16x32_bf16 v[64:67], a[48:51], a[16:19], v[64:67]\n\t"
        "ds_read_b64_tr_b16 a[12:13], v241 offset:33024\n\t"
        "v_mfma_f32_16x16x32_bf16 v[68:71], a[52:55], a[16:19], v[68:71]\n\t"
        "ds_read_b64_tr_b16 a[14:15], v241 offset:34048\n\t"
        "v_mfma_f32_16x16x32_bf16 v[72:75], a[56:59], a[16:19], v[72:75]\n\t"
        "global_load_dwordx4 a[36:39], v243, s[42:43] offset:1024\n\t"
        "v_mfma_f32_16x16x32_bf16 v[76:79], a[60:63], a[16:19], v[76:79]\n\t"
        "s_waitcnt lgkmcnt(12)\n\t"
        "v_mfma_f32_16x16x32_bf16 v[80:83], a[48:51], a[20:23], v[80:83]\n\t"
        "ds_read_b64_tr_b16 a[16:17], v240 offset:33280\n\t"
        "v_mfma_f32_16x16x32_bf16 v[84:87], a[52:55], a[20:23], v[84:87]\n\t"
        "ds_read_b64_tr_b16 a[18:19], v240 offset:34304\n\t"
        "v_mfma_f32_16x16x32_bf16 v[88:91], a[56:59], a[20:23], v[88:91]\n\t"
        "global_load_dwordx4 a[40:43], v243, s[42:43] offset:2048\n\t"
        "v_mfma_f32_16x16x32_bf16 v[92:95], a[60:63], a[20:23], v[92:95]\n\t"
        "s_waitcnt lgkmcnt(12)\n\t"
        "v_mfma_f32_16x16x32_bf16 v[96:99], a[48:51], a[24:27], v[96:99]\n\t"
        "ds_read_b64_tr_b16 a[20:21], v241 offset:33280\n\t"
        "v_mfma_f32_16x16x32_bf16 v[100:103], a[52:55], a[24:27], v[100:103]\n\t"
        "ds_read_b64_tr_b16 a[22:23], v241 offset:34304\n\t"
        "v_mfma_f32_16x16x32_bf16 v[104:107], a[56:59], a[24:27], v[104:107]\n\t"
        "global_load_dwordx4 a[44:47], v243, s[42:43] offset:3072\n\t"
        "v_mfma_f32_16x16x32_bf16 v[108:111], a[60:63], a[24:27], v[108:111]\n\t"
        "s_add_u32 s42, s42, 0x2000\n\t"
        "s_addc_u32 s43, s43, 0\n\t"
        "s_waitcnt lgkmcnt(12)\n\t"
        "v_mfma_f32_16x16x32_bf16 v[112:115], a[48:51], a[28:31], v[112:115]\n\t"
        "ds_read_b64_tr_b16 a[24:25], v240 offset:33536\n\t"
        "v_mfma_f32_16x16x32_bf16 v[116:119], a[52:55], a[28:31], v[116:119]\n\t"
        "ds_read_b64_tr_b16 a[26:27], v240 offset:34560\n\t"
        "v_mfma_f32_16x16x32_bf16 v[120:123], a[56:59], a[28:31], v[120:123]\n\t"
        "global_load_dwordx4 a[176:179], v243, s[68:69] offset:-1024\n\t"
        "v_mfma_f32_16x16x32_bf16 v[124:127], a[60:63], a[28:31], v[124:127]\n\t"
        "s_waitcnt vmcnt(19)\n\t"
        "s_barrier\n\t"
        "s_waitcnt lgkmcnt(12)\n\t"
        "v_mfma_f32_16x16x32_bf16 v[0:3], a[64:67], a[0:3], v[0:3]\n\t"
        "ds_read_b64_tr_b16 a[28:29], v241 offset:33536\n\t"
        "v_mfma_f32_16x16x32_bf16 v[4:7], a[68:71], a[0:3], v[4:7]\n\t"
        "ds_read_b64_tr_b16 a[30:31], v241 offset:34560\n\t"
        "v_mfma_f32_16x16x32_bf16 v[8:11], a[72:75], a[0:3], v[8:11]\n\t"
        "v_mfma_f32_16x16x32_bf16 v[12:15], a[76:79], a[0:3], v[12:15]\n\t"
        "s_waitcnt lgkmcnt(12)\n\t"
        "v_mfma_f32_16x16x32_bf16 v[16:19], a[64:67], a[4:7], v[16:19]\n\t"
        "ds_read_b64_tr_b16 a[0:1], v240 offset:40960\n\t"
        "v_mfma_f32_16x16x32_bf16 v[20:23], a[68:71], a[4:7], v[20:23]\n\t"
        "ds_read_b64_tr_b16 a[2:3], v240 offset:41984\n\t"
        "v_mfma_f32_16x16x32_bf16 v[24:27], a[72:75], a[4:7], v[24:27]\n\t"
        "global_load_lds_dwordx4 v242, s[40:41]\n\t"
        "s_add_u32 m0, m0, 0x1000\n\t"
        "v_mfma_f32_16x16x32_bf16 v[28:31], a[76:79], a[4:7], v[28:31]\n\t"
        "s_add_u32 s40, s40, 0x10000\n\t"
        "s_addc_u32 s41, s41, 0\n\t"
        "s_waitcnt lgkmcnt(12)\n\t"
        "v_mfma_f32_16x16x32_bf16 v[32:35], a[64:67], a[8:11], v[32:35]\n\t"
        "ds_read_b64_tr_b16 a[4:5], v241 offset:40960\n\t"
        "v_mfma_f32_16x16x32_bf16 v[36:39], a[68:71], a[8:11], v[36:39]\n\t"
        "ds_read_b64_tr_b16 a[6:7], v241 offset:41984\n\t"
        "v_mfma_f32_16x16x32_bf16 v[40:43], a[72:75], a[8:11], v[40:43]\n\t"
        "global_load_lds_dwordx4 v242, s[40:41]\n\t"
        "s_add_u32 m0, m0, 0x1000\n\t"
        "v_mfma_f32_16x16x32_bf16 v[44:47], a[76:79], a[8:11], v[44:47]\n\t"
        "s_add_u32 s40, s40, 0x10000\n\t"
        "s_addc_u32 s41, s41, 0\n\t"
        "s_waitcnt lgkmcnt(12)\n\t"
        "v_mfma_f32_16x16x32_bf16 v[48:51], a[64:67], a[12:15], v[48:51]\n\t"
        "ds_read_b64_tr_b16 a[8:9], v240 offset:41216\n\t"
        "v_mfma_f32_16x16x32_bf16 v[52:55], a[68:71], a[12:15], v[52:55]\n\t"
        "ds_read_b64_tr_b16 a[10:11], v240 offset:42240\n\t"
        "v_mfma_f32_16x16x32_bf16 v[56:59], a[72:75], a[12:15], v[56:59]\n\t"
        "global_load_dwordx4 a[48:51], v243, s[42:43] offset:-4096\n\t"
        "v_mfma_f32_16x16x32_bf16 v[60:63], a[76:79], a[12:15], v[60:63]\n\t"
        "s_waitcnt lgkmcnt(12)\n\t"
        "v_mfma_f32_16x16x32_bf16 v[64:67], a[64:67], a[16:19], v[64:67]\n\t"
        "ds_read_b64_tr_b16 a[12:13], v241 offset:41216\n\t"
        "v_mfma_f32_16x16x32_bf16 v[68:71], a[68:71], a[16:19], v[68:71]\n\t"
        "ds_read_b64_tr_b16 a[14:15], v241 offset:42240\n\t"
        "v_mfma_f32_16x16x32_bf16 v[72:75], a[72:75], a[16:19], v[72:75]\n\t"
        "global_load_dwordx4 a[52:55], v243, s[42:43] offset:-3072\n\t"
        "v_mfma_f32_16x16x32_bf16 v[76:79], a[76:79], a[16:19], v[76:79]\n\t"
        "s_waitcnt lgkmcnt(12)\n\t"
        "v_mfma_f32_16x16x32_bf16 v[80:83], a[64:67], a[20:23], v[80:83]\n\t"
        "ds_read_b64_tr_b16 a[16:17], v240 offset:41472\n\t"
        "v_mfma_f32_16x16x32_bf16 v[84:87], a[68:71], a[20:23], v[84:87]\n\t"
        "ds_read_b64_tr_b16 a[18:19], v240 offset:42496\n\t"
        "v_mfma_f32_16x16x32_bf16 v[88:91], a[72:75], a[20:23], v[88:91]\n\t"
        "global_load_dwordx4 a[56:59], v243, s[42:43] offset:-2048\n\t"
        "v_mfma_f32_16x16x32_bf16 v[92:95], a[76:79], a[20:23], v[92:95]\n\t"
        "s_waitcnt lgkmcnt(12)\n\t"
        "v_mfma_f32_16x16x32_bf16 v[96:99], a[64:67], a[24:27], v[96:99]\n\t"
        "ds_read_b64_tr_b16 a[20:21], v241 offset:41472\n\t"
        "v_mfma_f32_16x16x32_bf16 v[100:103], a[68:71], a[24:27], v[100:103]\n\t"
        "ds_read_b64_tr_b16 a[22:23], v241 offset:42496\n\t"
        "v_mfma_f32_16x16x32_bf16 v[104:107], a[72:75], a[24:27], v[104:107]\n\t"
        "global_load_dwordx4 a[60:63], v243, s[42:43] offset:-1024\n\t"
        "v_mfma_f32_16x16x32_bf16 v[108:111], a[76:79], a[24:27], v[108:111]\n\t"
        "s_waitcnt lgkmcnt(12)\n\t"
        "v_mfma_f32_16x16x32_bf16 v[112:115], a[64:67], a[28:31], v[112:115]\n\t"
        "ds_read_b64_tr_b16 a[24:25], v240 offset:41728\n\t"
        "v_mfma_f32_16x16x32_bf16 v[116:119], a[68:71], a[28:31], v[116:119]\n\t"
        "ds_read_b64_tr_b16 a[26:27], v240 offset:42752\n\t"
        "v_mfma_f32_16x16x32_bf16 v[120:123], a[72:75], a[28:31], v[120:123]\n\t"
        "global_load_dwordx4 a[180:183], v243, s[68:69] offset:0\n\t"
        "v_mfma_f32_16x16x32_bf16 v[124:127], a[76:79], a[28:31], v[124:127]\n\t"
        "s_waitcnt vmcnt(19)\n\t"
        "s_barrier\n\t"
        "s_waitcnt lgkmcnt(12)\n\t"
        "v_mfma_f32_16x16x32_bf16 v[0:3], a[80:83], a[0:3], v[0:3]\n\t"
        "ds_read_b64_tr_b16 a[28:29], v241 offset:41728\n\t"
        "v_mfma_f32_16x16x32_bf16 v[4:7], a[84:87], a[0:3], v[4:7]\n\t"
        "ds_read_b64_tr_b16 a[30:31], v241 offset:42752\n\t"
        "v_mfma_f32_16x16x32_bf16 v[8:11], a[88:91], a[0:3], v[8:11]\n\t"
        "v_mfma_f32_16x16x32_bf16 v[12:15], a[92:95], a[0:3], v[12:15]\n\t"
        "s_waitcnt lgkmcnt(12)\n\t"
        "v_mfma_f32_16x16x32_bf16 v[16:19], a[80:83], a[4:7], v[16:19]\n\t"
        "ds_read_b64_tr_b16 a[0:1], v240 offset:49152\n\t"
        "v_mfma_f32_16x16x32_bf16 v[20:23], a[84:87], a[4:7], v[20:23]\n\t"
        "ds_read_b64_tr_b16 a[2:3], v240 offset:50176\n\t"
        "v_mfma_f32_16x16x32_bf16 v[24:27], a[88:91], a[4:7], v[24:27]\n\t"
        "global_load_lds_dwordx4 v242, s[40:41]\n\t"
        "s_add_u32 m0, m0, 0x1000\n\t"
        "v_mfma_f32_16x16x32_bf16 v[28:31], a[92:95], a[4:7], v[28:31]\n\t"
        "s_add_u32 s40, s40, 0x10000\n\t"
        "s_addc_u32 s41, s41, 0\n\t"
        "s_waitcnt lgkmcnt(12)\n\t"
        "v_mfma_f32_16x16x32_bf16 v[32:35], a[80:83], a[8:11], v[32:35]\n\t"
        "ds_read_b64_tr_b16 a[4:5], v241 offset:49152\n\t"
        "v_mfma_f32_16x16x32_bf16 v[36:39], a[84:87], a[8:11], v[36:39]\n\t"
        "ds_read_b64_tr_b16 a[6:7], v241 offset:50176\n\t"
        "v_mfma_f32_16x16x32_bf16 v[40:43], a[88:91], a[8:11], v[40:43]\n\t"
        "global_load_lds_dwordx4 v242, s[40:41]\n\t"
        "s_add_u32 m0, m0, 0x1000\n\t"
        "v_mfma_f32_16x16x32_bf16 v[44:47], a[92:95], a[8:11], v[44:47]\n\t"
        "s_add_u32 s40, s40, 0x10000\n\t"
        "s_addc_u32 s41, s41, 0\n\t"
        "s_waitcnt lgkmcnt(12)\n\t"
        "v_mfma_f32_16x16x32_bf16 v[48:51], a[80:83], a[12:15], v[48:51]\n\t"
        "ds_read_b64_tr_b16 a[8:9], v240 offset:49408\n\t"
        "v_mfma_f32_16x16x32_bf16 v[52:55], a[84:87], a[12:15], v[52:55]\n\t"
        "ds_read_b64_tr_b16 a[10:11], v240 offset:50432\n\t"
        "v_mfma_f32_16x16x32_bf16 v[56:59], a[88:91], a[12:15], v[56:59]\n\t"
        "global_load_dwordx4 a[64:67], v243, s[42:43] offset:0\n\t"
        "v_mfma_f32_16x16x32_bf16 v[60:63], a[92:95], a[12:15], v[60:63]\n\t"
        "s_waitcnt lgkmcnt(12)\n\t"
        "v_mfma_f32_16x16x32_bf16 v[64:67], a[80:83], a[16:19], v[64:67]\n\t"
        "ds_read_b64_tr_b16 a[12:13], v241 offset:49408\n\t"
        "v_mfma_f32_16x16x32_bf16 v[68:71], a[84:87], a[16:19], v[68:71]\n\t"
        "ds_read_b64_tr_b16 a[14:15], v241 offset:50432\n\t"
        "v_mfma_f32_16x16x32_bf16 v[72:75], a[88:91], a[16:19], v[72:75]\n\t"
        "global_load_dwordx4 a[68:71], v243, s[42:43] offset:1024\n\t"
        "v_mfma_f32_16x16x32_bf16 v[76:79], a[92:95], a[16:19], v[76:79]\n\t"
        "s_waitcnt lgkmcnt(12)\n\t"
        "v_mfma_f32_16x16x32_bf16 v[80:83], a[80:83], a[20:23], v[80:83]\n\t"
        "ds_read_b64_tr_b16 a[16:17], v240 offset:49664\n\t"
        "v_mfma_f32_16x16x32_bf16 v[84:87], a[84:87], a[20:23], v[84:87]\n\t"
        "ds_read_b64_tr_b16 a[18:19], v240 offset:50688\n\t"
        "v_mfma_f32_16x16x32_bf16 v[88:91], a[88:91], a[20:23], v[88:91]\n\t"
        "global_load_dwordx4 a[72:75], v243, s[42:43] offset:2048\n\t"
        "v_mfma_f32_16x16x32_bf16 v[92:95], a[92:95], a[20:23], v[92:95]\n\t"
        "s_waitcnt lgkmcnt(12)\n\t"
        "v_mfma_f32_16x16x32_bf16 v[96:99], a[80:83], a[24:27], v[96:99]\n\t"
        "ds_read_b64_tr_b16 a[20:21], v241 offset:49664\n\t"
        "v_mfma_f32_16x16x32_bf16 v[100:103], a[84:87], a[24:27], v[100:103]\n\t"
        "ds_read_b64_tr_b16 a[22:23], v241 offset:50688\n\t"
        "v_mfma_f32_16x16x32_bf16 v[104:107], a[88:91], a[24:27], v[104:107]\n\t"
        "global_load_dwordx4 a[76:79], v243, s[42:43] offset:3072\n\t"
        "v_mfma_f32_16x16x32_bf16 v[108:111], a[92:95], a[24:27], v[108:111]\n\t"
        "s_add_u32 s42, s42, 0x2000\n\t"
        "s_addc_u32 s43, s43, 0\n\t"
        "s_waitcnt lgkmcnt(12)\n\t"
        "v_mfma_f32_16x16x32_bf16 v[112:115], a[80:83], a[28:31], v[112:115]\n\t"
        "ds_read_b64_tr_b16 a[24:25], v240 offset:49920\n\t"
        "v_mfma_f32_16x16x32_bf16 v[116:119], a[84:87], a[28:31], v[116:119]\n\t"
        "ds_read_b64_tr_b16 a[26:27], v240 offset:50944\n\t"
        "v_mfma_f32_16x16x32_bf16 v[120:123], a[88:91], a[28:31], v[120:123]\n\t"
        "global_load_dwordx4 a[184:187], v243, s[68:69] offset:1024\n\t"
        "v_mfma_f32_16x16x32_bf16 v[124:127], a[92:95], a[28:31], v[124:127]\n\t"
        "s_waitcnt vmcnt(19)\n\t"
        "s_barrier\n\t"
        "s_waitcnt lgkmcnt(12)\n\t"
        "v_mfma_f32_16x16x32_bf16 v[0:3], a[96:99], a[0:3], v[0:3]\n\t"
        "ds_read_b64_tr_b16 a[28:29], v241 offset:49920\n\t"
        "v_mfma_f32_16x16x32_bf16 v[4:7], a[100:103], a[0:3], v[4:7]\n\t"
        "ds_read_b64_tr_b16 a[30:31], v241 offset:50944\n\t"
        "v_mfma_f32_16x16x32_bf16 v[8:11], a[104:107], a[0:3], v[8:11]\n\t"
        "v_mfma_f32_16x16x32_bf16 v[12:15], a[108:111], a[0:3], v[12:15]\n\t"
        "s_waitcnt lgkmcnt(12)\n\t"
        "v_mfma_f32_16x16x32_bf16 v[16:19], a[96:99], a[4:7], v[16:19]\n\t"
        "ds_read_b64_tr_b16 a[0:1], v240 offset:57344\n\t"
        "v_mfma_f32_16x16x32_bf16 v[20:23], a[100:103], a[4:7], v[20:23]\n\t"
        "ds_read_b64_tr_b16 a[2:3], v240 offset:58368\n\t"
        "v_mfma_f32_16x16x32_bf16 v[24:27], a[104:107], a[4:7], v[24:27]\n\t"
        "global_load_lds_dwordx4 v242, s[40:41]\n\t"
        "s_add_u32 m0, m0, 0x1000\n\t"
        "v_mfma_f32_16x16x32_bf16 v[28:31], a[108:111], a[4:7], v[28:31]\n\t"
        "s_add_u32 s40, s40, 0x10000\n\t"
        "s_addc_u32 s41, s41, 0\n\t"
        "s_waitcnt lgkmcnt(12)\n\t"
        "v_mfma_f32_16x16x32_bf16 v[32:35], a[96:99], a[8:11], v[32:35]\n\t"
        "ds_read_b64_tr_b16 a[4:5], v241 offset:57344\n\t"
        "v_mfma_f32_16x16x32_bf16 v[36:39], a[100:103], a[8:11], v[36:39]\n\t"
        "ds_read_b64_tr_b16 a[6:7], v241 offset:58368\n\t"
        "v_mfma_f32_16x16x32_bf16 v[40:43], a[104:107], a[8:11], v[40:43]\n\t"
        "global_load_lds_dwordx4 v242, s[40:41]\n\t"
        "s_add_u32 m0, m0, 0x1000\n\t"
        "v_mfma_f32_16x16x32_bf16 v[44:47], a[108:111], a[8:11], v[44:47]\n\t"
        "s_add_u32 s40, s40, 0x10000\n\t"
        "s_addc_u32 s41, s41, 0\n\t"
        "s_waitcnt lgkmcnt(12)\n\t"
        "v_mfma_f32_16x16x32_bf16 v[48:51], a[96:99], a[12:15], v[48:51]\n\t"
        "ds_read_b64_tr_b16 a[8:9], v240 offset:57600\n\t"
        "v_mfma_f32_16x16x32_bf16 v[52:55], a[100:103], a[12:15], v[52:55]\n\t"
        "ds_read_b64_tr_b16 a[10:11], v240 offset:58624\n\t"
        "v_mfma_f32_16x16x32_bf16 v[56:59], a[104:107], a[12:15], v[56:59]\n\t"
        "global_load_dwordx4 a[80:83], v243, s[42:43] offset:-4096\n\t"
        "v_mfma_f32_16x16x32_bf16 v[60:63], a[108:111], a[12:15], v[60:63]\n\t"
        "s_waitcnt lgkmcnt(12)\n\t"
        "v_mfma_f32_16x16x32_bf16 v[64:67], a[96:99], a[16:19], v[64:67]\n\t"
        "ds_read_b64_tr_b16 a[12:13], v241 offset:57600\n\t"
        "v_mfma_f32_16x16x32_bf16 v[68:71], a[100:103], a[16:19], v[68:71]\n\t"
        "ds_read_b64_tr_b16 a[14:15], v241 offset:58624\n\t"
        "v_mfma_f32_16x16x32_bf16 v[72:75], a[104:107], a[16:19], v[72:75]\n\t"
        "global_load_dwordx4 a[84:87], v243, s[42:43] offset:-3072\n\t"
        "v_mfma_f32_16x16x32_bf16 v[76:79], a[108:111], a[16:19], v[76:79]\n\t"
        "s_waitcnt lgkmcnt(12)\n\t"
        "v_mfma_f32_16x16x32_bf16 v[80:83], a[96:99], a[20:23], v[80:83]\n\t"
        "ds_read_b64_tr_b16 a[16:17], v240 offset:57856\n\t"
        "v_mfma_f32_16x16x32_bf16 v[84:87], a[100:103], a[20:23], v[84:87]\n\t"
        "ds_read_b64_tr_b16 a[18:19], v240 offset:58880\n\t"
        "v_mfma_f32_16x16x32_bf16 v[88:91], a[104:107], a[20:23], v[88:91]\n\t"
        "global_load_dwordx4 a[88:91], v243, s[42:43] offset:-2048\n\t"
        "v_mfma_f32_16x16x32_bf16 v[92:95], a[108:111], a[20:23], v[92:95]\n\t"
        "s_waitcnt lgkmcnt(12)\n\t"
        "v_mfma_f32_16x16x32_bf16 v[96:99], a[96:99], a[24:27], v[96:99]\n\t"
        "ds_read_b64_tr_b16 a[20:21], v241 offset:57856\n\t"
        "v_mfma_f32_16x16x32_bf16 v[100:103], a[100:103], a[24:27], v[100:103]\n\t"
        "ds_read_b64_tr_b16 a[22:23], v241 offset:58880\n\t"
        "v_mfma_f32_16x16x32_bf16 v[104:107], a[104:107], a[24:27], v[104:107]\n\t"
        "global_load_dwordx4 a[92:95], v243, s[42:43] offset:-1024\n\t"
        "v_mfma_f32_16x16x32_bf16 v[108:111], a[108:111], a[24:27], v[108:111]\n\t"
        "s_waitcnt lgkmcnt(12)\n\t"
        "v_mfma_f32_16x16x32_bf16 v[112:115], a[96:99], a[28:31], v[112:115]\n\t"
        "ds_read_b64_tr_b16 a[24:25], v240 offset:58112\n\t"
        "v_mfma_f32_16x16x32_bf16 v[116:119], a[100:103], a[28:31], v[116:119]\n\t"
        "ds_read_b64_tr_b16 a[26:27], v240 offset:59136\n\t"
        "v_mfma_f32_16x16x32_bf16 v[120:123], a[104:107], a[28:31], v[120:123]\n\t"
        "global_load_dwordx4 a[188:191], v243, s[68:69] offset:2048\n\t"
        "s_add_u32 s68, s68, 0x5000\n\t"
        "s_addc_u32 s69, s69, 0\n\t"
        "v_mfma_f32_16x16x32_bf16 v[124:127], a[108:111], a[28:31], v[124:127]\n\t"
        "s_waitcnt vmcnt(19)\n\t"
        "s_barrier\n\t"
        "s_waitcnt lgkmcnt(12)\n\t"
        "v_mfma_f32_16x16x32_bf16 v[0:3], a[32:35], a[0:3], v[0:3]\n\t"
        "ds_read_b64_tr_b16 a[28:29], v241 offset:58112\n\t"
        "v_mfma_f32_16x16x32_bf16 v[4:7], a[36:39], a[0:3], v[4:7]\n\t"
        "ds_read_b64_tr_b16 a[30:31], v241 offset:59136\n\t"
        "v_mfma_f32_16x16x32_bf16 v[8:11], a[40:43], a[0:3], v[8:11]\n\t"
        "v_add_u32 v240, 0x10000, v240\n\t"
        "v_add_u32 v241, 0x10000, v241\n\t"
        "v_mfma_f32_16x16x32_bf16 v[12:15], a[44:47], a[0:3], v[12:15]\n\t"
        "s_waitcnt lgkmcnt(12)\n\t"
        "v_mfma_f32_16x16x32_bf16 v[16:19], a[32:35], a[4:7], v[16:19]\n\t"
        "ds_read_b64_tr_b16 a[0:1], v240 offset:0\n\t"
        "v_mfma_f32_16x16x32_bf16 v[20:23], a[36:39], a[4:7], v[20:23]\n\t"
        "ds_read_b64_tr_b16 a[2:3], v240 offset:1024\n\t"
        "v_mfma_f32_16x16x32_bf16 v[24:27], a[40:43], a[4:7], v[24:27]\n\t"
        "global_load_lds_dwordx4 v242, s[40:41]\n\t"
        "s_add_u32 m0, m0, 0x1000\n\t"
        "v_mfma_f32_16x16x32_bf16 v[28:31], a[44:47], a[4:7], v[28:31]\n\t"
        "s_add_u32 s40, s40, 0x10000\n\t"
        "s_addc_u32 s41, s41, 0\n\t"
        "s_waitcnt lgkmcnt(12)\n\t"
        "v_mfma_f32_16x16x32_bf16 v[32:35], a[32:35], a[8:11], v[32:35]\n\t"
        "ds_read_b64_tr_b16 a[4:5], v241 offset:0\n\t"
        "v_mfma_f32_16x16x32_bf16 v[36:39], a[36:39], a[8:11], v[36:39]\n\t"
        "ds_read_b64_tr_b16 a[6:7], v241 offset:1024\n\t"
        "v_mfma_f32_16x16x32_bf16 v[40:43], a[40:43], a[8:11], v[40:43]\n\t"
        "global_load_lds_dwordx4 v242, s[40:41]\n\t"
        "s_add_u32 m0, m0, 0x1000\n\t"
        "v_mfma_f32_16x16x32_bf16 v[44:47], a[44:47], a[8:11], v[44:47]\n\t"
        "s_add_u32 s40, s40, 0x10000\n\t"
        "s_addc_u32 s41, s41, 0\n\t"
        "s_waitcnt lgkmcnt(12)\n\t"
        "v_mfma_f32_16x16x32_bf16 v[48:51], a[32:35], a[12:15], v[48:51]\n\t"
        "ds_read_b64_tr_b16 a[8:9], v240 offset:256\n\t"
        "v_mfma_f32_16x16x32_bf16 v[52:55], a[36:39], a[12:15], v[52:55]\n\t"
        "ds_read_b64_tr_b16 a[10:11], v240 offset:1280\n\t"
        "v_mfma_f32_16x16x32_bf16 v[56:59], a[40:43], a[12:15], v[56:59]\n\t"
        "global_load_dwordx4 a[96:99], v243, s[42:43] offset:0\n\t"
        "v_mfma_f32_16x16x32_bf16 v[60:63], a[44:47], a[12:15], v[60:63]\n\t"
        "s_waitcnt lgkmcnt(12)\n\t"
        "v_mfma_f32_16x16x32_bf16 v[64:67], a[32:35], a[16:19], v[64:67]\n\t"
        "ds_read_b64_tr_b16 a[12:13], v241 offset:256\n\t"
        "v_mfma_f32_16x16x32_bf16 v[68:71], a[36:39], a[16:19], v[68:71]\n\t"
        "ds_read_b64_tr_b16 a[14:15], v241 offset:1280\n\t"
        "v_mfma_f32_16x16x32_bf16 v[72:75], a[40:43], a[16:19], v[72:75]\n\t"
        "global_load_dwordx4 a[100:103], v243, s[42:43] offset:1024\n\t"
        "v_mfma_f32_16x16x32_bf16 v[76:79], a[44:47], a[16:19], v[76:79]\n\t"
        "s_waitcnt lgkmcnt(12)\n\t"
        "v_mfma_f32_16x16x32_bf16 v[80:83], a[32:35], a[20:23], v[80:83]\n\t"
        "ds_read_b64_tr_b16 a[16:17], v240 offset:512\n\t"
        "v_mfma_f32_16x16x32_bf16 v[84:87], a[36:39], a[20:23], v[84:87]\n\t"
        "ds_read_b64_tr_b16 a[18:19], v240 offset:1536\n\t"
        "v_mfma_f32_16x16x32_bf16 v[88:91], a[40:43], a[20:23], v[88:91]\n\t"
        "global_load_dwordx4 a[104:107], v243, s[42:43] offset:2048\n\t"
        "v_mfma_f32_16x16x32_bf16 v[92:95], a[44:47], a[20:23], v[92:95]\n\t"
        "s_waitcnt lgkmcnt(12)\n\t"
        "v_mfma_f32_16x16x32_bf16 v[96:99], a[32:35], a[24:27], v[96:99]\n\t"
        "ds_read_b64_tr_b16 a[20:21], v241 offset:512\n\t"
        "v_mfma_f32_16x16x32_bf16 v[100:103], a[36:39], a[24:27], v[100:103]\n\t"
        "ds_read_b64_tr_b16 a[22:23], v241 offset:1536\n\t"
        "v_mfma_f32_16x16x32_bf16 v[104:107], a[40:43], a[24:27], v[104:107]\n\t"
        "global_load_dwordx4 a[108:111], v243, s[42:43] offset:3072\n\t"
        "v_mfma_f32_16x16x32_bf16 v[108:111], a[44:47], a[24:27], v[108:111]\n\t"
        "s_add_u32 s42, s42, 0x2000\n\t"
        "s_addc_u32 s43, s43, 0\n\t"
        "s_waitcnt lgkmcnt(12)\n\t"
        "v_mfma_f32_16x16x32_bf16 v[112:115], a[32:35], a[28:31], v[112:115]\n\t"
        "ds_read_b64_tr_b16 a[24:25], v240 offset:768\n\t"
        "v_mfma_f32_16x16x32_bf16 v[116:119], a[36:39], a[28:31], v[116:119]\n\t"
        "ds_read_b64_tr_b16 a[26:27], v240 offset:1792\n\t"
        "v_mfma_f32_16x16x32_bf16 v[120:123], a[40:43], a[28:31], v[120:123]\n\t"
        "global_load_dwordx4 a[192:195], v243, s[68:69] offset:-2048\n\t"
        "v_mfma_f32_16x16x32_bf16 v[124:127], a[44:47], a[28:31], v[124:127]\n\t"
        "s_waitcnt vmcnt(19)\n\t"
        "s_barrier\n\t"
        "s_waitcnt lgkmcnt(12)\n\t"
        "v_mfma_f32_16x16x32_bf16 v[0:3], a[48:51], a[0:3], v[0:3]\n\t"
        "ds_read_b64_tr_b16 a[28:29], v241 offset:768\n\t"
        "v_mfma_f32_16x16x32_bf16 v[4:7], a[52:55], a[0:3], v[4:7]\n\t"
        "ds_read_b64_tr_b16 a[30:31], v241 offset:1792\n\t"
        "v_mfma_f32_16x16x32_bf16 v[8:11], a[56:59], a[0:3], v[8:11]\n\t"
        "v_mfma_f32_16x16x32_bf16 v[12:15], a[60:63], a[0:3], v[12:15]\n\t"
        "s_waitcnt lgkmcnt(12)\n\t"
        "v_mfma_f32_16x16x32_bf16 v[16:19], a[48:51], a[4:7], v[16:19]\n\t"
        "ds_read_b64_tr_b16 a[0:1], v240 offset:8192\n\t"
        "v_mfma_f32_16x16x32_bf16 v[20:23], a[52:55], a[4:7], v[20:23]\n\t"
        "ds_read_b64_tr_b16 a[2:3], v240 offset:9216\n\t"
        "v_mfma_f32_16x16x32_bf16 v[24:27], a[56:59], a[4:7], v[24:27]\n\t"
        "global_load_dwordx4 v[128:131], v244, s[44:45] offset:0 sc1\n\t"
        "v_mfma_f32_16x16x32_bf16 v[28:31], a[60:63], a[4:7], v[28:31]\n\t"
        "global_load_dwordx4 v[132:135], v244, s[44:45] offset:64 sc1\n\t"
        "s_waitcnt lgkmcnt(12)\n\t"
        "v_mfma_f32_16x16x32_bf16 v[32:35], a[48:51], a[8:11], v[32:35]\n\t"
        "ds_read_b64_tr_b16 a[4:5], v241 offset:8192\n\t"
        "v_mfma_f32_16x16x32_bf16 v[36:39], a[52:55], a[8:11], v[36:39]\n\t"
        "ds_read_b64_tr_b16 a[6:7], v241 offset:9216\n\t"
        "v_mfma_f32_16x16x32_bf16 v[40:43], a[56:59], a[8:11], v[40:43]\n\t"
        "global_load_dwordx4 v[136:139], v244, s[44:45] offset:128 sc1\n\t"
        "v_mfma_f32_16x16x32_bf16 v[44:47], a[60:63], a[8:11], v[44:47]\n\t"
        "global_load_dwordx4 v[140:143], v244, s[44:45] offset:192 sc1\n\t"
        "s_waitcnt lgkmcnt(12)\n\t"
        "v_mfma_f32_16x16x32_bf16 v[48:51], a[48:51], a[12:15], v[48:51]\n\t"
        "ds_read_b64_tr_b16 a[8:9], v240 offset:8448\n\t"
        "v_mfma_f32_16x16x32_bf16 v[52:55], a[52:55], a[12:15], v[52:55]\n\t"
        "ds_read_b64_tr_b16 a[10:11], v240 offset:9472\n\t"
        "v_mfma_f32_16x16x32_bf16 v[56:59], a[56:59], a[12:15], v[56:59]\n\t"
        "global_load_dwordx4 a[196:199], v243, s[68:69] offset:-1024\n\t"
        "v_mfma_f32_16x16x32_bf16 v[60:63], a[60:63], a[12:15], v[60:63]\n\t"
        "s_waitcnt lgkmcnt(12)\n\t"
        "v_mfma_f32_16x16x32_bf16 v[64:67], a[48:51], a[16:19], v[64:67]\n\t"
        "ds_read_b64_tr_b16 a[12:13], v241 offset:8448\n\t"
        "v_mfma_f32_16x16x32_bf16 v[68:71], a[52:55], a[16:19], v[68:71]\n\t"
        "ds_read_b64_tr_b16 a[14:15], v241 offset:9472\n\t"
        "v_mfma_f32_16x16x32_bf16 v[72:75], a[56:59], a[16:19], v[72:75]\n\t"
        "v_mfma_f32_16x16x32_bf16 v[76:79], a[60:63], a[16:19], v[76:79]\n\t"
        "s_waitcnt lgkmcnt(12)\n\t"
        "v_mfma_f32_16x16x32_bf16 v[80:83], a[48:51], a[20:23], v[80:83]\n\t"
        "ds_read_b64_tr_b16 a[16:17], v240 offset:8704\n\t"
        "v_mfma_f32_16x16x32_bf16 v[84:87], a[52:55], a[20:23], v[84:87]\n\t"
        "ds_read_b64_tr_b16 a[18:19], v240 offset:9728\n\t"
        "v_mfma_f32_16x16x32_bf16 v[88:91], a[56:59], a[20:23], v[88:91]\n\t"
        "v_mfma_f32_16x16x32_bf16 v[92:95], a[60:63], a[20:23], v[92:95]\n\t"
        "s_waitcnt lgkmcnt(12)\n\t"
        "v_mfma_f32_16x16x32_bf16 v[96:99], a[48:51], a[24:27], v[96:99]\n\t"
        "ds_read_b64_tr_b16 a[20:21], v241 offset:8704\n\t"
        "v_mfma_f32_16x16x32_bf16 v[100:103], a[52:55], a[24:27], v[100:103]\n\t"
        "ds_read_b64_tr_b16 a[22:23], v241 offset:9728\n\t"
        "v_mfma_f32_16x16x32_bf16 v[104:107], a[56:59], a[24:27], v[104:107]\n\t"
        "v_mfma_f32_16x16x32_bf16 v[108:111], a[60:63], a[24:27], v[108:111]\n\t"
        "s_waitcnt lgkmcnt(12)\n\t"
        "v_mfma_f32_16x16x32_bf16 v[112:115], a[48:51], a[28:31], v[112:115]\n\t"
        "ds_read_b64_tr_b16 a[24:25], v240 offset:8960\n\t"
        "v_mfma_f32_16x16x32_bf16 v[116:119], a[52:55], a[28:31], v[116:119]\n\t"
        "ds_read_b64_tr_b16 a[26:27], v240 offset:9984\n\t"
        "v_mfma_f32_16x16x32_bf16 v[120:123], a[56:59], a[28:31], v[120:123]\n\t"
        "v_mfma_f32_16x16x32_bf16 v[124:127], a[60:63], a[28:31], v[124:127]\n\t"
        "s_waitcnt vmcnt(17)\n\t"
        "s_barrier\n\t"
        "s_waitcnt lgkmcnt(12)\n\t"
        "v_mfma_f32_16x16x32_bf16 v[0:3], a[64:67], a[0:3], v[0:3]\n\t"
        "ds_read_b64_tr_b16 a[28:29], v241 offset:8960\n\t"
        "v_mfma_f32_16x16x32_bf16 v[4:7], a[68:71], a[0:3], v[4:7]\n\t"
        "ds_read_b64_tr_b16 a[30:31], v241 offset:9984\n\t"
        "v_mfma_f32_16x16x32_bf16 v[8:11], a[72:75], a[0:3], v[8:11]\n\t"
        "v_mfma_f32_16x16x32_bf16 v[12:15], a[76:79], a[0:3], v[12:15]\n\t"
        "s_waitcnt lgkmcnt(12)\n\t"
        "v_mfma_f32_16x16x32_bf16 v[16:19], a[64:67], a[4:7], v[16:19]\n\t"
        "ds_read_b64_tr_b16 a[0:1], v240 offset:16384\n\t"
        "v_mfma_f32_16x16x32_bf16 v[20:23], a[68:71], a[4:7], v[20:23]\n\t"
        "ds_read_b64_tr_b16 a[2:3], v240 offset:17408\n\t"
        "v_mfma_f32_16x16x32_bf16 v[24:27], a[72:75], a[4:7], v[24:27]\n\t"
        "global_load_dwordx4 v[144:147], v244, s[44:45] offset:256 sc1\n\t"
        "v_mfma_f32_16x16x32_bf16 v[28:31], a[76:79], a[4:7], v[28:31]\n\t"
        "global_load_dwordx4 v[148:151], v244, s[44:45] offset:320 sc1\n\t"
        "s_waitcnt lgkmcnt(12)\n\t"
        "v_mfma_f32_16x16x32_bf16 v[32:35], a[64:67], a[8:11], v[32:35]\n\t"
        "ds_read_b64_tr_b16 a[4:5], v241 offset:16384\n\t"
        "v_mfma_f32_16x16x32_bf16 v[36:39], a[68:71], a[8:11], v[36:39]\n\t"
        "ds_read_b64_tr_b16 a[6:7], v241 offset:17408\n\t"
        "v_mfma_f32_16x16x32_bf16 v[40:43], a[72:75], a[8:11], v[40:43]\n\t"
        "global_load_dwordx4 v[152:155], v244, s[44:45] offset:384 sc1\n\t"
        "v_mfma_f32_16x16x32_bf16 v[44:47], a[76:79], a[8:11], v[44:47]\n\t"
        "global_load_dwordx4 v[156:159], v244, s[44:45] offset:448 sc1\n\t"
        "s_waitcnt lgkmcnt(12)\n\t"
        "v_mfma_f32_16x16x32_bf16 v[48:51], a[64:67], a[12:15], v[48:51]\n\t"
        "ds_read_b64_tr_b16 a[8:9], v240 offset:16640\n\t"
        "v_mfma_f32_16x16x32_bf16 v[52:55], a[68:71], a[12:15], v[52:55]\n\t"
        "ds_read_b64_tr_b16 a[10:11], v240 offset:17664\n\t"
        "v_mfma_f32_16x16x32_bf16 v[56:59], a[72:75], a[12:15], v[56:59]\n\t"
        "global_load_dwordx4 a[200:203], v243, s[68:69] offset:0\n\t"
        "v_mfma_f32_16x16x32_bf16 v[60:63], a[76:79], a[12:15], v[60:63]\n\t"
        "s_waitcnt lgkmcnt(12)\n\t"
        "v_mfma_f32_16x16x32_bf16 v[64:67], a[64:67], a[16:19], v[64:67]\n\t"
        "ds_read_b64_tr_b16 a[12:13], v241 offset:16640\n\t"
        "v_mfma_f32_16x16x32_bf16 v[68:71], a[68:71], a[16:19], v[68:71]\n\t"
        "ds_read_b64_tr_b16 a[14:15], v241 offset:17664\n\t"
        "v_mfma_f32_16x16x32_bf16 v[72:75], a[72:75], a[16:19], v[72:75]\n\t"
        "v_mfma_f32_16x16x32_bf16 v[76:79], a[76:79], a[16:19], v[76:79]\n\t"
        "s_waitcnt lgkmcnt(12)\n\t"
        "v_mfma_f32_16x16x32_bf16 v[80:83], a[64:67], a[20:23], v[80:83]\n\t"
        "ds_read_b64_tr_b16 a[16:17], v240 offset:16896\n\t"
        "v_mfma_f32_16x16x32_bf16 v[84:87], a[68:71], a[20:23], v[84:87]\n\t"
        "ds_read_b64_tr_b16 a[18:19], v240 offset:17920\n\t"
        "v_mfma_f32_16x16x32_bf16 v[88:91], a[72:75], a[20:23], v[88:91]\n\t"
        "v_mfma_f32_16x16x32_bf16 v[92:95], a[76:79], a[20:23], v[92:95]\n\t"
        "s_waitcnt lgkmcnt(12)\n\t"
        "v_mfma_f32_16x16x32_bf16 v[96:99], a[64:67], a[24:27], v[96:99]\n\t"
        "ds_read_b64_tr_b16 a[20:21], v241 offset:16896\n\t"
        "v_mfma_f32_16x16x32_bf16 v[100:103], a[68:71], a[24:27], v[100:103]\n\t"
        "ds_read_b64_tr_b16 a[22:23], v241 offset:17920\n\t"
        "v_mfma_f32_16x16x32_bf16 v[104:107], a[72:75], a[24:27], v[104:107]\n\t"
        "v_mfma_f32_16x16x32_bf16 v[108:111], a[76:79], a[24:27], v[108:111]\n\t"
        "s_waitcnt lgkmcnt(12)\n\t"
        "v_mfma_f32_16x16x32_bf16 v[112:115], a[64:67], a[28:31], v[112:115]\n\t"
        "ds_read_b64_tr_b16 a[24:25], v240 offset:17152\n\t"
        "v_mfma_f32_16x16x32_bf16 v[116:119], a[68:71], a[28:31], v[116:119]\n\t"
        "ds_read_b64_tr_b16 a[26:27], v240 offset:18176\n\t"
        "v_mfma_f32_16x16x32_bf16 v[120:123], a[72:75], a[28:31], v[120:123]\n\t"
        "v_mfma_f32_16x16x32_bf16 v[124:127], a[76:79], a[28:31], v[124:127]\n\t"
        "s_waitcnt vmcnt(15)\n\t"
        "s_barrier\n\t"
        "s_waitcnt lgkmcnt(12)\n\t"
        "v_mfma_f32_16x16x32_bf16 v[0:3], a[80:83], a[0:3], v[0:3]\n\t"
        "ds_read_b64_tr_b16 a[28:29], v241 offset:17152\n\t"
        "v_mfma_f32_16x16x32_bf16 v[4:7], a[84:87], a[0:3], v[4:7]\n\t"
        "ds_read_b64_tr_b16 a[30:31], v241 offset:18176\n\t"
        "v_mfma_f32_16x16x32_bf16 v[8:11], a[88:91], a[0:3], v[8:11]\n\t"
        "v_mfma_f32_16x16x32_bf16 v[12:15], a[92:95], a[0:3], v[12:15]\n\t"
        "s_waitcnt lgkmcnt(12)\n\t"
        "v_mfma_f32_16x16x32_bf16 v[16:19], a[80:83], a[4:7], v[16:19]\n\t"
        "ds_read_b64_tr_b16 a[0:1], v240 offset:24576\n\t"
        "v_mfma_f32_16x16x32_bf16 v[20:23], a[84:87], a[4:7], v[20:23]\n\t"
        "ds_read_b64_tr_b16 a[2:3], v240 offset:25600\n\t"
        "v_mfma_f32_16x16x32_bf16 v[24:27], a[88:91], a[4:7], v[24:27]\n\t"
        "global_load_dwordx2 v[192:193], v245, s[46:47] offset:0 sc1\n\t"
        "v_mfma_f32_16x16x32_bf16 v[28:31], a[92:95], a[4:7], v[28:31]\n\t"
        "global_load_dwordx2 v[194:195], v245, s[46:47] offset:128 sc1\n\t"
        "s_waitcnt lgkmcnt(12)\n\t"
        "v_mfma_f32_16x16x32_bf16 v[32:35], a[80:83], a[8:11], v[32:35]\n\t"
        "ds_read_b64_tr_b16 a[4:5], v241 offset:24576\n\t"
        "v_mfma_f32_16x16x32_bf16 v[36:39], a[84:87], a[8:11], v[36:39]\n\t"
        "ds_read_b64_tr_b16 a[6:7], v241 offset:25600\n\t"
        "v_mfma_f32_16x16x32_bf16 v[40:43], a[88:91], a[8:11], v[40:43]\n\t"
        "global_load_dwordx2 v[196:197], v245, s[46:47] offset:256 sc1\n\t"
        "v_mfma_f32_16x16x32_bf16 v[44:47], a[92:95], a[8:11], v[44:47]\n\t"
        "global_load_dwordx2 v[198:199], v245, s[46:47] offset:384 sc1\n\t"
        "s_waitcnt lgkmcnt(12)\n\t"
        "v_mfma_f32_16x16x32_bf16 v[48:51], a[80:83], a[12:15], v[48:51]\n\t"
        "ds_read_b64_tr_b16 a[8:9], v240 offset:24832\n\t"
        "v_mfma_f32_16x16x32_bf16 v[52:55], a[84:87], a[12:15], v[52:55]\n\t"
        "ds_read_b64_tr_b16 a[10:11], v240 offset:25856\n\t"
        "v_mfma_f32_16x16x32_bf16 v[56:59], a[88:91], a[12:15], v[56:59]\n\t"
        "global_load_dwordx4 a[204:207], v243, s[68:69] offset:1024\n\t"
        "v_mfma_f32_16x16x32_bf16 v[60:63], a[92:95], a[12:15], v[60:63]\n\t"
        "s_waitcnt lgkmcnt(12)\n\t"
        "v_mfma_f32_16x16x32_bf16 v[64:67], a[80:83], a[16:19], v[64:67]\n\t"
        "ds_read_b64_tr_b16 a[12:13], v241 offset:24832\n\t"
        "v_mfma_f32_16x16x32_bf16 v[68:71], a[84:87], a[16:19], v[68:71]\n\t"
        "ds_read_b64_tr_b16 a[14:15], v241 offset:25856\n\t"
        "v_mfma_f32_16x16x32_bf16 v[72:75], a[88:91], a[16:19], v[72:75]\n\t"
        "v_mfma_f32_16x16x32_bf16 v[76:79], a[92:95], a[16:19], v[76:79]\n\t"
        "s_waitcnt lgkmcnt(12)\n\t"
        "v_mfma_f32_16x16x32_bf16 v[80:83], a[80:83], a[20:23], v[80:83]\n\t"
        "ds_read_b64_tr_b16 a[16:17], v240 offset:25088\n\t"
        "v_mfma_f32_16x16x32_bf16 v[84:87], a[84:87], a[20:23], v[84:87]\n\t"
        "ds_read_b64_tr_b16 a[18:19], v240 offset:26112\n\t"
        "v_mfma_f32_16x16x32_bf16 v[88:91], a[88:91], a[20:23], v[88:91]\n\t"
        "v_mfma_f32_16x16x32_bf16 v[92:95], a[92:95], a[20:23], v[92:95]\n\t"
        "s_waitcnt lgkmcnt(12)\n\t"
        "v_mfma_f32_16x16x32_bf16 v[96:99], a[80:83], a[24:27], v[96:99]\n\t"
        "ds_read_b64_tr_b16 a[20:21], v241 offset:25088\n\t"
        "v_mfma_f32_16x16x32_bf16 v[100:103], a[84:87], a[24:27], v[100:103]\n\t"
        "ds_read_b64_tr_b16 a[22:23], v241 offset:26112\n\t"
        "v_mfma_f32_16x16x32_bf16 v[104:107], a[88:91], a[24:27], v[104:107]\n\t"
        "v_mfma_f32_16x16x32_bf16 v[108:111], a[92:95], a[24:27], v[108:111]\n\t"
        "s_waitcnt lgkmcnt(12)\n\t"
        "v_mfma_f32_16x16x32_bf16 v[112:115], a[80:83], a[28:31], v[112:115]\n\t"
        "ds_read_b64_tr_b16 a[24:25], v240 offset:25344\n\t"
        "v_mfma_f32_16x16x32_bf16 v[116:119], a[84:87], a[28:31], v[116:119]\n\t"
        "ds_read_b64_tr_b16 a[26:27], v240 offset:26368\n\t"
        "v_mfma_f32_16x16x32_bf16 v[120:123], a[88:91], a[28:31], v[120:123]\n\t"
        "v_mfma_f32_16x16x32_bf16 v[124:127], a[92:95], a[28:31], v[124:127]\n\t"
        "s_waitcnt vmcnt(16)\n\t"
        "s_waitcnt lgkmcnt(12)\n\t"
        "v_mfma_f32_16x16x32_bf16 v[0:3], a[96:99], a[0:3], v[0:3]\n\t"
        "ds_read_b64_tr_b16 a[28:29], v241 offset:25344\n\t"
        "v_mfma_f32_16x16x32_bf16 v[4:7], a[100:103], a[0:3], v[4:7]\n\t"
        "ds_read_b64_tr_b16 a[30:31], v241 offset:26368\n\t"
        "v_mfma_f32_16x16x32_bf16 v[8:11], a[104:107], a[0:3], v[8:11]\n\t"
        "v_mfma_f32_16x16x32_bf16 v[12:15], a[108:111], a[0:3], v[12:15]\n\t"
        "s_waitcnt lgkmcnt(12)\n\t"
        "v_mfma_f32_16x16x32_bf16 v[16:19], a[96:99], a[4:7], v[16:19]\n\t"
        "v_mfma_f32_16x16x32_bf16 v[20:23], a[100:103], a[4:7], v[20:23]\n\t"
        "v_mfma_f32_16x16x32_bf16 v[24:27], a[104:107], a[4:7], v[24:27]\n\t"
        "global_load_dwordx2 v[200:201], v245, s[46:47] offset:512 sc1\n\t"
        "v_mfma_f32_16x16x32_bf16 v[28:31], a[108:111], a[4:7], v[28:31]\n\t"
        "global_load_dwordx2 v[202:203], v245, s[46:47] offset:640 sc1\n\t"
        "s_waitcnt lgkmcnt(10)\n\t"
        "v_mfma_f32_16x16x32_bf16 v[32:35], a[96:99], a[8:11], v[32:35]\n\t"
        "v_mfma_f32_16x16x32_bf16 v[36:39], a[100:103], a[8:11], v[36:39]\n\t"
        "v_mfma_f32_16x16x32_bf16 v[40:43], a[104:107], a[8:11], v[40:43]\n\t"
        "global_load_dwordx2 v[204:205], v245, s[46:47] offset:768 sc1\n\t"
        "v_mfma_f32_16x16x32_bf16 v[44:47], a[108:111], a[8:11], v[44:47]\n\t"
        "global_load_dwordx2 v[206:207], v245, s[46:47] offset:896 sc1\n\t"
        "s_waitcnt lgkmcnt(8)\n\t"
        "v_mfma_f32_16x16x32_bf16 v[48:51], a[96:99], a[12:15], v[48:51]\n\t"
        "v_mfma_f32_16x16x32_bf16 v[52:55], a[100:103], a[12:15], v[52:55]\n\t"
        "v_mfma_f32_16x16x32_bf16 v[56:59], a[104:107], a[12:15], v[56:59]\n\t"
        "global_load_dwordx4 a[208:211], v243, s[68:69] offset:2048\n\t"
        "s_add_u32 s68, s68, 0x5000\n\t"
        "s_addc_u32 s69, s69, 0\n\t"
        "v_mfma_f32_16x16x32_bf16 v[60:63], a[108:111], a[12:15], v[60:63]\n\t"
        "s_waitcnt lgkmcnt(6)\n\t"
        "v_mfma_f32_16x16x32_bf16 v[64:67], a[96:99], a[16:19], v[64:67]\n\t"
        "v_mfma_f32_16x16x32_bf16 v[68:71], a[100:103], a[16:19], v[68:71]\n\t"
        "v_mfma_f32_16x16x32_bf16 v[72:75], a[104:107], a[16:19], v[72:75]\n\t"
        "v_mfma_f32_16x16x32_bf16 v[76:79], a[108:111], a[16:19], v[76:79]\n\t"
        "s_waitcnt lgkmcnt(4)\n\t"
        "v_mfma_f32_16x16x32_bf16 v[80:83], a[96:99], a[20:23], v[80:83]\n\t"
        "v_mfma_f32_16x16x32_bf16 v[84:87], a[100:103], a[20:23], v[84:87]\n\t"
        "v_mfma_f32_16x16x32_bf16 v[88:91], a[104:107], a[20:23], v[88:91]\n\t"
        "v_mfma_f32_16x16x32_bf16 v[92:95], a[108:111], a[20:23], v[92:95]\n\t"
        "s_waitcnt lgkmcnt(2)\n\t"
        "v_mfma_f32_16x16x32_bf16 v[96:99], a[96:99], a[24:27], v[96:99]\n\t"
        "v_mfma_f32_16x16x32_bf16 v[100:103], a[100:103], a[24:27], v[100:103]\n\t"
        "v_mfma_f32_16x16x32_bf16 v[104:107], a[104:107], a[24:27], v[104:107]\n\t"
        "v_mfma_f32_16x16x32_bf16 v[108:111], a[108:111], a[24:27], v[108:111]\n\t"
        "s_waitcnt lgkmcnt(0)\n\t"
        "v_mfma_f32_16x16x32_bf16 v[112:115], a[96:99], a[28:31], v[112:115]\n\t"
        "v_mfma_f32_16x16x32_bf16 v[116:119], a[100:103], a[28:31], v[116:119]\n\t"
        "v_mfma_f32_16x16x32_bf16 v[120:123], a[104:107], a[28:31], v[120:123]\n\t"
        "v_mfma_f32_16x16x32_bf16 v[124:127], a[108:111], a[28:31], v[124:127]\n\t"
        "s_waitcnt vmcnt(1)\n\t"
        "s_mov_b32 s76, 0x20000\n\t"
        "LSC_%=:\n\t"
        "v_min3_u32 v252, v193, v195, v197\n\t"
        "v_min3_u32 v253, v199, v201, v203\n\t"
        "v_min3_u32 v252, v252, v205, v207\n\t"
        "v_min_u32 v252, v252, v253\n\t"
        "v_max3_u32 v253, v128, v129, v130\n\t"
        "v_max3_u32 v253, v253, v131, v132\n\t"
        "v_max3_u32 v253, v253, v133, v134\n\t"
        "v_max3_u32 v253, v253, v135, v136\n\t"
        "v_max3_u32 v253, v253, v137, v138\n\t"
        "v_max3_u32 v253, v253, v139, v140\n\t"
        "v_max3_u32 v253, v253, v141, v142\n\t"
        "v_max3_u32 v253, v253, v143, v144\n\t"
        "v_max3_u32 v253, v253, v145, v146\n\t"
        "v_max3_u32 v253, v253, v147, v148\n\t"
        "v_max3_u32 v253, v253, v149, v150\n\t"
        "v_max3_u32 v253, v253, v151, v152\n\t"
        "v_max3_u32 v253, v253, v153, v154\n\t"
        "v_max3_u32 v253, v253, v155, v156\n\t"
        "v_max3_u32 v253, v253, v157, v158\n\t"
        "v_max_u32 v253, v253, v159\n\t"
        "v_not_b32 v253, v253\n\t"
        "v_min_u32 v252, v252, v253\n\t"
        "v_cmp_eq_u32 vcc, 0, v252\n\t"
        "s_cbranch_vccz LSD_%=\n\t"
        "s_sub_u32 s76, s76, 1\n\t"
        "s_cbranch_scc1 LSD_%=\n\t"
        "s_sleep 1\n\t"
        "global_load_dwordx2 v[192:193], v245, s[46:47] offset:0 sc1\n\t"
        "global_load_dwordx4 v[128:131], v244, s[44:45] offset:0 sc1\n\t"
        "global_load_dwordx2 v[194:195], v245, s[46:47] offset:128 sc1\n\t"
        "global_load_dwordx4 v[132:135], v244, s[44:45] offset:64 sc1\n\t"
        "global_load_dwordx2 v[196:197], v245, s[46:47] offset:256 sc1\n\t"
        "global_load_dwordx4 v[136:139], v244, s[44:45] offset:128 sc1\n\t"
        "global_load_dwordx2 v[198:199], v245, s[46:47] offset:384 sc1\n\t"
        "global_load_dwordx4 v[140:143], v244, s[44:45] offset:192 sc1\n\t"
        "global_load_dwordx2 v[200:201], v245, s[46:47] offset:512 sc1\n\t"
        "global_load_dwordx4 v[144:147], v244, s[44:45] offset:256 sc1\n\t"
        "global_load_dwordx2 v[202:203], v245, s[46:47] offset:640 sc1\n\t"
        "global_load_dwordx4 v[148:151], v244, s[44:45] offset:320 sc1\n\t"
        "global_load_dwordx2 v[204:205], v245, s[46:47] offset:768 sc1\n\t"
        "global_load_dwordx4 v[152:155], v244, s[44:45] offset:384 sc1\n\t"
        "global_load_dwordx2 v[206:207], v245, s[46:47] offset:896 sc1\n\t"
        "global_load_dwordx4 v[156:159], v244, s[44:45] offset:448 sc1\n\t"
        "s_waitcnt vmcnt(0)\n\t"
        "s_branch LSC_%=\n\t"
        "LSD_%=:\n\t"
        "v_pk_fma_f32 v[0:1], v[192:193], v[128:129], v[0:1] op_sel_hi:[0,1,1] neg_lo:[1,0,0] neg_hi:[1,0,0]\n\t"
        "v_pk_fma_f32 v[2:3], v[192:193], v[130:131], v[2:3] op_sel_hi:[0,1,1] neg_lo:[1,0,0] neg_hi:[1,0,0]\n\t"
        "v_pk_fma_f32 v[4:5], v[192:193], v[132:133], v[4:5] op_sel_hi:[0,1,1] neg_lo:[1,0,0] neg_hi:[1,0,0]\n\t"
        "v_pk_fma_f32 v[6:7], v[192:193], v[134:135], v[6:7] op_sel_hi:[0,1,1] neg_lo:[1,0,0] neg_hi:[1,0,0]\n\t"
        "v_pk_fma_f32 v[8:9], v[192:193], v[136:137], v[8:9] op_sel_hi:[0,1,1] neg_lo:[1,0,0] neg_hi:[1,0,0]\n\t"
        "v_pk_fma_f32 v[10:11], v[192:193], v[138:139], v[10:11] op_sel_hi:[0,1,1] neg_lo:[1,0,0] neg_hi:[1,0,0]\n\t"
        "v_pk_fma_f32 v[12:13], v[192:193], v[140:141], v[12:13] op_sel_hi:[0,1,1] neg_lo:[1,0,0] neg_hi:[1,0,0]\n\t"
        "v_pk_fma_f32 v[14:15], v[192:193], v[142:143], v[14:15] op_sel_hi:[0,1,1] neg_lo:[1,0,0] neg_hi:[1,0,0]\n\t"
        "v_pk_fma_f32 v[0:1], v[192:193], v[0:1], v[144:145] op_sel:[1,0,0]\n\t"
        "v_pk_fma_f32 v[2:3], v[192:193], v[2:3], v[146:147] op_sel:[1,0,0]\n\t"
        "global_load_dwordx4 a[212:215], v243, s[68:69] offset:-2048\n\t"
        "v_pk_fma_f32 v[4:5], v[192:193], v[4:5], v[148:149] op_sel:[1,0,0]\n\t"
        "v_pk_fma_f32 v[6:7], v[192:193], v[6:7], v[150:151] op_sel:[1,0,0]\n\t"
        "v_pk_fma_f32 v[8:9], v[192:193], v[8:9], v[152:153] op_sel:[1,0,0]\n\t"
        "v_pk_fma_f32 v[10:11], v[192:193], v[10:11], v[154:155] op_sel:[1,0,0]\n\t"
        "v_pk_fma_f32 v[12:13], v[192:193], v[12:13], v[156:157] op_sel:[1,0,0]\n\t"
        "v_pk_fma_f32 v[14:15], v[192:193], v[14:15], v[158:159] op_sel:[1,0,0]\n\t"
        "v_max_f32 v0, 0, v0\n\t"
        "v_max_f32 v1, 0, v1\n\t"
        "v_max_f32 v2, 0, v2\n\t"
        "v_max_f32 v3, 0, v3\n\t"
        "global_load_dwordx4 a[216:219], v243, s[68:69] offset:-1024\n\t"
        "v_max_f32 v8, 0, v8\n\t"
        "v_max_f32 v9, 0, v9\n\t"
        "v_max_f32 v10, 0, v10\n\t"
        "v_max_f32 v11, 0, v11\n\t"
        "v_pk_mul_f32 v[0:1], v[0:1], v[4:5]\n\t"
        "v_pk_mul_f32 v[2:3], v[2:3], v[6:7]\n\t"
        "v_pk_mul_f32 v[8:9], v[8:9], v[12:13]\n\t"
        "v_pk_mul_f32 v[10:11], v[10:11], v[14:15]\n\t"
        "v_cvt_pk_bf16_f32 v0, v0, v1\n\t"
        "v_cvt_pk_bf16_f32 v1, v2, v3\n\t"
        "v_cvt_pk_bf16_f32 v2, v8, v9\n\t"
        "v_cvt_pk_bf16_f32 v3, v10, v11\n\t"
        "global_store_dwordx4 v243, v[0:3], s[66:67] sc1\n\t"
        "s_add_u32 s66, s66, 55296\n\t"
        "s_addc_u32 s67, s67, 0\n\t"
        "v_pk_fma_f32 v[16:17], v[194:195], v[128:129], v[16:17] op_sel_hi:[0,1,1] neg_lo:[1,0,0] neg_hi:[1,0,0]\n\t"
        "v_pk_fma_f32 v[18:19], v[194:195], v[130:131], v[18:19] op_sel_hi:[0,1,1] neg_lo:[1,0,0] neg_hi:[1,0,0]\n\t"
        "v_pk_fma_f32 v[20:21], v[194:195], v[132:133], v[20:21] op_sel_hi:[0,1,1] neg_lo:[1,0,0] neg_hi:[1,0,0]\n\t"
        "v_pk_fma_f32 v[22:23], v[194:195], v[134:135], v[22:23] op_sel_hi:[0,1,1] neg_lo:[1,0,0] neg_hi:[1,0,0]\n\t"
        "v_pk_fma_f32 v[24:25], v[194:195], v[136:137], v[24:25] op_sel_hi:[0,1,1] neg_lo:[1,0,0] neg_hi:[1,0,0]\n\t"
        "v_pk_fma_f32 v[26:27], v[194:195], v[138:139], v[26:27] op_sel_hi:[0,1,1] neg_lo:[1,0,0] neg_hi:[1,0,0]\n\t"
        "v_pk_fma_f32 v[28:29], v[194:195], v[140:141], v[28:29] op_sel_hi:[0,1,1] neg_lo:[1,0,0] neg_hi:[1,0,0]\n\t"
        "v_pk_fma_f32 v[30:31], v[194:195], v[142:143], v[30:31] op_sel_hi:[0,1,1] neg_lo:[1,0,0] neg_hi:[1,0,0]\n\t"
        "v_pk_fma_f32 v[16:17], v[194:195], v[16:17], v[144:145] op_sel:[1,0,0]\n\t"
        "v_pk_fma_f32 v[18:19], v[194:195], v[18:19], v[146:147] op_sel:[1,0,0]\n\t"
        "global_load_dwordx4 a[220:223], v243, s[68:69] offset:0\n\t"
        "v_pk_fma_f32 v[20:21], v[194:195], v[20:21], v[148:149] op_sel:[1,0,0]\n\t"
        "v_pk_fma_f32 v[22:23], v[194:195], v[22:23], v[150:151] op_sel:[1,0,0]\n\t"
        "v_pk_fma_f32 v[24:25], v[194:195], v[24:25], v[152:153] op_sel:[1,0,0]\n\t"
        "v_pk_fma_f32 v[26:27], v[194:195], v[26:27], v[154:155] op_sel:[1,0,0]\n\t"
        "v_pk_fma_f32 v[28:29], v[194:195], v[28:29], v[156:157] op_sel:[1,0,0]\n\t"
        "v_pk_fma_f32 v[30:31], v[194:195], v[30:31], v[158:159] op_sel:[1,0,0]\n\t"
        "v_max_f32 v16, 0, v16\n\t"
        "v_max_f32 v17, 0, v17\n\t"
        "v_max_f32 v18, 0, v18\n\t"
        "v_max_f32 v19, 0, v19\n\t"
        "global_load_dwordx4 a[224:227], v243, s[68:69] offset:1024\n\t"
        "v_max_f32 v24, 0, v24\n\t"
        "v_max_f32 v25, 0, v25\n\t"
        "v_max_f32 v26, 0, v26\n\t"
        "v_max_f32 v27, 0, v27\n\t"
        "v_pk_mul_f32 v[16:17], v[16:17], v[20:21]\n\t"
        "v_pk_mul_f32 v[18:19], v[18:19], v[22:23]\n\t"
        "v_pk_mul_f32 v[24:25], v[24:25], v[28:29]\n\t"
        "v_pk_mul_f32 v[26:27], v[26:27], v[30:31]\n\t"
        "v_cvt_pk_bf16_f32 v16, v16, v17\n\t"
        "v_cvt_pk_bf16_f32 v17, v18, v19\n\t"
        "v_cvt_pk_bf16_f32 v18, v24, v25\n\t"
        "v_cvt_pk_bf16_f32 v19, v26, v27\n\t"
        "global_store_dwordx4 v243, v[16:19], s[66:67] sc1\n\t"
        "s_add_u32 s66, s66, 55296\n\t"
        "s_addc_u32 s67, s67, 0\n\t"
        "v_pk_fma_f32 v[32:33], v[196:197], v[128:129], v[32:33] op_sel_hi:[0,1,1] neg_lo:[1,0,0] neg_hi:[1,0,0]\n\t"
        "v_pk_fma_f32 v[34:35], v[196:197], v[130:131], v[34:35] op_sel_hi:[0,1,1] neg_lo:[1,0,0] neg_hi:[1,0,0]\n\t"
        "v_pk_fma_f32 v[36:37], v[196:197], v[132:133], v[36:37] op_sel_hi:[0,1,1] neg_lo:[1,0,0] neg_hi:[1,0,0]\n\t"
        "v_pk_fma_f32 v[38:39], v[196:197], v[134:135], v[38:39] op_sel_hi:[0,1,1] neg_lo:[1,0,0] neg_hi:[1,0,0]\n\t"
        "v_pk_fma_f32 v[40:41], v[196:197], v[136:137], v[40:41] op_sel_hi:[0,1,1] neg_lo:[1,0,0] neg_hi:[1,0,0]\n\t"
        "v_pk_fma_f32 v[42:43], v[196:197], v[138:139], v[42:43] op_sel_hi:[0,1,1] neg_lo:[1,0,0] neg_hi:[1,0,0]\n\t"
        "v_pk_fma_f32 v[44:45], v[196:197], v[140:141], v[44:45] op_sel_hi:[0,1,1] neg_lo:[1,0,0] neg_hi:[1,0,0]\n\t"
        "v_pk_fma_f32 v[46:47], v[196:197], v[142:143], v[46:47] op_sel_hi:[0,1,1] neg_lo:[1,0,0] neg_hi:[1,0,0]\n\t"
        "v_pk_fma_f32 v[32:33], v[196:197], v[32:33], v[144:145] op_sel:[1,0,0]\n\t"
        "v_pk_fma_f32 v[34:35], v[196:197], v[34:35], v[146:147] op_sel:[1,0,0]\n\t"
        "global_load_dwordx4 a[228:231], v243, s[68:69] offset:2048\n\t"
        "s_add_u32 s68, s68, 0x5000\n\t"
        "s_addc_u32 s69, s69, 0\n\t"
        "v_pk_fma_f32 v[36:37], v[196:197], v[36:37], v[148:149] op_sel:[1,0,0]\n\t"
        "v_pk_fma_f32 v[38:39], v[196:197], v[38:39], v[150:151] op_sel:[1,0,0]\n\t"
        "v_pk_fma_f32 v[40:41], v[196:197], v[40:41], v[152:153] op_sel:[1,0,0]\n\t"
        "v_pk_fma_f32 v[42:43], v[196:197], v[42:43], v[154:155] op_sel:[1,0,0]\n\t"
        "v_pk_fma_f32 v[44:45], v[196:197], v[44:45], v[156:157] op_sel:[1,0,0]\n\t"
        "v_pk_fma_f32 v[46:47], v[196:197], v[46:47], v[158:159] op_sel:[1,0,0]\n\t"
        "v_max_f32 v32, 0, v32\n\t"
        "v_max_f32 v33, 0, v33\n\t"
        "v_max_f32 v34, 0, v34\n\t"
        "v_max_f32 v35, 0, v35\n\t"
        "global_load_dwordx4 a[232:235], v243, s[68:69] offset:-2048\n\t"
        "v_max_f32 v40, 0, v40\n\t"
        "v_max_f32 v41, 0, v41\n\t"
        "v_max_f32 v42, 0, v42\n\t"
        "v_max_f32 v43, 0, v43\n\t"
        "v_pk_mul_f32 v[32:33], v[32:33], v[36:37]\n\t"
        "v_pk_mul_f32 v[34:35], v[34:35], v[38:39]\n\t"
        "v_pk_mul_f32 v[40:41], v[40:41], v[44:45]\n\t"
        "v_pk_mul_f32 v[42:43], v[42:43], v[46:47]\n\t"
        "v_cvt_pk_bf16_f32 v32, v32, v33\n\t"
        "v_cvt_pk_bf16_f32 v33, v34, v35\n\t"
        "v_cvt_pk_bf16_f32 v34, v40, v41\n\t"
        "v_cvt_pk_bf16_f32 v35, v42, v43\n\t"
        "global_store_dwordx4 v243, v[32:35], s[66:67] sc1\n\t"
        "s_add_u32 s66, s66, 55296\n\t"
        "s_addc_u32 s67, s67, 0\n\t"
        "v_pk_fma_f32 v[48:49], v[198:199], v[128:129], v[48:49] op_sel_hi:[0,1,1] neg_lo:[1,0,0] neg_hi:[1,0,0]\n\t"
        "v_pk_fma_f32 v[50:51], v[198:199], v[130:131], v[50:51] op_sel_hi:[0,1,1] neg_lo:[1,0,0] neg_hi:[1,0,0]\n\t"
        "v_pk_fma_f32 v[52:53], v[198:199], v[132:133], v[52:53] op_sel_hi:[0,1,1] neg_lo:[1,0,0] neg_hi:[1,0,0]\n\t"
        "v_pk_fma_f32 v[54:55], v[198:199], v[134:135], v[54:55] op_sel_hi:[0,1,1] neg_lo:[1,0,0] neg_hi:[1,0,0]\n\t"
        "v_pk_fma_f32 v[56:57], v[198:199], v[136:137], v[56:57] op_sel_hi:[0,1,1] neg_lo:[1,0,0] neg_hi:[1,0,0]\n\t"
        "v_pk_fma_f32 v[58:59], v[198:199], v[138:139], v[58:59] op_sel_hi:[0,1,1] neg_lo:[1,0,0] neg_hi:[1,0,0]\n\t"
        "v_pk_fma_f32 v[60:61], v[198:199], v[140:141], v[60:61] op_sel_hi:[0,1,1] neg_lo:[1,0,0] neg_hi:[1,0,0]\n\t"
        "v_pk_fma_f32 v[62:63], v[198:199], v[142:143], v[62:63] op_sel_hi:[0,1,1] neg_lo:[1,0,0] neg_hi:[1,0,0]\n\t"
        "v_pk_fma_f32 v[48:49], v[198:199], v[48:49], v[144:145] op_sel:[1,0,0]\n\t"
        "v_pk_fma_f32 v[50:51], v[198:199], v[50:51], v[146:147] op_sel:[1,0,0]\n\t"
        "global_load_dwordx4 a[236:239], v243, s[68:69] offset:-1024\n\t"
        "v_pk_fma_f32 v[52:53], v[198:199], v[52:53], v[148:149] op_sel:[1,0,0]\n\t"
        "v_pk_fma_f32 v[54:55], v[198:199], v[54:55], v[150:151] op_sel:[1,0,0]\n\t"
        "v_pk_fma_f32 v[56:57], v[198:199], v[56:57], v[152:153] op_sel:[1,0,0]\n\t"
        "v_pk_fma_f32 v[58:59], v[198:199], v[58:59], v[154:155] op_sel:[1,0,0]\n\t"
        "v_pk_fma_f32 v[60:61], v[198:199], v[60:61], v[156:157] op_sel:[1,0,0]\n\t"
        "v_pk_fma_f32 v[62:63], v[198:199], v[62:63], v[158:159] op_sel:[1,0,0]\n\t"
        "v_max_f32 v48, 0, v48\n\t"
        "v_max_f32 v49, 0, v49\n\t"
        "v_max_f32 v50, 0, v50\n\t"
        "v_max_f32 v51, 0, v51\n\t"
        "global_load_dwordx4 a[240:243], v243, s[68:69] offset:0\n\t"
        "v_max_f32 v56, 0, v56\n\t"
        "v_max_f32 v57, 0, v57\n\t"
        "v_max_f32 v58, 0, v58\n\t"
        "v_max_f32 v59, 0, v59\n\t"
        "v_pk_mul_f32 v[48:49], v[48:49], v[52:53]\n\t"
        "v_pk_mul_f32 v[50:51], v[50:51], v[54:55]\n\t"
        "v_pk_mul_f32 v[56:57], v[56:57], v[60:61]\n\t"
        "v_pk_mul_f32 v[58:59], v[58:59], v[62:63]\n\t"
        "v_cvt_pk_bf16_f32 v48, v48, v49\n\t"
        "v_cvt_pk_bf16_f32 v49, v50, v51\n\t"
        "v_cvt_pk_bf16_f32 v50, v56, v57\n\t"
        "v_cvt_pk_bf16_f32 v51, v58, v59\n\t"
        "global_store_dwordx4 v243, v[48:51], s[66:67] sc1\n\t"
        "s_add_u32 s66, s66, 55296\n\t"
        "s_addc_u32 s67, s67, 0\n\t"
        "v_pk_fma_f32 v[64:65], v[200:201], v[128:129], v[64:65] op_sel_hi:[0,1,1] neg_lo:[1,0,0] neg_hi:[1,0,0]\n\t"
        "v_pk_fma_f32 v[66:67], v[200:201], v[130:131], v[66:67] op_sel_hi:[0,1,1] neg_lo:[1,0,0] neg_hi:[1,0,0]\n\t"
        "v_pk_fma_f32 v[68:69], v[200:201], v[132:133], v[68:69] op_sel_hi:[0,1,1] neg_lo:[1,0,0] neg_hi:[1,0,0]\n\t"
        "v_pk_fma_f32 v[70:71], v[200:201], v[134:135], v[70:71] op_sel_hi:[0,1,1] neg_lo:[1,0,0] neg_hi:[1,0,0]\n\t"
        "v_pk_fma_f32 v[72:73], v[200:201], v[136:137], v[72:73] op_sel_hi:[0,1,1] neg_lo:[1,0,0] neg_hi:[1,0,0]\n\t"
        "v_pk_fma_f32 v[74:75], v[200:201], v[138:139], v[74:75] op_sel_hi:[0,1,1] neg_lo:[1,0,0] neg_hi:[1,0,0]\n\t"
        "v_pk_fma_f32 v[76:77], v[200:201], v[140:141], v[76:77] op_sel_hi:[0,1,1] neg_lo:[1,0,0] neg_hi:[1,0,0]\n\t"
        "v_pk_fma_f32 v[78:79], v[200:201], v[142:143], v[78:79] op_sel_hi:[0,1,1] neg_lo:[1,0,0] neg_hi:[1,0,0]\n\t"
        "v_pk_fma_f32 v[64:65], v[200:201], v[64:65], v[144:145] op_sel:[1,0,0]\n\t"
        "v_pk_fma_f32 v[66:67], v[200:201], v[66:67], v[146:147] op_sel:[1,0,0]\n\t"
        "global_load_dwordx4 a[244:247], v243, s[68:69] offset:1024\n\t"
        "v_pk_fma_f32 v[68:69], v[200:201], v[68:69], v[148:149] op_sel:[1,0,0]\n\t"
        "v_pk_fma_f32 v[70:71], v[200:201], v[70:71], v[150:151] op_sel:[1,0,0]\n\t"
        "v_pk_fma_f32 v[72:73], v[200:201], v[72:73], v[152:153] op_sel:[1,0,0]\n\t"
        "v_pk_fma_f32 v[74:75], v[200:201], v[74:75], v[154:155] op_sel:[1,0,0]\n\t"
        "v_pk_fma_f32 v[76:77], v[200:201], v[76:77], v[156:157] op_sel:[1,0,0]\n\t"
        "v_pk_fma_f32 v[78:79], v[200:201], v[78:79], v[158:159] op_sel:[1,0,0]\n\t"
        "v_max_f32 v64, 0, v64\n\t"
        "v_max_f32 v65, 0, v65\n\t"
        "v_max_f32 v66, 0, v66\n\t"
        "v_max_f32 v67, 0, v67\n\t"
        "global_load_dwordx4 a[248:251], v243, s[68:69] offset:2048\n\t"
        "s_add_u32 s68, s68, 0x5000\n\t"
        "s_addc_u32 s69, s69, 0\n\t"
        "v_max_f32 v72, 0, v72\n\t"
        "v_max_f32 v73, 0, v73\n\t"
        "v_max_f32 v74, 0, v74\n\t"
        "v_max_f32 v75, 0, v75\n\t"
        "v_pk_mul_f32 v[64:65], v[64:65], v[68:69]\n\t"
        "v_pk_mul_f32 v[66:67], v[66:67], v[70:71]\n\t"
        "v_pk_mul_f32 v[72:73], v[72:73], v[76:77]\n\t"
        "v_pk_mul_f32 v[74:75], v[74:75], v[78:79]\n\t"
        "v_cvt_pk_bf16_f32 v64, v64, v65\n\t"
        "v_cvt_pk_bf16_f32 v65, v66, v67\n\t"
        "v_cvt_pk_bf16_f32 v66, v72, v73\n\t"
        "v_cvt_pk_bf16_f32 v67, v74, v75\n\t"
        "global_store_dwordx4 v243, v[64:67], s[66:67] sc1\n\t"
        "s_add_u32 s66, s66, 55296\n\t"
        "s_addc_u32 s67, s67, 0\n\t"
        "v_pk_fma_f32 v[80:81], v[202:203], v[128:129], v[80:81] op_sel_hi:[0,1,1] neg_lo:[1,0,0] neg_hi:[1,0,0]\n\t"
        "v_pk_fma_f32 v[82:83], v[202:203], v[130:131], v[82:83] op_sel_hi:[0,1,1] neg_lo:[1,0,0] neg_hi:[1,0,0]\n\t"
        "v_pk_fma_f32 v[84:85], v[202:203], v[132:133], v[84:85] op_sel_hi:[0,1,1] neg_lo:[1,0,0] neg_hi:[1,0,0]\n\t"
        "v_pk_fma_f32 v[86:87], v[202:203], v[134:135], v[86:87] op_sel_hi:[0,1,1] neg_lo:[1,0,0] neg_hi:[1,0,0]\n\t"
        "v_pk_fma_f32 v[88:89], v[202:203], v[136:137], v[88:89] op_sel_hi:[0,1,1] neg_lo:[1,0,0] neg_hi:[1,0,0]\n\t"
        "v_pk_fma_f32 v[90:91], v[202:203], v[138:139], v[90:91] op_sel_hi:[0,1,1] neg_lo:[1,0,0] neg_hi:[1,0,0]\n\t"
        "v_pk_fma_f32 v[92:93], v[202:203], v[140:141], v[92:93] op_sel_hi:[0,1,1] neg_lo:[1,0,0] neg_hi:[1,0,0]\n\t"
        "v_pk_fma_f32 v[94:95], v[202:203], v[142:143], v[94:95] op_sel_hi:[0,1,1] neg_lo:[1,0,0] neg_hi:[1,0,0]\n\t"
        "v_pk_fma_f32 v[80:81], v[202:203], v[80:81], v[144:145] op_sel:[1,0,0]\n\t"
        "v_pk_fma_f32 v[82:83], v[202:203], v[82:83], v[146:147] op_sel:[1,0,0]\n\t"
        "global_load_dwordx4 a[252:255], v243, s[68:69] offset:-2048\n\t"
        "v_pk_fma_f32 v[84:85], v[202:203], v[84:85], v[148:149] op_sel:[1,0,0]\n\t"
        "v_pk_fma_f32 v[86:87], v[202:203], v[86:87], v[150:151] op_sel:[1,0,0]\n\t"
        "v_pk_fma_f32 v[88:89], v[202:203], v[88:89], v[152:153] op_sel:[1,0,0]\n\t"
        "v_pk_fma_f32 v[90:91], v[202:203], v[90:91], v[154:155] op_sel:[1,0,0]\n\t"
        "v_pk_fma_f32 v[92:93], v[202:203], v[92:93], v[156:157] op_sel:[1,0,0]\n\t"
        "v_pk_fma_f32 v[94:95], v[202:203], v[94:95], v[158:159] op_sel:[1,0,0]\n\t"
        "v_max_f32 v80, 0, v80\n\t"
        "v_max_f32 v81, 0, v81\n\t"
        "v_max_f32 v82, 0, v82\n\t"
        "v_max_f32 v83, 0, v83\n\t"
        "global_load_dwordx4 a[0:3], v243, s[68:69] offset:-1024\n\t"
        "v_max_f32 v88, 0, v88\n\t"
        "v_max_f32 v89, 0, v89\n\t"
        "v_max_f32 v90, 0, v90\n\t"
        "v_max_f32 v91, 0, v91\n\t"
        "v_pk_mul_f32 v[80:81], v[80:81], v[84:85]\n\t"
        "v_pk_mul_f32 v[82:83], v[82:83], v[86:87]\n\t"
        "v_pk_mul_f32 v[88:89], v[88:89], v[92:93]\n\t"
        "v_pk_mul_f32 v[90:91], v[90:91], v[94:95]\n\t"
        "v_cvt_pk_bf16_f32 v80, v80, v81\n\t"
        "v_cvt_pk_bf16_f32 v81, v82, v83\n\t"
        "v_cvt_pk_bf16_f32 v82, v88, v89\n\t"
        "v_cvt_pk_bf16_f32 v83, v90, v91\n\t"
        "global_store_dwordx4 v243, v[80:83], s[66:67] sc1\n\t"
        "s_add_u32 s66, s66, 55296\n\t"
        "s_addc_u32 s67, s67, 0\n\t"
        "v_pk_fma_f32 v[96:97], v[204:205], v[128:129], v[96:97] op_sel_hi:[0,1,1] neg_lo:[1,0,0] neg_hi:[1,0,0]\n\t"
        "v_pk_fma_f32 v[98:99], v[204:205], v[130:131], v[98:99] op_sel_hi:[0,1,1] neg_lo:[1,0,0] neg_hi:[1,0,0]\n\t"
        "v_pk_fma_f32 v[100:101], v[204:205], v[132:133], v[100:101] op_sel_hi:[0,1,1] neg_lo:[1,0,0] neg_hi:[1,0,0]\n\t"
        "v_pk_fma_f32 v[102:103], v[204:205], v[134:135], v[102:103] op_sel_hi:[0,1,1] neg_lo:[1,0,0] neg_hi:[1,0,0]\n\t"
        "v_pk_fma_f32 v[104:105], v[204:205], v[136:137], v[104:105] op_sel_hi:[0,1,1] neg_lo:[1,0,0] neg_hi:[1,0,0]\n\t"
        "v_pk_fma_f32 v[106:107], v[204:205], v[138:139], v[106:107] op_sel_hi:[0,1,1] neg_lo:[1,0,0] neg_hi:[1,0,0]\n\t"
        "v_pk_fma_f32 v[108:109], v[204:205], v[140:141], v[108:109] op_sel_hi:[0,1,1] neg_lo:[1,0,0] neg_hi:[1,0,0]\n\t"
        "v_pk_fma_f32 v[110:111], v[204:205], v[142:143], v[110:111] op_sel_hi:[0,1,1] neg_lo:[1,0,0] neg_hi:[1,0,0]\n\t"
        "v_pk_fma_f32 v[96:97], v[204:205], v[96:97], v[144:145] op_sel:[1,0,0]\n\t"
        "v_pk_fma_f32 v[98:99], v[204:205], v[98:99], v[146:147] op_sel:[1,0,0]\n\t"
        "global_load_dwordx4 a[4:7], v243, s[68:69] offset:0\n\t"
        "v_pk_fma_f32 v[100:101], v[204:205], v[100:101], v[148:149] op_sel:[1,0,0]\n\t"
        "v_pk_fma_f32 v[102:103], v[204:205], v[102:103], v[150:151] op_sel:[1,0,0]\n\t"
        "v_pk_fma_f32 v[104:105], v[204:205], v[104:105], v[152:153] op_sel:[1,0,0]\n\t"
        "v_pk_fma_f32 v[106:107], v[204:205], v[106:107], v[154:155] op_sel:[1,0,0]\n\t"
        "v_pk_fma_f32 v[108:109], v[204:205], v[108:109], v[156:157] op_sel:[1,0,0]\n\t"
        "v_pk_fma_f32 v[110:111], v[204:205], v[110:111], v[158:159] op_sel:[1,0,0]\n\t"
        "v_max_f32 v96, 0, v96\n\t"
        "v_max_f32 v97, 0, v97\n\t"
        "v_max_f32 v98, 0, v98\n\t"
        "v_max_f32 v99, 0, v99\n\t"
        "global_load_dwordx4 a[8:11], v243, s[68:69] offset:1024\n\t"
        "v_max_f32 v104, 0, v104\n\t"
        "v_max_f32 v105, 0, v105\n\t"
        "v_max_f32 v106, 0, v106\n\t"
        "v_max_f32 v107, 0, v107\n\t"
        "v_pk_mul_f32 v[96:97], v[96:97], v[100:101]\n\t"
        "v_pk_mul_f32 v[98:99], v[98:99], v[102:103]\n\t"
        "v_pk_mul_f32 v[104:105], v[104:105], v[108:109]\n\t"
        "v_pk_mul_f32 v[106:107], v[106:107], v[110:111]\n\t"
        "v_cvt_pk_bf16_f32 v96, v96, v97\n\t"
        "v_cvt_pk_bf16_f32 v97, v98, v99\n\t"
        "v_cvt_pk_bf16_f32 v98, v104, v105\n\t"
        "v_cvt_pk_bf16_f32 v99, v106, v107\n\t"
        "global_store_dwordx4 v243, v[96:99], s[66:67] sc1\n\t"
        "s_add_u32 s66, s66, 55296\n\t"
        "s_addc_u32 s67, s67, 0\n\t"
        "v_pk_fma_f32 v[112:113], v[206:207], v[128:129], v[112:113] op_sel_hi:[0,1,1] neg_lo:[1,0,0] neg_hi:[1,0,0]\n\t"
        "v_pk_fma_f32 v[114:115], v[206:207], v[130:131], v[114:115] op_sel_hi:[0,1,1] neg_lo:[1,0,0] neg_hi:[1,0,0]\n\t"
        "v_pk_fma_f32 v[116:117], v[206:207], v[132:133], v[116:117] op_sel_hi:[0,1,1] neg_lo:[1,0,0] neg_hi:[1,0,0]\n\t"
        "v_pk_fma_f32 v[118:119], v[206:207], v[134:135], v[118:119] op_sel_hi:[0,1,1] neg_lo:[1,0,0] neg_hi:[1,0,0]\n\t"
        "v_pk_fma_f32 v[120:121], v[206:207], v[136:137], v[120:121] op_sel_hi:[0,1,1] neg_lo:[1,0,0] neg_hi:[1,0,0]\n\t"
        "v_pk_fma_f32 v[122:123], v[206:207], v[138:139], v[122:123] op_sel_hi:[0,1,1] neg_lo:[1,0,0] neg_hi:[1,0,0]\n\t"
        "v_pk_fma_f32 v[124:125], v[206:207], v[140:141], v[124:125] op_sel_hi:[0,1,1] neg_lo:[1,0,0] neg_hi:[1,0,0]\n\t"
        "v_pk_fma_f32 v[126:127], v[206:207], v[142:143], v[126:127] op_sel_hi:[0,1,1] neg_lo:[1,0,0] neg_hi:[1,0,0]\n\t"
        "v_pk_fma_f32 v[112:113], v[206:207], v[112:113], v[144:145] op_sel:[1,0,0]\n\t"
        "v_pk_fma_f32 v[114:115], v[206:207], v[114:115], v[146:147] op_sel:[1,0,0]\n\t"
        "global_load_dwordx4 a[12:15], v243, s[68:69] offset:2048\n\t"
        "s_add_u32 s68, s68, 0x5000\n\t"
        "s_addc_u32 s69, s69, 0\n\t"
        "v_pk_fma_f32 v[116:117], v[206:207], v[116:117], v[148:149] op_sel:[1,0,0]\n\t"
        "v_pk_fma_f32 v[118:119], v[206:207], v[118:119], v[150:151] op_sel:[1,0,0]\n\t"
        "v_pk_fma_f32 v[120:121], v[206:207], v[120:121], v[152:153] op_sel:[1,0,0]\n\t"
        "v_pk_fma_f32 v[122:123], v[206:207], v[122:123], v[154:155] op_sel:[1,0,0]\n\t"
        "v_pk_fma_f32 v[124:125], v[206:207], v[124:125], v[156:157] op_sel:[1,0,0]\n\t"
        "v_pk_fma_f32 v[126:127], v[206:207], v[126:127], v[158:159] op_sel:[1,0,0]\n\t"
        "v_max_f32 v112, 0, v112\n\t"
        "v_max_f32 v113, 0, v113\n\t"
        "v_max_f32 v114, 0, v114\n\t"
        "v_max_f32 v115, 0, v115\n\t"
        "global_load_dwordx4 a[16:19], v243, s[68:69] offset:-2048\n\t"
        "v_max_f32 v120, 0, v120\n\t"
        "v_max_f32 v121, 0, v121\n\t"
        "v_max_f32 v122, 0, v122\n\t"
        "v_max_f32 v123, 0, v123\n\t"
        "v_pk_mul_f32 v[112:113], v[112:113], v[116:117]\n\t"
        "v_pk_mul_f32 v[114:115], v[114:115], v[118:119]\n\t"
        "v_pk_mul_f32 v[120:121], v[120:121], v[124:125]\n\t"
        "v_pk_mul_f32 v[122:123], v[122:123], v[126:127]\n\t"
        "v_cvt_pk_bf16_f32 v112, v112, v113\n\t"
        "v_cvt_pk_bf16_f32 v113, v114, v115\n\t"
        "v_cvt_pk_bf16_f32 v114, v120, v121\n\t"
        "v_cvt_pk_bf16_f32 v115, v122, v123\n\t"
        "global_store_dwordx4 v243, v[112:115], s[66:67] sc1\n\t"
        "s_add_u32 s66, s66, 55296\n\t"
        "s_addc_u32 s67, s67, 0\n\t"
        "global_load_dwordx4 a[20:23], v243, s[68:69] offset:-1024\n\t"
        "global_load_dwordx4 a[24:27], v243, s[68:69] offset:0\n\t"
        "global_load_dwordx4 a[28:31], v243, s[68:69] offset:1024\n\t"
        "global_load_dwordx4 a[32:35], v243, s[68:69] offset:2048\n\t"
        "s_add_u32 s68, s68, 0x5000\n\t"
        "s_addc_u32 s69, s69, 0\n\t"
        "global_load_dwordx4 a[36:39], v243, s[68:69] offset:-2048\n\t"
        "global_load_dwordx4 a[40:43], v243, s[68:69] offset:-1024\n\t"
        "global_load_dwordx4 a[44:47], v243, s[68:69] offset:0\n\t"
        "global_load_dwordx4 a[48:51], v243, s[68:69] offset:1024\n\t"
        "s_waitcnt vmcnt(8)\n\t"
        "s_branch LSG_%=\n\t"
        "LND_%=:\n\t"
        "s_cmp_eq_u32 s53, 1\n\t"
        "s_cbranch_scc0 LN2_%=\n\t"
        "global_load_lds_dwordx4 v242, s[40:41]\n\t"
        "s_add_u32 m0, m0, 0x1000\n\t"
        "s_add_u32 s40, s40, 0x10000\n\t"
        "s_addc_u32 s41, s41, 0\n\t"
        "global_load_lds_dwordx4 v242, s[40:41]\n\t"
        "s_add_u32 m0, m0, 0x1000\n\t"
        "s_add_u32 s40, s40, 0x10000\n\t"
        "s_addc_u32 s41, s41, 0\n\t"
        "global_load_lds_dwordx4 v242, s[40:41]\n\t"
        "s_add_u32 m0, m0, 0x1000\n\t"
        "s_add_u32 s40, s40, 0x10000\n\t"
        "s_addc_u32 s41, s41, 0\n\t"
        "global_load_lds_dwordx4 v242, s[40:41]\n\t"
        "s_add_u32 m0, m0, 0x1000\n\t"
        "s_add_u32 s40, s40, 0x10000\n\t"
        "s_addc_u32 s41, s41, 0\n\t"
        "global_load_lds_dwordx4 v242, s[40:41]\n\t"
        "s_add_u32 m0, m0, 0x1000\n\t"
        "s_add_u32 s40, s40, 0x10000\n\t"
        "s_addc_u32 s41, s41, 0\n\t"
        "global_load_lds_dwordx4 v242, s[40:41]\n\t"
        "s_add_u32 m0, m0, 0x1000\n\t"
        "s_add_u32 s40, s40, 0x10000\n\t"
        "s_addc_u32 s41, s41, 0\n\t"
        "global_load_lds_dwordx4 v242, s[40:41]\n\t"
        "s_add_u32 m0, m0, 0x1000\n\t"
        "s_add_u32 s40, s40, 0x10000\n\t"
        "s_addc_u32 s41, s41, 0\n\t"
        "global_load_lds_dwordx4 v242, s[40:41]\n\t"
        "s_add_u32 m0, m0, 0x1000\n\t"
        "s_add_u32 s40, s40, 0x10000\n\t"
        "s_addc_u32 s41, s41, 0\n\t"
        "s_waitcnt vmcnt(6)\n\t"
        "s_barrier\n\t"
        "s_waitcnt vmcnt(4)\n\t"
        "s_barrier\n\t"
        "global_load_lds_dwordx4 v242, s[40:41]\n\t"
        "s_add_u32 m0, m0, 0x1000\n\t"
        "s_add_u32 s40, s40, 0x10000\n\t"
        "s_addc_u32 s41, s41, 0\n\t"
        "global_load_lds_dwordx4 v242, s[40:41]\n\t"
        "s_add_u32 m0, m0, 0x1000\n\t"
        "s_add_u32 s40, s40, 0x10000\n\t"
        "s_addc_u32 s41, s41, 0\n\t"
        "s_waitcnt vmcnt(4)\n\t"
        "s_barrier\n\t"
        "global_load_lds_dwordx4 v242, s[40:41]\n\t"
        "s_add_u32 m0, m0, 0x1000\n\t"
        "s_add_u32 s40, s40, 0x10000\n\t"
        "s_addc_u32 s41, s41, 0\n\t"
        "global_load_lds_dwordx4 v242, s[40:41]\n\t"
        "s_add_u32 m0, m0, 0x1000\n\t"
        "s_add_u32 s40, s40, 0x10000\n\t"
        "s_addc_u32 s41, s41, 0\n\t"
        "s_waitcnt vmcnt(4)\n\t"
        "s_barrier\n\t"
        "global_load_lds_dwordx4 v242, s[40:41]\n\t"
        "s_add_u32 m0, m0, 0x1000\n\t"
        "s_add_u32 s40, s40, 0x10000\n\t"
        "s_addc_u32 s41, s41, 0\n\t"
        "global_load_lds_dwordx4 v242, s[40:41]\n\t"
        "s_add_u32 m0, m0, 0x1000\n\t"
        "s_add_u32 s40, s40, 0x10000\n\t"
        "s_addc_u32 s41, s41, 0\n\t"
        "s_waitcnt vmcnt(4)\n\t"
        "s_barrier\n\t"
        "global_load_lds_dwordx4 v242, s[40:41]\n\t"
        "s_add_u32 m0, m0, 0x1000\n\t"
        "s_add_u32 s40, s40, 0x10000\n\t"
        "s_addc_u32 s41, s41, 0\n\t"
        "global_load_lds_dwordx4 v242, s[40:41]\n\t"
        "s_add_u32 m0, m0, 0x1000\n\t"
        "s_add_u32 s40, s40, 0x10000\n\t"
        "s_addc_u32 s41, s41, 0\n\t"
        "s_waitcnt vmcnt(4)\n\t"
        "s_barrier\n\t"
        "global_load_lds_dwordx4 v242, s[40:41]\n\t"
        "s_add_u32 m0, m0, 0x1000\n\t"
        "s_add_u32 s40, s40, 0x10000\n\t"
        "s_addc_u32 s41, s41, 0\n\t"
        "global_load_lds_dwordx4 v242, s[40:41]\n\t"
        "s_add_u32 m0, m0, 0x1000\n\t"
        "s_add_u32 s40, s40, 0x10000\n\t"
        "s_addc_u32 s41, s41, 0\n\t"
        "s_waitcnt vmcnt(4)\n\t"
        "s_barrier\n\t"
        "global_load_lds_dwordx4 v242, s[40:41]\n\t"
        "s_add_u32 m0, m0, 0x1000\n\t"
        "s_add_u32 s40, s40, 0x10000\n\t"
        "s_addc_u32 s41, s41, 0\n\t"
        "global_load_lds_dwordx4 v242, s[40:41]\n\t"
        "s_add_u32 m0, m0, 0x1000\n\t"
        "s_add_u32 s40, s40, 0x10000\n\t"
        "s_addc_u32 s41, s41, 0\n\t"
        "s_waitcnt vmcnt(4)\n\t"
        "s_barrier\n\t"
        "global_load_lds_dwordx4 v242, s[40:41]\n\t"
        "s_add_u32 m0, m0, 0x1000\n\t"
        "s_add_u32 s40, s40, 0x10000\n\t"
        "s_addc_u32 s41, s41, 0\n\t"
        "global_load_lds_dwordx4 v242, s[40:41]\n\t"
        "s_add_u32 m0, m0, 0x1000\n\t"
        "s_add_u32 s40, s40, 0x10000\n\t"
        "s_addc_u32 s41, s41, 0\n\t"
        "s_waitcnt vmcnt(4)\n\t"
        "s_barrier\n\t"
        "global_load_lds_dwordx4 v242, s[40:41]\n\t"
        "s_add_u32 m0, m0, 0x1000\n\t"
        "s_add_u32 s40, s40, 0x10000\n\t"
        "s_addc_u32 s41, s41, 0\n\t"
        "global_load_lds_dwordx4 v242, s[40:41]\n\t"
        "s_add_u32 m0, m0, 0x1000\n\t"
        "s_add_u32 s40, s40, 0x10000\n\t"
        "s_addc_u32 s41, s41, 0\n\t"
        "s_waitcnt vmcnt(4)\n\t"
        "s_barrier\n\t"
        "global_load_lds_dwordx4 v242, s[40:41]\n\t"
        "s_add_u32 m0, m0, 0x1000\n\t"
        "s_add_u32 s40, s40, 0x10000\n\t"
        "s_addc_u32 s41, s41, 0\n\t"
        "global_load_lds_dwordx4 v242, s[40:41]\n\t"
        "s_add_u32 m0, m0, 0x1000\n\t"
        "s_add_u32 s40, s40, 0x10000\n\t"
        "s_addc_u32 s41, s41, 0\n\t"
        "s_waitcnt vmcnt(4)\n\t"
        "s_barrier\n\t"
        "global_load_lds_dwordx4 v242, s[40:41]\n\t"
        "s_add_u32 m0, m0, 0x1000\n\t"
        "s_add_u32 s40, s40, 0x10000\n\t"
        "s_addc_u32 s41, s41, 0\n\t"
        "global_load_lds_dwordx4 v242, s[40:41]\n\t"
        "s_add_u32 m0, m0, 0x1000\n\t"
        "s_add_u32 s40, s40, 0x10000\n\t"
        "s_addc_u32 s41, s41, 0\n\t"
        "s_waitcnt vmcnt(4)\n\t"
        "s_barrier\n\t"
        "global_load_lds_dwordx4 v242, s[40:41]\n\t"
        "s_add_u32 m0, m0, 0x1000\n\t"
        "s_add_u32 s40, s40, 0x10000\n\t"
        "s_addc_u32 s41, s41, 0\n\t"
        "global_load_lds_dwordx4 v242, s[40:41]\n\t"
        "s_add_u32 m0, m0, 0x1000\n\t"
        "s_add_u32 s40, s40, 0x10000\n\t"
        "s_addc_u32 s41, s41, 0\n\t"
        "s_waitcnt vmcnt(4)\n\t"
        "s_barrier\n\t"
        "global_load_lds_dwordx4 v242, s[40:41]\n\t"
        "s_add_u32 m0, m0, 0x1000\n\t"
        "s_add_u32 s40, s40, 0x10000\n\t"
        "s_addc_u32 s41, s41, 0\n\t"
        "global_load_lds_dwordx4 v242, s[40:41]\n\t"
        "s_add_u32 m0, m0, 0x1000\n\t"
        "s_add_u32 s40, s40, 0x10000\n\t"
        "s_addc_u32 s41, s41, 0\n\t"
        "s_waitcnt vmcnt(4)\n\t"
        "s_barrier\n\t"
        "global_load_lds_dwordx4 v242, s[40:41]\n\t"
        "s_add_u32 m0, m0, 0x1000\n\t"
        "s_add_u32 s40, s40, 0x10000\n\t"
        "s_addc_u32 s41, s41, 0\n\t"
        "global_load_lds_dwordx4 v242, s[40:41]\n\t"
        "s_add_u32 m0, m0, 0x1000\n\t"
        "s_add_u32 s40, s40, 0x10000\n\t"
        "s_addc_u32 s41, s41, 0\n\t"
        "s_waitcnt vmcnt(4)\n\t"
        "s_barrier\n\t"
        "global_load_lds_dwordx4 v242, s[40:41]\n\t"
        "s_add_u32 m0, m0, 0x1000\n\t"
        "s_add_u32 s40, s40, 0x10000\n\t"
        "s_addc_u32 s41, s41, 0\n\t"
        "global_load_lds_dwordx4 v242, s[40:41]\n\t"
        "s_add_u32 m0, m0, 0x1000\n\t"
        "s_add_u32 s40, s40, 0x10000\n\t"
        "s_addc_u32 s41, s41, 0\n\t"
        "s_waitcnt vmcnt(4)\n\t"
        "s_barrier\n\t"
        "global_load_lds_dwordx4 v242, s[40:41]\n\t"
        "s_add_u32 m0, m0, 0x1000\n\t"
        "s_add_u32 s40, s40, 0x10000\n\t"
        "s_addc_u32 s41, s41, 0\n\t"
        "global_load_lds_dwordx4 v242, s[40:41]\n\t"
        "s_add_u32 m0, m0, 0x1000\n\t"
        "s_add_u32 s40, s40, 0x10000\n\t"
        "s_addc_u32 s41, s41, 0\n\t"
        "s_waitcnt vmcnt(4)\n\t"
        "s_barrier\n\t"
        "global_load_lds_dwordx4 v242, s[40:41]\n\t"
        "s_add_u32 m0, m0, 0x1000\n\t"
        "s_add_u32 s40, s40, 0x10000\n\t"
        "s_addc_u32 s41, s41, 0\n\t"
        "global_load_lds_dwordx4 v242, s[40:41]\n\t"
        "s_add_u32 m0, m0, 0x1000\n\t"
        "s_add_u32 s40, s40, 0x10000\n\t"
        "s_addc_u32 s41, s41, 0\n\t"
        "s_waitcnt vmcnt(4)\n\t"
        "s_barrier\n\t"
        "s_waitcnt vmcnt(2)\n\t"
        "s_barrier\n\t"
        "s_waitcnt vmcnt(0)\n\t"
        "s_barrier\n\t"
        "s_cmp_ge_u32 s62, 2\n\t"
        "s_cbranch_scc1 LNO2_%=\n\t"
        "s_add_u32 s72, s68, 266240\n\t"
        "s_addc_u32 s73, s69, 0\n\t"
        "global_load_dwordx4 a[112:115], v243, s[72:73] offset:-2048\n\t"
        "global_load_dwordx4 a[116:119], v243, s[72:73] offset:-1024\n\t"
        "global_load_dwordx4 a[120:123], v243, s[72:73] offset:0\n\t"
        "global_load_dwordx4 a[124:127], v243, s[72:73] offset:1024\n\t"
        "global_load_dwordx4 a[128:131], v243, s[72:73] offset:2048\n\t"
        "LNO2_%=:\n\t"
        "global_load_dwordx4 a[132:135], v243, s[68:69] offset:-2048\n\t"
        "global_load_dwordx4 a[136:139], v243, s[68:69] offset:-1024\n\t"
        "global_load_dwordx4 a[140:143], v243, s[68:69] offset:0\n\t"
        "global_load_dwordx4 a[144:147], v243, s[68:69] offset:1024\n\t"
        "global_load_dwordx4 a[148:151], v243, s[68:69] offset:2048\n\t"
        "s_add_u32 s68, s68, 0x5000\n\t"
        "s_addc_u32 s69, s69, 0\n\t"
        "global_load_dwordx4 a[152:155], v243, s[68:69] offset:-2048\n\t"
        "global_load_dwordx4 a[156:159], v243, s[68:69] offset:-1024\n\t"
        "global_load_dwordx4 a[160:163], v243, s[68:69] offset:0\n\t"
        "global_load_dwordx4 a[164:167], v243, s[68:69] offset:1024\n\t"
        "global_load_dwordx4 a[168:171], v243, s[68:69] offset:2048\n\t"
        "s_add_u32 s68, s68, 0x5000\n\t"
        "s_addc_u32 s69, s69, 0\n\t"
        "global_load_dwordx4 a[172:175], v243, s[68:69] offset:-2048\n\t"
        "global_load_dwordx4 a[176:179], v243, s[68:69] offset:-1024\n\t"
        "global_load_dwordx4 a[180:183], v243, s[68:69] offset:0\n\t"
        "global_load_dwordx4 a[184:187], v243, s[68:69] offset:1024\n\t"
        "global_load_dwordx4 a[188:191], v243, s[68:69] offset:2048\n\t"
        "s_add_u32 s68, s68, 0x5000\n\t"
        "s_addc_u32 s69, s69, 0\n\t"
        "global_load_dwordx4 a[192:195], v243, s[68:69] offset:-2048\n\t"
        "global_load_dwordx4 a[196:199], v243, s[68:69] offset:-1024\n\t"
        "global_load_dwordx4 a[200:203], v243, s[68:69] offset:0\n\t"
        "global_load_dwordx4 a[204:207], v243, s[68:69] offset:1024\n\t"
        "global_load_dwordx4 a[208:211], v243, s[68:69] offset:2048\n\t"
        "s_add_u32 s68, s68, 0x5000\n\t"
        "s_addc_u32 s69, s69, 0\n\t"
        "global_load_dwordx4 a[212:215], v243, s[68:69] offset:-2048\n\t"
        "global_load_dwordx4 a[216:219], v243, s[68:69] offset:-1024\n\t"
        "global_load_dwordx4 a[220:223], v243, s[68:69] offset:0\n\t"
        "global_load_dwordx4 a[224:227], v243, s[68:69] offset:1024\n\t"
        "global_load_dwordx4 a[228:231], v243, s[68:69] offset:2048\n\t"
        "s_add_u32 s68, s68, 0x5000\n\t"
        "s_addc_u32 s69, s69, 0\n\t"
        "global_load_dwordx4 a[232:235], v243, s[68:69] offset:-2048\n\t"
        "global_load_dwordx4 a[236:239], v243, s[68:69] offset:-1024\n\t"
        "global_load_dwordx4 a[240:243], v243, s[68:69] offset:0\n\t"
        "global_load_dwordx4 a[244:247], v243, s[68:69] offset:1024\n\t"
        "global_load_dwordx4 a[248:251], v243, s[68:69] offset:2048\n\t"
        "s_add_u32 s68, s68, 0x5000\n\t"
        "s_addc_u32 s69, s69, 0\n\t"
        "global_load_dwordx4 a[252:255], v243, s[68:69] offset:-2048\n\t"
        "global_load_dwordx4 a[0:3], v243, s[68:69] offset:-1024\n\t"
        "global_load_dwordx4 a[4:7], v243, s[68:69] offset:0\n\t"
        "global_load_dwordx4 a[8:11], v243, s[68:69] offset:1024\n\t"
        "global_load_dwordx4 a[12:15], v243, s[68:69] offset:2048\n\t"
        "s_add_u32 s68, s68, 0x5000\n\t"
        "s_addc_u32 s69, s69, 0\n\t"
        "global_load_dwordx4 a[16:19], v243, s[68:69] offset:-2048\n\t"
        "global_load_dwordx4 a[20:23], v243, s[68:69] offset:-1024\n\t"
        "global_load_dwordx4 a[24:27], v243, s[68:69] offset:0\n\t"
        "global_load_dwordx4 a[28:31], v243, s[68:69] offset:1024\n\t"
        "global_load_dwordx4 a[32:35], v243, s[68:69] offset:2048\n\t"
        "s_add_u32 s68, s68, 0x5000\n\t"
        "s_addc_u32 s69, s69, 0\n\t"
        "global_load_dwordx4 a[36:39], v243, s[68:69] offset:-2048\n\t"
        "global_load_dwordx4 a[40:43], v243, s[68:69] offset:-1024\n\t"
        "global_load_dwordx4 a[44:47], v243, s[68:69] offset:0\n\t"
        "global_load_dwordx4 a[48:51], v243, s[68:69] offset:1024\n\t"
        "LSG_%=:\n\t"
        "s_barrier\n\t"
        "s_cmp_eq_u32 s62, 0\n\t"
        "s_cbranch_scc0 LRS_%=\n\t"
        "s_add_u32 s72, s64, s57\n\t"
        "s_addc_u32 s73, s65, 0\n\t"
        "s_mov_b64 exec, 0xff\n\t"
        "v_mov_b32 v252, 1\n\t"
        "global_atomic_add v251, v252, s[72:73]\n\t"
        "s_mov_b64 exec, -1\n\t"
        "LRS_%=:\n\t"
        "s_waitcnt vmcnt(20)\n\t"
        "global_load_dwordx4 a[52:55], v243, s[68:69] offset:2048\n\t"
        "s_add_u32 s68, s68, 0x5000\n\t"
        "s_addc_u32 s69, s69, 0\n\t"
        "global_load_dwordx4 a[56:59], v243, s[68:69] offset:-2048\n\t"
        "global_load_dwordx4 a[60:63], v243, s[68:69] offset:-1024\n\t"
        "global_load_dwordx4 a[64:67], v243, s[68:69] offset:0\n\t"
        "global_load_dwordx4 a[68:71], v243, s[68:69] offset:1024\n\t"
        "global_load_dwordx4 a[72:75], v243, s[68:69] offset:2048\n\t"
        "s_add_u32 s68, s68, 0x5000\n\t"
        "s_addc_u32 s69, s69, 0\n\t"
        "global_load_dwordx4 a[76:79], v243, s[68:69] offset:-2048\n\t"
        "global_load_dwordx4 a[80:83], v243, s[68:69] offset:-1024\n\t"
        "global_load_dwordx4 a[84:87], v243, s[68:69] offset:0\n\t"
        "global_load_dwordx4 a[88:91], v243, s[68:69] offset:1024\n\t"
        "global_load_dwordx4 a[92:95], v243, s[68:69] offset:2048\n\t"
        "s_add_u32 s68, s68, 0x5000\n\t"
        "s_addc_u32 s69, s69, 0\n\t"
        "global_load_dwordx4 a[96:99], v243, s[68:69] offset:-2048\n\t"
        "global_load_dwordx4 a[100:103], v243, s[68:69] offset:-1024\n\t"
        "global_load_dwordx4 a[104:107], v243, s[68:69] offset:0\n\t"
        "global_load_dwordx4 a[108:111], v243, s[68:69] offset:1024\n\t"
        "global_load_dwordx4 v[216:219], v243, s[68:69] offset:2048\n\t"
        "s_add_u32 s68, s68, 0x5000\n\t"
        "s_addc_u32 s69, s69, 0\n\t"
        "global_load_dwordx4 v[220:223], v243, s[68:69] offset:-2048\n\t"
        "global_load_dwordx4 v[224:227], v243, s[68:69] offset:-1024\n\t"
        "global_load_dwordx4 v[228:231], v243, s[68:69] offset:0\n\t"
        "global_load_dwordx4 v[232:235], v243, s[68:69] offset:1024\n\t"
        "global_load_dwordx4 v[236:239], v243, s[68:69] offset:2048\n\t"
        "s_add_u32 s68, s68, 0x5000\n\t"
        "s_addc_u32 s69, s69, 0\n\t"
        "s_branch LPL_%=\n\t"
        "LN2_%=:\n\t"
        "s_cmp_ge_u32 s62, 2\n\t"
        "s_cbranch_scc1 LNO3_%=\n\t"
        "s_add_u32 s72, s68, 266240\n\t"
        "s_addc_u32 s73, s69, 0\n\t"
        "global_load_dwordx4 a[112:115], v243, s[72:73] offset:-2048\n\t"
        "global_load_dwordx4 a[116:119], v243, s[72:73] offset:-1024\n\t"
        "global_load_dwordx4 a[120:123], v243, s[72:73] offset:0\n\t"
        "global_load_dwordx4 a[124:127], v243, s[72:73] offset:1024\n\t"
        "global_load_dwordx4 a[128:131], v243, s[72:73] offset:2048\n\t"
        "LNO3_%=:\n\t"
        "global_load_dwordx4 a[132:135], v243, s[68:69] offset:-2048\n\t"
        "global_load_dwordx4 a[136:139], v243, s[68:69] offset:-1024\n\t"
        "global_load_dwordx4 a[140:143], v243, s[68:69] offset:0\n\t"
        "global_load_dwordx4 a[144:147], v243, s[68:69] offset:1024\n\t"
        "global_load_dwordx4 a[148:151], v243, s[68:69] offset:2048\n\t"
        "s_add_u32 s68, s68, 0x5000\n\t"
        "s_addc_u32 s69, s69, 0\n\t"
        "global_load_dwordx4 a[152:155], v243, s[68:69] offset:-2048\n\t"
        "global_load_dwordx4 a[156:159], v243, s[68:69] offset:-1024\n\t"
        "global_load_dwordx4 a[160:163], v243, s[68:69] offset:0\n\t"
        "global_load_dwordx4 a[164:167], v243, s[68:69] offset:1024\n\t"
        "global_load_dwordx4 a[168:171], v243, s[68:69] offset:2048\n\t"
        "s_add_u32 s68, s68, 0x5000\n\t"
        "s_addc_u32 s69, s69, 0\n\t"
        "global_load_dwordx4 a[172:175], v243, s[68:69] offset:-2048\n\t"
        "global_load_dwordx4 a[176:179], v243, s[68:69] offset:-1024\n\t"
        "global_load_dwordx4 a[180:183], v243, s[68:69] offset:0\n\t"
        "global_load_dwordx4 a[184:187], v243, s[68:69] offset:1024\n\t"
        "global_load_dwordx4 a[188:191], v243, s[68:69] offset:2048\n\t"
        "s_add_u32 s68, s68, 0x5000\n\t"
        "s_addc_u32 s69, s69, 0\n\t"
        "global_load_dwordx4 a[192:195], v243, s[68:69] offset:-2048\n\t"
        "global_load_dwordx4 a[196:199], v243, s[68:69] offset:-1024\n\t"
        "global_load_dwordx4 a[200:203], v243, s[68:69] offset:0\n\t"
        "global_load_dwordx4 a[204:207], v243, s[68:69] offset:1024\n\t"
        "global_load_dwordx4 a[208:211], v243, s[68:69] offset:2048\n\t"
        "s_add_u32 s68, s68, 0x5000\n\t"
        "s_addc_u32 s69, s69, 0\n\t"
        "global_load_dwordx4 a[212:215], v243, s[68:69] offset:-2048\n\t"
        "global_load_dwordx4 a[216:219], v243, s[68:69] offset:-1024\n\t"
        "global_load_dwordx4 a[220:223], v243, s[68:69] offset:0\n\t"
        "global_load_dwordx4 a[224:227], v243, s[68:69] offset:1024\n\t"
        "global_load_dwordx4 a[228:231], v243, s[68:69] offset:2048\n\t"
        "s_add_u32 s68, s68, 0x5000\n\t"
        "s_addc_u32 s69, s69, 0\n\t"
        "global_load_dwordx4 a[232:235], v243, s[68:69] offset:-2048\n\t"
        "global_load_dwordx4 a[236:239], v243, s[68:69] offset:-1024\n\t"
        "global_load_dwordx4 a[240:243], v243, s[68:69] offset:0\n\t"
        "global_load_dwordx4 a[244:247], v243, s[68:69] offset:1024\n\t"
        "global_load_dwordx4 a[248:251], v243, s[68:69] offset:2048\n\t"
        "s_add_u32 s68, s68, 0x5000\n\t"
        "s_addc_u32 s69, s69, 0\n\t"
        "global_load_dwordx4 a[252:255], v243, s[68:69] offset:-2048\n\t"
        "global_load_dwordx4 a[0:3], v243, s[68:69] offset:-1024\n\t"
        "global_load_dwordx4 a[4:7], v243, s[68:69] offset:0\n\t"
        "global_load_dwordx4 a[8:11], v243, s[68:69] offset:1024\n\t"
        "global_load_dwordx4 a[12:15], v243, s[68:69] offset:2048\n\t"
        "s_add_u32 s68, s68, 0x5000\n\t"
        "s_addc_u32 s69, s69, 0\n\t"
        "global_load_dwordx4 a[16:19], v243, s[68:69] offset:-2048\n\t"
        "global_load_dwordx4 a[20:23], v243, s[68:69] offset:-1024\n\t"
        "global_load_dwordx4 a[24:27], v243, s[68:69] offset:0\n\t"
        "global_load_dwordx4 a[28:31], v243, s[68:69] offset:1024\n\t"
        "global_load_dwordx4 a[32:35], v243, s[68:69] offset:2048\n\t"
        "s_add_u32 s68, s68, 0x5000\n\t"
        "s_addc_u32 s69, s69, 0\n\t"
        "global_load_dwordx4 a[36:39], v243, s[68:69] offset:-2048\n\t"
        "global_load_dwordx4 a[40:43], v243, s[68:69] offset:-1024\n\t"
        "global_load_dwordx4 a[44:47], v243, s[68:69] offset:0\n\t"
        "global_load_dwordx4 a[48:51], v243, s[68:69] offset:1024\n\t"
        "global_load_dwordx4 a[52:55], v243, s[68:69] offset:2048\n\t"
        "s_add_u32 s68, s68, 0x5000\n\t"
        "s_addc_u32 s69, s69, 0\n\t"
        "global_load_dwordx4 a[56:59], v243, s[68:69] offset:-2048\n\t"
        "global_load_dwordx4 a[60:63], v243, s[68:69] offset:-1024\n\t"
        "global_load_dwordx4 a[64:67], v243, s[68:69] offset:0\n\t"
        "global_load_dwordx4 a[68:71], v243, s[68:69] offset:1024\n\t"
        "global_load_dwordx4 a[72:75], v243, s[68:69] offset:2048\n\t"
        "s_add_u32 s68, s68, 0x5000\n\t"
        "s_addc_u32 s69, s69, 0\n\t"
        "global_load_dwordx4 a[76:79], v243, s[68:69] offset:-2048\n\t"
        "global_load_dwordx4 a[80:83], v243, s[68:69] offset:-1024\n\t"
        "global_load_dwordx4 a[84:87], v243, s[68:69] offset:0\n\t"
        "global_load_dwordx4 a[88:91], v243, s[68:69] offset:1024\n\t"
        "global_load_dwordx4 a[92:95], v243, s[68:69] offset:2048\n\t"
        "s_add_u32 s68, s68, 0x5000\n\t"
        "s_addc_u32 s69, s69, 0\n\t"
        "global_load_dwordx4 a[96:99], v243, s[68:69] offset:-2048\n\t"
        "s_waitcnt vmcnt(48)\n\t"
        "global_load_dwordx4 a[100:103], v243, s[68:69] offset:-1024\n\t"
        "global_load_dwordx4 a[104:107], v243, s[68:69] offset:0\n\t"
        "global_load_dwordx4 a[108:111], v243, s[68:69] offset:1024\n\t"
        "global_load_dwordx4 v[216:219], v243, s[68:69] offset:2048\n\t"
        "s_add_u32 s68, s68, 0x5000\n\t"
        "s_addc_u32 s69, s69, 0\n\t"
        "global_load_dwordx4 v[220:223], v243, s[68:69] offset:-2048\n\t"
        "global_load_dwordx4 v[224:227], v243, s[68:69] offset:-1024\n\t"
        "global_load_dwordx4 v[228:231], v243, s[68:69] offset:0\n\t"
        "global_load_dwordx4 v[232:235], v243, s[68:69] offset:1024\n\t"
        "s_waitcnt vmcnt(48)\n\t"
        "global_load_dwordx4 v[236:239], v243, s[68:69] offset:2048\n\t"
        "s_add_u32 s68, s68, 0x5000\n\t"
        "s_addc_u32 s69, s69, 0\n\t"
        "s_barrier\n\t"
        "LPL_%=:\n\t"
        "s_waitcnt vmcnt(40)\n\t"
        "s_add_u32 s72, s64, s59\n\t"
        "s_addc_u32 s73, s65, 0\n\t"
        "s_mov_b32 m0, s60\n\t"
        "s_nop 0\n\t"
        "global_load_lds_dwordx4 v249, s[72:73]\n\t"
        "s_add_u32 m0, m0, 0x1000\n\t"
        "s_add_u32 s72, s72, 0x20000\n\t"
        "s_addc_u32 s73, s73, 0\n\t"
        "global_load_lds_dwordx4 v249, s[72:73]\n\t"
        "s_add_u32 m0, m0, 0x1000\n\t"
        "s_add_u32 s72, s72, 0x20000\n\t"
        "s_addc_u32 s73, s73, 0\n\t"
        "s_cmp_ge_u32 s62, 2\n\t"
        "s_cbranch_scc1 LX3_%=\n\t"
        "global_load_lds_dwordx4 v249, s[72:73]\n\t"
        "s_add_u32 m0, m0, 0x1000\n\t"
        "s_add_u32 s72, s72, 0x20000\n\t"
        "s_addc_u32 s73, s73, 0\n\t"
        "LX3_%=:\n\t"
        "s_add_u32 s72, s64, s61\n\t"
        "s_addc_u32 s73, s65, 0\n\t"
        "global_load_dwordx4 v[176:179], v250, s[72:73] offset:0\n\t"
        "global_load_dwordx4 v[180:183], v250, s[72:73] offset:16\n\t"
        "global_load_dwordx4 v[184:187], v250, s[72:73] offset:32\n\t"
        "global_load_dwordx4 v[188:191], v250, s[72:73] offset:48\n\t"
        "global_load_dwordx4 v[192:195], v250, s[72:73] offset:64\n\t"
        "s_cmp_eq_u32 s62, 3\n\t"
        "s_cbranch_scc0 LGO_%=\n\t"
        "s_add_u32 s72, s64, s58\n\t"
        "s_addc_u32 s73, s65, 0\n\t"
        "v_mov_b32 v253, 0\n\t"
        "s_mov_b32 s76, 0x20000\n\t"
        "LPP_%=:\n\t"
        "global_load_dword v252, v253, s[72:73] sc1\n\t"
        "s_waitcnt vmcnt(0)\n\t"
        "v_readfirstlane_b32 s77, v252\n\t"
        "s_nop 1\n\t"
        "s_cmp_ge_u32 s77, 14\n\t"
        "s_cbranch_scc1 LGO_%=\n\t"
        "s_sub_u32 s76, s76, 1\n\t"
        "s_cbranch_scc1 LGO_%=\n\t"
        "s_sleep 1\n\t"
        "s_branch LPP_%=\n\t"
        "LGO_%=:\n\t"
        "s_barrier\n\t"
        "s_waitcnt vmcnt(30)\n\t"
        "s_cmp_ge_u32 s62, 2\n\t"
        "s_cbranch_scc1 LH1_%=\n\t"
        "s_add_u32 s72, s70, 53248\n\t"
        "s_addc_u32 s73, s71, 0\n\t"
        "global_load_dwordx4 v[196:199], v255, s[72:73] sc1\n\t"
        "global_load_dwordx4 v[200:203], v246, s[72:73] sc1\n\t"
        "global_load_dwordx4 v[204:207], v247, s[72:73] sc1\n\t"
        "global_load_dwordx4 v[208:211], v248, s[72:73] sc1\n\t"
        "LH1_%=:\n\t"
        "global_load_dwordx4 v[80:83], v255, s[70:71] sc1\n\t"
        "global_load_dwordx4 v[84:87], v246, s[70:71] sc1\n\t"
        "global_load_dwordx4 v[88:91], v247, s[70:71] sc1\n\t"
        "global_load_dwordx4 v[92:95], v248, s[70:71] sc1\n\t"
        "s_add_u32 s70, s70, 0x1000\n\t"
        "s_addc_u32 s71, s71, 0\n\t"
        "global_load_dwordx4 v[96:99], v255, s[70:71] sc1\n\t"
        "global_load_dwordx4 v[100:103], v246, s[70:71] sc1\n\t"
        "global_load_dwordx4 v[104:107], v247, s[70:71] sc1\n\t"
        "global_load_dwordx4 v[108:111], v248, s[70:71] sc1\n\t"
        "s_add_u32 s70, s70, 0x1000\n\t"
        "s_addc_u32 s71, s71, 0\n\t"
        "global_load_dwordx4 v[112:115], v255, s[70:71] sc1\n\t"
        "global_load_dwordx4 v[116:119], v246, s[70:71] sc1\n\t"
        "global_load_dwordx4 v[120:123], v247, s[70:71] sc1\n\t"
        "global_load_dwordx4 v[124:127], v248, s[70:71] sc1\n\t"
        "s_add_u32 s70, s70, 0x1000\n\t"
        "s_addc_u32 s71, s71, 0\n\t"
        "s_waitcnt vmcnt(8)\n\t"
        "v_mfma_f32_16x16x32_bf16 v[0:3], a[132:135], v[80:83], 0\n\t"
        "v_mfma_f32_16x16x32_bf16 v[4:7], a[136:139], v[80:83], 0\n\t"
        "v_mfma_f32_16x16x32_bf16 v[8:11], a[140:143], v[80:83], 0\n\t"
        "v_mfma_f32_16x16x32_bf16 v[12:15], a[144:147], v[80:83], 0\n\t"
        "v_mfma_f32_16x16x32_bf16 v[16:19], a[148:151], v[80:83], 0\n\t"
        "v_mfma_f32_16x16x32_bf16 v[20:23], a[132:135], v[84:87], 0\n\t"
        "v_mfma_f32_16x16x32_bf16 v[24:27], a[136:139], v[84:87], 0\n\t"
        "global_load_dwordx4 v[80:83], v255, s[70:71] sc1\n\t"
        "v_mfma_f32_16x16x32_bf16 v[28:31], a[140:143], v[84:87], 0\n\t"
        "v_mfma_f32_16x16x32_bf16 v[32:35], a[144:147], v[84:87], 0\n\t"
        "v_mfma_f32_16x16x32_bf16 v[36:39], a[148:151], v[84:87], 0\n\t"
        "v_mfma_f32_16x16x32_bf16 v[40:43], a[132:135], v[88:91], 0\n\t"
        "v_mfma_f32_16x16x32_bf16 v[44:47], a[136:139], v[88:91], 0\n\t"
        "global_load_dwordx4 v[84:87], v246, s[70:71] sc1\n\t"
        "v_mfma_f32_16x16x32_bf16 v[48:51], a[140:143], v[88:91], 0\n\t"
        "v_mfma_f32_16x16x32_bf16 v[52:55], a[144:147], v[88:91], 0\n\t"
        "v_mfma_f32_16x16x32_bf16 v[56:59], a[148:151], v[88:91], 0\n\t"
        "v_mfma_f32_16x16x32_bf16 v[60:63], a[132:135], v[92:95], 0\n\t"
        "v_mfma_f32_16x16x32_bf16 v[64:67], a[136:139], v[92:95], 0\n\t"
        "global_load_dwordx4 v[88:91], v247, s[70:71] sc1\n\t"
        "v_mfma_f32_16x16x32_bf16 v[68:71], a[140:143], v[92:95], 0\n\t"
        "v_mfma_f32_16x16x32_bf16 v[72:75], a[144:147], v[92:95], 0\n\t"
        "v_mfma_f32_16x16x32_bf16 v[76:79], a[148:151], v[92:95], 0\n\t"
        "global_load_dwordx4 v[92:95], v248, s[70:71] sc1\n\t"
        "s_add_u32 s70, s70, 0x1000\n\t"
        "s_addc_u32 s71, s71, 0\n\t"
        "s_waitcnt vmcnt(8)\n\t"
        "v_mfma_f32_16x16x32_bf16 v[0:3], a[152:155], v[96:99], v[0:3]\n\t"
        "v_mfma_f32_16x16x32_bf16 v[4:7], a[156:159], v[96:99], v[4:7]\n\t"
        "v_mfma_f32_16x16x32_bf16 v[8:11], a[160:163], v[96:99], v[8:11]\n\t"
        "v_mfma_f32_16x16x32_bf16 v[12:15], a[164:167], v[96:99], v[12:15]\n\t"
        "v_mfma_f32_16x16x32_bf16 v[16:19], a[168:171], v[96:99], v[16:19]\n\t"
        "v_mfma_f32_16x16x32_bf16 v[20:23], a[152:155], v[100:103], v[20:23]\n\t"
        "v_mfma_f32_16x16x32_bf16 v[24:27], a[156:159], v[100:103], v[24:27]\n\t"
        "global_load_dwordx4 v[96:99], v255, s[70:71] sc1\n\t"
        "v_mfma_f32_16x16x32_bf16 v[28:31], a[160:163], v[100:103], v[28:31]\n\t"
        "v_mfma_f32_16x16x32_bf16 v[32:35], a[164:167], v[100:103], v[32:35]\n\t"
        "v_mfma_f32_16x16x32_bf16 v[36:39], a[168:171], v[100:103], v[36:39]\n\t"
        "v_mfma_f32_16x16x32_bf16 v[40:43], a[152:155], v[104:107], v[40:43]\n\t"
        "v_mfma_f32_16x16x32_bf16 v[44:47], a[156:159], v[104:107], v[44:47]\n\t"
        "global_load_dwordx4 v[100:103], v246, s[70:71] sc1\n\t"
        "v_mfma_f32_16x16x32_bf16 v[48:51], a[160:163], v[104:107], v[48:51]\n\t"
        "v_mfma_f32_16x16x32_bf16 v[52:55], a[164:167], v[104:107], v[52:55]\n\t"
        "v_mfma_f32_16x16x32_bf16 v[56:59], a[168:171], v[104:107], v[56:59]\n\t"
        "v_mfma_f32_16x16x32_bf16 v[60:63], a[152:155], v[108:111], v[60:63]\n\t"
        "v_mfma_f32_16x16x32_bf16 v[64:67], a[156:159], v[108:111], v[64:67]\n\t"
        "global_load_dwordx4 v[104:107], v247, s[70:71] sc1\n\t"
        "v_mfma_f32_16x16x32_bf16 v[68:71], a[160:163], v[108:111], v[68:71]\n\t"
        "v_mfma_f32_16x16x32_bf16 v[72:75], a[164:167], v[108:111], v[72:75]\n\t"
        "v_mfma_f32_16x16x32_bf16 v[76:79], a[168:171], v[108:111], v[76:79]\n\t"
        "global_load_dwordx4 v[108:111], v248, s[70:71] sc1\n\t"
        "s_add_u32 s70, s70, 0x1000\n\t"
        "s_addc_u32 s71, s71, 0\n\t"
        "s_waitcnt vmcnt(8)\n\t"
        "v_mfma_f32_16x16x32_bf16 v[0:3], a[172:175], v[112:115], v[0:3]\n\t"
        "v_mfma_f32_16x16x32_bf16 v[4:7], a[176:179], v[112:115], v[4:7]\n\t"
        "v_mfma_f32_16x16x32_bf16 v[8:11], a[180:183], v[112:115], v[8:11]\n\t"
        "v_mfma_f32_16x16x32_bf16 v[12:15], a[184:187], v[112:115], v[12:15]\n\t"
        "v_mfma_f32_16x16x32_bf16 v[16:19], a[188:191], v[112:115], v[16:19]\n\t"
        "v_mfma_f32_16x16x32_bf16 v[20:23], a[172:175], v[116:119], v[20:23]\n\t"
        "v_mfma_f32_16x16x32_bf16 v[24:27], a[176:179], v[116:119], v[24:27]\n\t"
        "global_load_dwordx4 v[112:115], v255, s[70:71] sc1\n\t"
        "v_mfma_f32_16x16x32_bf16 v[28:31], a[180:183], v[116:119], v[28:31]\n\t"
        "v_mfma_f32_16x16x32_bf16 v[32:35], a[184:187], v[116:119], v[32:35]\n\t"
        "v_mfma_f32_16x16x32_bf16 v[36:39], a[188:191], v[116:119], v[36:39]\n\t"
        "v_mfma_f32_16x16x32_bf16 v[40:43], a[172:175], v[120:123], v[40:43]\n\t"
        "v_mfma_f32_16x16x32_bf16 v[44:47], a[176:179], v[120:123], v[44:47]\n\t"
        "global_load_dwordx4 v[116:119], v246, s[70:71] sc1\n\t"
        "v_mfma_f32_16x16x32_bf16 v[48:51], a[180:183], v[120:123], v[48:51]\n\t"
        "v_mfma_f32_16x16x32_bf16 v[52:55], a[184:187], v[120:123], v[52:55]\n\t"
        "v_mfma_f32_16x16x32_bf16 v[56:59], a[188:191], v[120:123], v[56:59]\n\t"
        "v_mfma_f32_16x16x32_bf16 v[60:63], a[172:175], v[124:127], v[60:63]\n\t"
        "v_mfma_f32_16x16x32_bf16 v[64:67], a[176:179], v[124:127], v[64:67]\n\t"
        "global_load_dwordx4 v[120:123], v247, s[70:71] sc1\n\t"
        "v_mfma_f32_16x16x32_bf16 v[68:71], a[180:183], v[124:127], v[68:71]\n\t"
        "v_mfma_f32_16x16x32_bf16 v[72:75], a[184:187], v[124:127], v[72:75]\n\t"
        "v_mfma_f32_16x16x32_bf16 v[76:79], a[188:191], v[124:127], v[76:79]\n\t"
        "global_load_dwordx4 v[124:127], v248, s[70:71] sc1\n\t"
        "s_add_u32 s70, s70, 0x1000\n\t"
        "s_addc_u32 s71, s71, 0\n\t"
        "s_waitcnt vmcnt(8)\n\t"
        "v_mfma_f32_16x16x32_bf16 v[0:3], a[192:195], v[80:83], v[0:3]\n\t"
        "v_mfma_f32_16x16x32_bf16 v[4:7], a[196:199], v[80:83], v[4:7]\n\t"
        "v_mfma_f32_16x16x32_bf16 v[8:11], a[200:203], v[80:83], v[8:11]\n\t"
        "v_mfma_f32_16x16x32_bf16 v[12:15], a[204:207], v[80:83], v[12:15]\n\t"
        "v_mfma_f32_16x16x32_bf16 v[16:19], a[208:211], v[80:83], v[16:19]\n\t"
        "v_mfma_f32_16x16x32_bf16 v[20:23], a[192:195], v[84:87], v[20:23]\n\t"
        "v_mfma_f32_16x16x32_bf16 v[24:27], a[196:199], v[84:87], v[24:27]\n\t"
        "global_load_dwordx4 v[80:83], v255, s[70:71] sc1\n\t"
        "v_mfma_f32_16x16x32_bf16 v[28:31], a[200:203], v[84:87], v[28:31]\n\t"
        "v_mfma_f32_16x16x32_bf16 v[32:35], a[204:207], v[84:87], v[32:35]\n\t"
        "v_mfma_f32_16x16x32_bf16 v[36:39], a[208:211], v[84:87], v[36:39]\n\t"
        "v_mfma_f32_16x16x32_bf16 v[40:43], a[192:195], v[88:91], v[40:43]\n\t"
        "v_mfma_f32_16x16x32_bf16 v[44:47], a[196:199], v[88:91], v[44:47]\n\t"
        "global_load_dwordx4 v[84:87], v246, s[70:71] sc1\n\t"
        "v_mfma_f32_16x16x32_bf16 v[48:51], a[200:203], v[88:91], v[48:51]\n\t"
        "v_mfma_f32_16x16x32_bf16 v[52:55], a[204:207], v[88:91], v[52:55]\n\t"
        "v_mfma_f32_16x16x32_bf16 v[56:59], a[208:211], v[88:91], v[56:59]\n\t"
        "v_mfma_f32_16x16x32_bf16 v[60:63], a[192:195], v[92:95], v[60:63]\n\t"
        "v_mfma_f32_16x16x32_bf16 v[64:67], a[196:199], v[92:95], v[64:67]\n\t"
        "global_load_dwordx4 v[88:91], v247, s[70:71] sc1\n\t"
        "v_mfma_f32_16x16x32_bf16 v[68:71], a[200:203], v[92:95], v[68:71]\n\t"
        "v_mfma_f32_16x16x32_bf16 v[72:75], a[204:207], v[92:95], v[72:75]\n\t"
        "v_mfma_f32_16x16x32_bf16 v[76:79], a[208:211], v[92:95], v[76:79]\n\t"
        "global_load_dwordx4 v[92:95], v248, s[70:71] sc1\n\t"
        "s_add_u32 s70, s70, 0x1000\n\t"
        "s_addc_u32 s71, s71, 0\n\t"
        "s_waitcnt vmcnt(8)\n\t"
        "v_mfma_f32_16x16x32_bf16 v[0:3], a[212:215], v[96:99], v[0:3]\n\t"
        "v_mfma_f32_16x16x32_bf16 v[4:7], a[216:219], v[96:99], v[4:7]\n\t"
        "v_mfma_f32_16x16x32_bf16 v[8:11], a[220:223], v[96:99], v[8:11]\n\t"
        "v_mfma_f32_16x16x32_bf16 v[12:15], a[224:227], v[96:99], v[12:15]\n\t"
        "v_mfma_f32_16x16x32_bf16 v[16:19], a[228:231], v[96:99], v[16:19]\n\t"
        "v_mfma_f32_16x16x32_bf16 v[20:23], a[212:215], v[100:103], v[20:23]\n\t"
        "v_mfma_f32_16x16x32_bf16 v[24:27], a[216:219], v[100:103], v[24:27]\n\t"
        "global_load_dwordx4 v[96:99], v255, s[70:71] sc1\n\t"
        "v_mfma_f32_16x16x32_bf16 v[28:31], a[220:223], v[100:103], v[28:31]\n\t"
        "v_mfma_f32_16x16x32_bf16 v[32:35], a[224:227], v[100:103], v[32:35]\n\t"
        "v_mfma_f32_16x16x32_bf16 v[36:39], a[228:231], v[100:103], v[36:39]\n\t"
        "v_mfma_f32_16x16x32_bf16 v[40:43], a[212:215], v[104:107], v[40:43]\n\t"
        "v_mfma_f32_16x16x32_bf16 v[44:47], a[216:219], v[104:107], v[44:47]\n\t"
        "global_load_dwordx4 v[100:103], v246, s[70:71] sc1\n\t"
        "v_mfma_f32_16x16x32_bf16 v[48:51], a[220:223], v[104:107], v[48:51]\n\t"
        "v_mfma_f32_16x16x32_bf16 v[52:55], a[224:227], v[104:107], v[52:55]\n\t"
        "v_mfma_f32_16x16x32_bf16 v[56:59], a[228:231], v[104:107], v[56:59]\n\t"
        "v_mfma_f32_16x16x32_bf16 v[60:63], a[212:215], v[108:111], v[60:63]\n\t"
        "v_mfma_f32_16x16x32_bf16 v[64:67], a[216:219], v[108:111], v[64:67]\n\t"
        "global_load_dwordx4 v[104:107], v247, s[70:71] sc1\n\t"
        "v_mfma_f32_16x16x32_bf16 v[68:71], a[220:223], v[108:111], v[68:71]\n\t"
        "v_mfma_f32_16x16x32_bf16 v[72:75], a[224:227], v[108:111], v[72:75]\n\t"
        "v_mfma_f32_16x16x32_bf16 v[76:79], a[228:231], v[108:111], v[76:79]\n\t"
        "global_load_dwordx4 v[108:111], v248, s[70:71] sc1\n\t"
        "s_add_u32 s70, s70, 0x1000\n\t"
        "s_addc_u32 s71, s71, 0\n\t"
        "s_waitcnt vmcnt(8)\n\t"
        "v_mfma_f32_16x16x32_bf16 v[0:3], a[232:235], v[112:115], v[0:3]\n\t"
        "v_mfma_f32_16x16x32_bf16 v[4:7], a[236:239], v[112:115], v[4:7]\n\t"
        "v_mfma_f32_16x16x32_bf16 v[8:11], a[240:243], v[112:115], v[8:11]\n\t"
        "v_mfma_f32_16x16x32_bf16 v[12:15], a[244:247], v[112:115], v[12:15]\n\t"
        "v_mfma_f32_16x16x32_bf16 v[16:19], a[248:251], v[112:115], v[16:19]\n\t"
        "v_mfma_f32_16x16x32_bf16 v[20:23], a[232:235], v[116:119], v[20:23]\n\t"
        "v_mfma_f32_16x16x32_bf16 v[24:27], a[236:239], v[116:119], v[24:27]\n\t"
        "global_load_dwordx4 v[112:115], v255, s[70:71] sc1\n\t"
        "v_mfma_f32_16x16x32_bf16 v[28:31], a[240:243], v[116:119], v[28:31]\n\t"
        "v_mfma_f32_16x16x32_bf16 v[32:35], a[244:247], v[116:119], v[32:35]\n\t"
        "v_mfma_f32_16x16x32_bf16 v[36:39], a[248:251], v[116:119], v[36:39]\n\t"
        "v_mfma_f32_16x16x32_bf16 v[40:43], a[232:235], v[120:123], v[40:43]\n\t"
        "v_mfma_f32_16x16x32_bf16 v[44:47], a[236:239], v[120:123], v[44:47]\n\t"
        "global_load_dwordx4 v[116:119], v246, s[70:71] sc1\n\t"
        "v_mfma_f32_16x16x32_bf16 v[48:51], a[240:243], v[120:123], v[48:51]\n\t"
        "v_mfma_f32_16x16x32_bf16 v[52:55], a[244:247], v[120:123], v[52:55]\n\t"
        "v_mfma_f32_16x16x32_bf16 v[56:59], a[248:251], v[120:123], v[56:59]\n\t"
        "v_mfma_f32_16x16x32_bf16 v[60:63], a[232:235], v[124:127], v[60:63]\n\t"
        "v_mfma_f32_16x16x32_bf16 v[64:67], a[236:239], v[124:127], v[64:67]\n\t"
        "global_load_dwordx4 v[120:123], v247, s[70:71] sc1\n\t"
        "v_mfma_f32_16x16x32_bf16 v[68:71], a[240:243], v[124:127], v[68:71]\n\t"
        "v_mfma_f32_16x16x32_bf16 v[72:75], a[244:247], v[124:127], v[72:75]\n\t"
        "v_mfma_f32_16x16x32_bf16 v[76:79], a[248:251], v[124:127], v[76:79]\n\t"
        "global_load_dwordx4 v[124:127], v248, s[70:71] sc1\n\t"
        "s_add_u32 s70, s70, 0x1000\n\t"
        "s_addc_u32 s71, s71, 0\n\t"
        "s_waitcnt vmcnt(8)\n\t"
        "v_mfma_f32_16x16x32_bf16 v[0:3], a[252:255], v[80:83], v[0:3]\n\t"
        "v_mfma_f32_16x16x32_bf16 v[4:7], a[0:3], v[80:83], v[4:7]\n\t"
        "v_mfma_f32_16x16x32_bf16 v[8:11], a[4:7], v[80:83], v[8:11]\n\t"
        "v_mfma_f32_16x16x32_bf16 v[12:15], a[8:11], v[80:83], v[12:15]\n\t"
        "v_mfma_f32_16x16x32_bf16 v[16:19], a[12:15], v[80:83], v[16:19]\n\t"
        "v_mfma_f32_16x16x32_bf16 v[20:23], a[252:255], v[84:87], v[20:23]\n\t"
        "v_mfma_f32_16x16x32_bf16 v[24:27], a[0:3], v[84:87], v[24:27]\n\t"
        "global_load_dwordx4 v[80:83], v255, s[70:71] sc1\n\t"
        "v_mfma_f32_16x16x32_bf16 v[28:31], a[4:7], v[84:87], v[28:31]\n\t"
        "v_mfma_f32_16x16x32_bf16 v[32:35], a[8:11], v[84:87], v[32:35]\n\t"
        "v_mfma_f32_16x16x32_bf16 v[36:39], a[12:15], v[84:87], v[36:39]\n\t"
        "v_mfma_f32_16x16x32_bf16 v[40:43], a[252:255], v[88:91], v[40:43]\n\t"
        "v_mfma_f32_16x16x32_bf16 v[44:47], a[0:3], v[88:91], v[44:47]\n\t"
        "global_load_dwordx4 v[84:87], v246, s[70:71] sc1\n\t"
        "v_mfma_f32_16x16x32_bf16 v[48:51], a[4:7], v[88:91], v[48:51]\n\t"
        "v_mfma_f32_16x16x32_bf16 v[52:55], a[8:11], v[88:91], v[52:55]\n\t"
        "v_mfma_f32_16x16x32_bf16 v[56:59], a[12:15], v[88:91], v[56:59]\n\t"
        "v_mfma_f32_16x16x32_bf16 v[60:63], a[252:255], v[92:95], v[60:63]\n\t"
        "v_mfma_f32_16x16x32_bf16 v[64:67], a[0:3], v[92:95], v[64:67]\n\t"
        "global_load_dwordx4 v[88:91], v247, s[70:71] sc1\n\t"
        "v_mfma_f32_16x16x32_bf16 v[68:71], a[4:7], v[92:95], v[68:71]\n\t"
        "v_mfma_f32_16x16x32_bf16 v[72:75], a[8:11], v[92:95], v[72:75]\n\t"
        "v_mfma_f32_16x16x32_bf16 v[76:79], a[12:15], v[92:95], v[76:79]\n\t"
        "global_load_dwordx4 v[92:95], v248, s[70:71] sc1\n\t"
        "s_add_u32 s70, s70, 0x1000\n\t"
        "s_addc_u32 s71, s71, 0\n\t"
        "s_waitcnt vmcnt(8)\n\t"
        "v_mfma_f32_16x16x32_bf16 v[0:3], a[16:19], v[96:99], v[0:3]\n\t"
        "v_mfma_f32_16x16x32_bf16 v[4:7], a[20:23], v[96:99], v[4:7]\n\t"
        "v_mfma_f32_16x16x32_bf16 v[8:11], a[24:27], v[96:99], v[8:11]\n\t"
        "v_mfma_f32_16x16x32_bf16 v[12:15], a[28:31], v[96:99], v[12:15]\n\t"
        "v_mfma_f32_16x16x32_bf16 v[16:19], a[32:35], v[96:99], v[16:19]\n\t"
        "v_mfma_f32_16x16x32_bf16 v[20:23], a[16:19], v[100:103], v[20:23]\n\t"
        "v_mfma_f32_16x16x32_bf16 v[24:27], a[20:23], v[100:103], v[24:27]\n\t"
        "global_load_dwordx4 v[96:99], v255, s[70:71] sc1\n\t"
        "v_mfma_f32_16x16x32_bf16 v[28:31], a[24:27], v[100:103], v[28:31]\n\t"
        "v_mfma_f32_16x16x32_bf16 v[32:35], a[28:31], v[100:103], v[32:35]\n\t"
        "v_mfma_f32_16x16x32_bf16 v[36:39], a[32:35], v[100:103], v[36:39]\n\t"
        "v_mfma_f32_16x16x32_bf16 v[40:43], a[16:19], v[104:107], v[40:43]\n\t"
        "v_mfma_f32_16x16x32_bf16 v[44:47], a[20:23], v[104:107], v[44:47]\n\t"
        "global_load_dwordx4 v[100:103], v246, s[70:71] sc1\n\t"
        "v_mfma_f32_16x16x32_bf16 v[48:51], a[24:27], v[104:107], v[48:51]\n\t"
        "v_mfma_f32_16x16x32_bf16 v[52:55], a[28:31], v[104:107], v[52:55]\n\t"
        "v_mfma_f32_16x16x32_bf16 v[56:59], a[32:35], v[104:107], v[56:59]\n\t"
        "v_mfma_f32_16x16x32_bf16 v[60:63], a[16:19], v[108:111], v[60:63]\n\t"
        "v_mfma_f32_16x16x32_bf16 v[64:67], a[20:23], v[108:111], v[64:67]\n\t"
        "global_load_dwordx4 v[104:107], v247, s[70:71] sc1\n\t"
        "v_mfma_f32_16x16x32_bf16 v[68:71], a[24:27], v[108:111], v[68:71]\n\t"
        "v_mfma_f32_16x16x32_bf16 v[72:75], a[28:31], v[108:111], v[72:75]\n\t"
        "v_mfma_f32_16x16x32_bf16 v[76:79], a[32:35], v[108:111], v[76:79]\n\t"
        "global_load_dwordx4 v[108:111], v248, s[70:71] sc1\n\t"
        "s_add_u32 s70, s70, 0x1000\n\t"
        "s_addc_u32 s71, s71, 0\n\t"
        "s_waitcnt vmcnt(8)\n\t"
        "v_mfma_f32_16x16x32_bf16 v[0:3], a[36:39], v[112:115], v[0:3]\n\t"
        "v_mfma_f32_16x16x32_bf16 v[4:7], a[40:43], v[112:115], v[4:7]\n\t"
        "v_mfma_f32_16x16x32_bf16 v[8:11], a[44:47], v[112:115], v[8:11]\n\t"
        "v_mfma_f32_16x16x32_bf16 v[12:15], a[48:51], v[112:115], v[12:15]\n\t"
        "v_mfma_f32_16x16x32_bf16 v[16:19], a[52:55], v[112:115], v[16:19]\n\t"
        "v_mfma_f32_16x16x32_bf16 v[20:23], a[36:39], v[116:119], v[20:23]\n\t"
        "v_mfma_f32_16x16x32_bf16 v[24:27], a[40:43], v[116:119], v[24:27]\n\t"
        "global_load_dwordx4 v[112:115], v255, s[70:71] sc1\n\t"
        "v_mfma_f32_16x16x32_bf16 v[28:31], a[44:47], v[116:119], v[28:31]\n\t"
        "v_mfma_f32_16x16x32_bf16 v[32:35], a[48:51], v[116:119], v[32:35]\n\t"
        "v_mfma_f32_16x16x32_bf16 v[36:39], a[52:55], v[116:119], v[36:39]\n\t"
        "v_mfma_f32_16x16x32_bf16 v[40:43], a[36:39], v[120:123], v[40:43]\n\t"
        "v_mfma_f32_16x16x32_bf16 v[44:47], a[40:43], v[120:123], v[44:47]\n\t"
        "global_load_dwordx4 v[116:119], v246, s[70:71] sc1\n\t"
        "v_mfma_f32_16x16x32_bf16 v[48:51], a[44:47], v[120:123], v[48:51]\n\t"
        "v_mfma_f32_16x16x32_bf16 v[52:55], a[48:51], v[120:123], v[52:55]\n\t"
        "v_mfma_f32_16x16x32_bf16 v[56:59], a[52:55], v[120:123], v[56:59]\n\t"
        "v_mfma_f32_16x16x32_bf16 v[60:63], a[36:39], v[124:127], v[60:63]\n\t"
        "v_mfma_f32_16x16x32_bf16 v[64:67], a[40:43], v[124:127], v[64:67]\n\t"
        "global_load_dwordx4 v[120:123], v247, s[70:71] sc1\n\t"
        "v_mfma_f32_16x16x32_bf16 v[68:71], a[44:47], v[124:127], v[68:71]\n\t"
        "v_mfma_f32_16x16x32_bf16 v[72:75], a[48:51], v[124:127], v[72:75]\n\t"
        "v_mfma_f32_16x16x32_bf16 v[76:79], a[52:55], v[124:127], v[76:79]\n\t"
        "global_load_dwordx4 v[124:127], v248, s[70:71] sc1\n\t"
        "s_add_u32 s70, s70, 0x1000\n\t"
        "s_addc_u32 s71, s71, 0\n\t"
        "s_waitcnt vmcnt(8)\n\t"
        "v_mfma_f32_16x16x32_bf16 v[0:3], a[56:59], v[80:83], v[0:3]\n\t"
        "v_mfma_f32_16x16x32_bf16 v[4:7], a[60:63], v[80:83], v[4:7]\n\t"
        "v_mfma_f32_16x16x32_bf16 v[8:11], a[64:67], v[80:83], v[8:11]\n\t"
        "v_mfma_f32_16x16x32_bf16 v[12:15], a[68:71], v[80:83], v[12:15]\n\t"
        "v_mfma_f32_16x16x32_bf16 v[16:19], a[72:75], v[80:83], v[16:19]\n\t"
        "v_mfma_f32_16x16x32_bf16 v[20:23], a[56:59], v[84:87], v[20:23]\n\t"
        "v_mfma_f32_16x16x32_bf16 v[24:27], a[60:63], v[84:87], v[24:27]\n\t"
        "global_load_dwordx4 v[80:83], v255, s[70:71] sc1\n\t"
        "v_mfma_f32_16x16x32_bf16 v[28:31], a[64:67], v[84:87], v[28:31]\n\t"
        "v_mfma_f32_16x16x32_bf16 v[32:35], a[68:71], v[84:87], v[32:35]\n\t"
        "v_mfma_f32_16x16x32_bf16 v[36:39], a[72:75], v[84:87], v[36:39]\n\t"
        "v_mfma_f32_16x16x32_bf16 v[40:43], a[56:59], v[88:91], v[40:43]\n\t"
        "v_mfma_f32_16x16x32_bf16 v[44:47], a[60:63], v[88:91], v[44:47]\n\t"
        "global_load_dwordx4 v[84:87], v246, s[70:71] sc1\n\t"
        "v_mfma_f32_16x16x32_bf16 v[48:51], a[64:67], v[88:91], v[48:51]\n\t"
        "v_mfma_f32_16x16x32_bf16 v[52:55], a[68:71], v[88:91], v[52:55]\n\t"
        "v_mfma_f32_16x16x32_bf16 v[56:59], a[72:75], v[88:91], v[56:59]\n\t"
        "v_mfma_f32_16x16x32_bf16 v[60:63], a[56:59], v[92:95], v[60:63]\n\t"
        "v_mfma_f32_16x16x32_bf16 v[64:67], a[60:63], v[92:95], v[64:67]\n\t"
        "global_load_dwordx4 v[88:91], v247, s[70:71] sc1\n\t"
        "v_mfma_f32_16x16x32_bf16 v[68:71], a[64:67], v[92:95], v[68:71]\n\t"
        "v_mfma_f32_16x16x32_bf16 v[72:75], a[68:71], v[92:95], v[72:75]\n\t"
        "v_mfma_f32_16x16x32_bf16 v[76:79], a[72:75], v[92:95], v[76:79]\n\t"
        "global_load_dwordx4 v[92:95], v248, s[70:71] sc1\n\t"
        "s_add_u32 s70, s70, 0x1000\n\t"
        "s_addc_u32 s71, s71, 0\n\t"
        "s_waitcnt vmcnt(8)\n\t"
        "v_mfma_f32_16x16x32_bf16 v[0:3], a[76:79], v[96:99], v[0:3]\n\t"
        "v_mfma_f32_16x16x32_bf16 v[4:7], a[80:83], v[96:99], v[4:7]\n\t"
        "v_mfma_f32_16x16x32_bf16 v[8:11], a[84:87], v[96:99], v[8:11]\n\t"
        "v_mfma_f32_16x16x32_bf16 v[12:15], a[88:91], v[96:99], v[12:15]\n\t"
        "v_mfma_f32_16x16x32_bf16 v[16:19], a[92:95], v[96:99], v[16:19]\n\t"
        "v_mfma_f32_16x16x32_bf16 v[20:23], a[76:79], v[100:103], v[20:23]\n\t"
        "v_mfma_f32_16x16x32_bf16 v[24:27], a[80:83], v[100:103], v[24:27]\n\t"
        "v_mfma_f32_16x16x32_bf16 v[28:31], a[84:87], v[100:103], v[28:31]\n\t"
        "v_mfma_f32_16x16x32_bf16 v[32:35], a[88:91], v[100:103], v[32:35]\n\t"
        "v_mfma_f32_16x16x32_bf16 v[36:39], a[92:95], v[100:103], v[36:39]\n\t"
        "v_mfma_f32_16x16x32_bf16 v[40:43], a[76:79], v[104:107], v[40:43]\n\t"
        "v_mfma_f32_16x16x32_bf16 v[44:47], a[80:83], v[104:107], v[44:47]\n\t"
        "v_mfma_f32_16x16x32_bf16 v[48:51], a[84:87], v[104:107], v[48:51]\n\t"
        "v_mfma_f32_16x16x32_bf16 v[52:55], a[88:91], v[104:107], v[52:55]\n\t"
        "v_mfma_f32_16x16x32_bf16 v[56:59], a[92:95], v[104:107], v[56:59]\n\t"
        "v_mfma_f32_16x16x32_bf16 v[60:63], a[76:79], v[108:111], v[60:63]\n\t"
        "v_mfma_f32_16x16x32_bf16 v[64:67], a[80:83], v[108:111], v[64:67]\n\t"
        "v_mfma_f32_16x16x32_bf16 v[68:71], a[84:87], v[108:111], v[68:71]\n\t"
        "v_mfma_f32_16x16x32_bf16 v[72:75], a[88:91], v[108:111], v[72:75]\n\t"
        "v_mfma_f32_16x16x32_bf16 v[76:79], a[92:95], v[108:111], v[76:79]\n\t"
        "s_waitcnt vmcnt(4)\n\t"
        "v_mfma_f32_16x16x32_bf16 v[0:3], a[96:99], v[112:115], v[0:3]\n\t"
        "v_mfma_f32_16x16x32_bf16 v[4:7], a[100:103], v[112:115], v[4:7]\n\t"
        "v_mfma_f32_16x16x32_bf16 v[8:11], a[104:107], v[112:115], v[8:11]\n\t"
        "v_mfma_f32_16x16x32_bf16 v[12:15], a[108:111], v[112:115], v[12:15]\n\t"
        "v_mfma_f32_16x16x32_bf16 v[16:19], v[216:219], v[112:115], v[16:19]\n\t"
        "v_mfma_f32_16x16x32_bf16 v[20:23], a[96:99], v[116:119], v[20:23]\n\t"
        "v_mfma_f32_16x16x32_bf16 v[24:27], a[100:103], v[116:119], v[24:27]\n\t"
        "v_mfma_f32_16x16x32_bf16 v[28:31], a[104:107], v[116:119], v[28:31]\n\t"
        "v_mfma_f32_16x16x32_bf16 v[32:35], a[108:111], v[116:119], v[32:35]\n\t"
        "v_mfma_f32_16x16x32_bf16 v[36:39], v[216:219], v[116:119], v[36:39]\n\t"
        "v_mfma_f32_16x16x32_bf16 v[40:43], a[96:99], v[120:123], v[40:43]\n\t"
        "v_mfma_f32_16x16x32_bf16 v[44:47], a[100:103], v[120:123], v[44:47]\n\t"
        "v_mfma_f32_16x16x32_bf16 v[48:51], a[104:107], v[120:123], v[48:51]\n\t"
        "v_mfma_f32_16x16x32_bf16 v[52:55], a[108:111], v[120:123], v[52:55]\n\t"
        "v_mfma_f32_16x16x32_bf16 v[56:59], v[216:219], v[120:123], v[56:59]\n\t"
        "v_mfma_f32_16x16x32_bf16 v[60:63], a[96:99], v[124:127], v[60:63]\n\t"
        "v_mfma_f32_16x16x32_bf16 v[64:67], a[100:103], v[124:127], v[64:67]\n\t"
        "v_mfma_f32_16x16x32_bf16 v[68:71], a[104:107], v[124:127], v[68:71]\n\t"
        "v_mfma_f32_16x16x32_bf16 v[72:75], a[108:111], v[124:127], v[72:75]\n\t"
        "v_mfma_f32_16x16x32_bf16 v[76:79], v[216:219], v[124:127], v[76:79]\n\t"
        "s_waitcnt vmcnt(0)\n\t"
        "v_mfma_f32_16x16x32_bf16 v[0:3], v[220:223], v[80:83], v[0:3]\n\t"
        "v_mfma_f32_16x16x32_bf16 v[4:7], v[224:227], v[80:83], v[4:7]\n\t"
        "v_mfma_f32_16x16x32_bf16 v[8:11], v[228:231], v[80:83], v[8:11]\n\t"
        "v_mfma_f32_16x16x32_bf16 v[12:15], v[232:235], v[80:83], v[12:15]\n\t"
        "v_mfma_f32_16x16x32_bf16 v[16:19], v[236:239], v[80:83], v[16:19]\n\t"
        "v_mfma_f32_16x16x32_bf16 v[20:23], v[220:223], v[84:87], v[20:23]\n\t"
        "v_mfma_f32_16x16x32_bf16 v[24:27], v[224:227], v[84:87], v[24:27]\n\t"
        "v_mfma_f32_16x16x32_bf16 v[28:31], v[228:231], v[84:87], v[28:31]\n\t"
        "v_mfma_f32_16x16x32_bf16 v[32:35], v[232:235], v[84:87], v[32:35]\n\t"
        "v_mfma_f32_16x16x32_bf16 v[36:39], v[236:239], v[84:87], v[36:39]\n\t"
        "v_mfma_f32_16x16x32_bf16 v[40:43], v[220:223], v[88:91], v[40:43]\n\t"
        "v_mfma_f32_16x16x32_bf16 v[44:47], v[224:227], v[88:91], v[44:47]\n\t"
        "v_mfma_f32_16x16x32_bf16 v[48:51], v[228:231], v[88:91], v[48:51]\n\t"
        "v_mfma_f32_16x16x32_bf16 v[52:55], v[232:235], v[88:91], v[52:55]\n\t"
        "v_mfma_f32_16x16x32_bf16 v[56:59], v[236:239], v[88:91], v[56:59]\n\t"
        "v_mfma_f32_16x16x32_bf16 v[60:63], v[220:223], v[92:95], v[60:63]\n\t"
        "v_mfma_f32_16x16x32_bf16 v[64:67], v[224:227], v[92:95], v[64:67]\n\t"
        "v_mfma_f32_16x16x32_bf16 v[68:71], v[228:231], v[92:95], v[68:71]\n\t"
        "v_mfma_f32_16x16x32_bf16 v[72:75], v[232:235], v[92:95], v[72:75]\n\t"
        "v_mfma_f32_16x16x32_bf16 v[76:79], v[236:239], v[92:95], v[76:79]\n\t"
        "s_cmp_ge_u32 s62, 2\n\t"
        "s_cbranch_scc1 LH2_%=\n\t"
        "v_mfma_f32_16x16x32_bf16 v[0:3], a[112:115], v[196:199], v[0:3]\n\t"
        "v_mfma_f32_16x16x32_bf16 v[4:7], a[116:119], v[196:199], v[4:7]\n\t"
        "v_mfma_f32_16x16x32_bf16 v[8:11], a[120:123], v[196:199], v[8:11]\n\t"
        "v_mfma_f32_16x16x32_bf16 v[12:15], a[124:127], v[196:199], v[12:15]\n\t"
        "v_mfma_f32_16x16x32_bf16 v[16:19], a[128:131], v[196:199], v[16:19]\n\t"
        "v_mfma_f32_16x16x32_bf16 v[20:23], a[112:115], v[200:203], v[20:23]\n\t"
        "v_mfma_f32_16x16x32_bf16 v[24:27], a[116:119], v[200:203], v[24:27]\n\t"
        "v_mfma_f32_16x16x32_bf16 v[28:31], a[120:123], v[200:203], v[28:31]\n\t"
        "v_mfma_f32_16x16x32_bf16 v[32:35], a[124:127], v[200:203], v[32:35]\n\t"
        "v_mfma_f32_16x16x32_bf16 v[36:39], a[128:131], v[200:203], v[36:39]\n\t"
        "v_mfma_f32_16x16x32_bf16 v[40:43], a[112:115], v[204:207], v[40:43]\n\t"
        "v_mfma_f32_16x16x32_bf16 v[44:47], a[116:119], v[204:207], v[44:47]\n\t"
        "v_mfma_f32_16x16x32_bf16 v[48:51], a[120:123], v[204:207], v[48:51]\n\t"
        "v_mfma_f32_16x16x32_bf16 v[52:55], a[124:127], v[204:207], v[52:55]\n\t"
        "v_mfma_f32_16x16x32_bf16 v[56:59], a[128:131], v[204:207], v[56:59]\n\t"
        "v_mfma_f32_16x16x32_bf16 v[60:63], a[112:115], v[208:211], v[60:63]\n\t"
        "v_mfma_f32_16x16x32_bf16 v[64:67], a[116:119], v[208:211], v[64:67]\n\t"
        "v_mfma_f32_16x16x32_bf16 v[68:71], a[120:123], v[208:211], v[68:71]\n\t"
        "v_mfma_f32_16x16x32_bf16 v[72:75], a[124:127], v[208:211], v[72:75]\n\t"
        "v_mfma_f32_16x16x32_bf16 v[76:79], a[128:131], v[208:211], v[76:79]\n\t"
        "LH2_%=:\n\t"
        "s_waitcnt vmcnt(0)\n\t"
        "s_nop 7\n\t"
        : "={v[0:15]}"(av[0]), "={v[16:31]}"(av[1]), "={v[32:47]}"(av[2]), "={v[48:63]}"(av[3]), "={v[64:79]}"(av[4]), "={v[176:191]}"(gv), "={v[192:195]}"(gw), "+{v[240:255]}"(vv)
        : "{s[48:63]}"(sv), "{s[64:65]}"(ws)
        : "a0", "a1", "a2", "a3", "a4", "a5", "a6", "a7", "a8", "a9", "a10", "a11", "a12", "a13", "a14", "a15", "a16", "a17", "a18", "a19", "a20", "a21", "a22", "a23", "a24", "a25", "a26", "a27", "a28", "a29", "a30", "a31", "a32", "a33", "a34", "a35", "a36", "a37", "a38", "a39", "a40", "a41", "a42", "a43", "a44", "a45", "a46", "a47", "a48", "a49", "a50", "a51", "a52", "a53", "a54", "a55", "a56", "a57", "a58", "a59", "a60", "a61", "a62", "a63", "a64", "a65", "a66", "a67", "a68", "a69", "a70", "a71", "a72", "a73", "a74", "a75", "a76", "a77", "a78", "a79", "a80", "a81", "a82", "a83", "a84", "a85", "a86", "a87", "a88", "a89", "a90", "a91", "a92", "a93", "a94", "a95", "a96", "a97", "a98", "a99", "a100", "a101", "a102", "a103", "a104", "a105", "a106", "a107", "a108", "a109", "a110", "a111", "a112", "a113", "a114", "a115", "a116", "a117", "a118", "a119", "a120", "a121", "a122", "a123", "a124", "a125", "a126", "a127", "a128", "a129", "a130", "a131", "a132", "a133", "a134", "a135", "a136", "a137", "a138", "a139", "a140", "a141", "a142", "a143", "a144", "a145", "a146", "a147", "a148", "a149", "a150", "a151", "a152", "a153", "a154", "a155", "a156", "a157", "a158", "a159", "a160", "a161", "a162", "a163", "a164", "a165", "a166", "a167", "a168", "a169", "a170", "a171", "a172", "a173", "a174", "a175", "a176", "a177", "a178", "a179", "a180", "a181", "a182", "a183", "a184", "a185", "a186", "a187", "a188", "a189", "a190", "a191", "a192", "a193", "a194", "a195", "a196", "a197", "a198", "a199", "a200", "a201", "a202", "a203", "a204", "a205", "a206", "a207", "a208", "a209", "a210", "a211", "a212", "a213", "a214", "a215", "a216", "a217", "a218", "a219", "a220", "a221", "a222", "a223", "a224", "a225", "a226", "a227", "a228", "a229", "a230", "a231", "a232", "a233", "a234", "a235", "a236", "a237", "a238", "a239", "a240", "a241", "a242", "a243", "a244", "a245", "a246", "a247", "a248", "a249", "a250", "a251", "a252", "a253", "a254", "a255", "v80", "v81", "v82", "v83", "v84", "v85", "v86", "v87", "v88", "v89", "v90", "v91", "v92", "v93", "v94", "v95", "v96", "v97", "v98", "v99", "v100", "v101", "v102", "v103", "v104", "v105", "v106", "v107", "v108", "v109", "v110", "v111", "v112", "v113", "v114", "v115", "v116", "v117", "v118", "v119", "v120", "v121", "v122", "v123", "v124", "v125", "v126", "v127", "v128", "v129", "v130", "v131", "v132", "v133", "v134", "v135", "v136", "v137", "v138", "v139", "v140", "v141", "v142", "v143", "v144", "v145", "v146", "v147", "v148", "v149", "v150", "v151", "v152", "v153", "v154", "v155", "v156", "v157", "v158", "v159", "v160", "v161", "v162", "v163", "v164", "v165", "v166", "v167", "v168", "v169", "v170", "v171", "v172", "v173", "v174", "v175", "v196", "v197", "v198", "v199", "v200", "v201", "v202", "v203", "v204", "v205", "v206", "v207", "v208", "v209", "v210", "v211", "v216", "v217", "v218", "v219", "v220", "v221", "v222", "v223", "v224", "v225", "v226", "v227", "v228", "v229", "v230", "v231", "v232", "v233", "v234", "v235", "v236", "v237", "v238", "v239", "s40", "s41", "s42", "s43", "s44", "s45", "s46", "s47", "s66", "s67", "s68", "s69", "s70", "s71", "s72", "s73", "s74", "s75", "s76", "s77", "vcc", "scc", "memory");
    {
    const int l = vv[3] >> 4, g = l >> 4, qq = (l & 15) >> 2, p = l & 3;
    STAMP2(6);
    f32x4 acc[4][5], gb[5];
#pragma unroll
    for (int i = 0; i < 20; ++i) acc[i / 5][i % 5] = (f32x4){av[i >> 2][4 * (i & 3)], av[i >> 2][4 * (i & 3) + 1], av[i >> 2][4 * (i & 3) + 2], av[i >> 2][4 * (i & 3) + 3]};
#pragma unroll
    for (int n = 0; n < 4; ++n) gb[n] = (f32x4){gv[4 * n], gv[4 * n + 1], gv[4 * n + 2], gv[4 * n + 3]};
    gb[4] = gw;
    LAS f32x4* red = (LAS f32x4*)smem + l;
    f32x4 own[5];
#pragma unroll
    for (int n = 0; n < 5; ++n) own[n] = acc[0][n];
#pragma unroll
    for (int m = 1; m < 4; ++m)
#pragma unroll
        for (int n = 0; n < 5; ++n) red[((((w + m) & 3) * 3 + m - 1) * 5 + n) * 64] = acc[m][n];
    WAITV_BAR(0);
#pragma unroll
    for (int rk = 0; rk < 3; ++rk)
#pragma unroll
        for (int n = 0; n < 5; ++n) own[n] += red[((w * 3 + rk) * 5 + n) * 64];
    const lptr xr = (lptr)smem + K3_X + (20 * g + qq) * 128 + w * 32 + p * 8;
    u32 o[10];
#pragma unroll
    for (int n = 0; n < 5; ++n) {
        const u32x2 xv = __builtin_bit_cast(u32x2, ldtr(xr + n * 512));
        o[2 * n] = cvtpk(lo16(xv[0]) + gb[n][0] * own[n][0], hi16(xv[0]) + gb[n][1] * own[n][1]);
        o[2 * n + 1] = cvtpk(lo16(xv[1]) + gb[n][2] * own[n][2], hi16(xv[1]) + gb[n][3] * own[n][3]);
    }
    u32* op = (u32*)(out + (size_t)(tt * 64 + w * 16 + (l & 15)) * D + cb * 80 + 20 * g);
#pragma unroll
    for (int n = 0; n < 5; ++n) *(u32x2*)(op + 2 * n) = (u32x2){o[2 * n], o[2 * n + 1]};
    STAMP2(1);
    }
}

extern "C" void kernel_launch(void* const* d_in, const int* in_sizes, int n_in, void* d_out, int out_size, void* d_ws, size_t ws_size, hipStream_t stream) {
    const u16* q = (const u16*)d_in[0];
    const u16* k = (const u16*)d_in[1];
    const u16* v = (const u16*)d_in[2];
    const u16* sh = (const u16*)d_in[3];
    const u16* sk = (const u16*)d_in[4];
    const u16* sD = (const u16*)d_in[5];
    const u16* wg = (const u16*)d_in[7];
    const u16* wu = (const u16*)d_in[8];
    const u16* wd = (const u16*)d_in[9];
    const u16* c = (const u16*)d_in[10];
    const u16* wm = (const u16*)d_in[11];
    char* ws = (char*)d_ws;
    (void)hipFuncSetAttribute((const void*)k1_conv, hipFuncAttributeMaxDynamicSharedMemorySize, K1_LDS);
    (void)hipFuncSetAttribute((const void*)k2_ffn, hipFuncAttributeMaxDynamicSharedMemorySize, K2_LDS);
    k0_prep<<<752, 256, K0_LDS, stream>>>(q, k, v, sh, c, wm, ws);
    k1_conv<<<256, 512, K1_LDS, stream>>>(sk, sD, wg, wu, wd, ws);
    k2_ffn<<<256, 256, K2_LDS, stream>>>(ws, (u16*)d_out);
}
```
